```python
import torch

def _softmax(x, axis=-1):
    return torch.softmax(x, dim=axis)
def _gelu(x):
    return torch.nn.functional.gelu(x, approximate="tanh")
def _silu(x):
    return torch.nn.functional.silu(x)
def _sigmoid(x):
    return torch.sigmoid(x)
def _layernorm(x, axis=-1, eps=1e-5):
    m = x.mean(dim=axis, keepdim=True)
    v = x.var(dim=axis, keepdim=True, unbiased=False)
    return (x - m) / torch.sqrt(v + eps)
def _scatter_add(tgt, idx, src, axis=0):
    return tgt.clone().index_add_(axis, idx, src)
def _segment_sum(src, seg, n):
    out = torch.zeros((n,) + src.shape[1:], dtype=src.dtype)
    return out.index_add_(0, seg, src)
def _pad(x, pw):
    flat = tuple(v for pair in reversed(pw) for v in pair)
    return torch.nn.functional.pad(x, flat)

kernel_name = 'interleave_layers__sliding_local_dilated__ssm_conv_s4d__prompt'

def setup_inputs():

    _w = (lambda a: (lambda t: t.to(torch.bfloat16) if t.is_floating_point() else t)(torch.as_tensor(a)))
    def _A_setup():
        B, S, D = 1, 2048, 512
        return {
            "x": _w((lambda *s: torch.randn(*s, dtype=torch.bfloat16) * 0.05)(B, S, D)),
            "wqkv": _w((lambda *s: torch.randn(*s, dtype=torch.bfloat16) * 0.05)(D, 3 * D)),
            "wo": _w((lambda *s: torch.randn(*s, dtype=torch.bfloat16) * 0.05)(D, D)),
        }
    _A_in = _A_setup()

    def _B_setup():
        Bsz, L, D, N = 1, 2048, 512, 128
        return {
            "u": _w((lambda *s: torch.randn(*s, dtype=torch.bfloat16) * 0.05)(Bsz, L, D)),
            "log_A_real": _w((lambda *s: torch.randn(*s, dtype=torch.bfloat16) * 6.4)(D, N) * 0.5),
            "A_imag": _w((lambda *s: torch.randn(*s, dtype=torch.bfloat16) * 0.1)(D, N) * 3.14),
            "C_real": _w((lambda *s: torch.randn(*s, dtype=torch.bfloat16) * 0.1)(D, N) / N ** 0.5),
            "C_imag": _w((lambda *s: torch.randn(*s, dtype=torch.bfloat16) * 0.1)(D, N) / N ** 0.5),
            "log_dt": _w((lambda *s: torch.randn(*s, dtype=torch.bfloat16) * 0.1)(D) * 0.5 - 4.0),
            "D_skip": _w((lambda *s: torch.randn(*s, dtype=torch.bfloat16) * 0.1)(D)),
        }
    _B_in = _B_setup()

    return {
        "A_x": _A_in["x"],
        "A_wqkv": _A_in["wqkv"],
        "A_wo": _A_in["wo"],
        "B_u": _B_in["u"],
        "B_log_A_real": _B_in["log_A_real"],
        "B_A_imag": _B_in["A_imag"],
        "B_C_real": _B_in["C_real"],
        "B_C_imag": _B_in["C_imag"],
        "B_log_dt": _B_in["log_dt"],
        "B_D_skip": _B_in["D_skip"],
    }

def reference(A_x, A_wqkv, A_wo, B_u, B_log_A_real, B_A_imag, B_C_real, B_C_imag, B_log_dt, B_D_skip):
    def _A_ref(x, wqkv, wo):
        B, S, D = x.shape
        H = 32; Dh = D // H
        W, dil = 8, 2
        qkv = (lambda x, *d: x.permute(*d))((x @ wqkv).reshape(B, S, 3, H, Dh), 2, 0, 3, 1, 4)
        q, k, v = qkv[0], qkv[1], qkv[2]
        scale = Dh ** -0.5
        pos = torch.arange(S)
        off = torch.arange(W) * dil
        idx = pos[:, None] - off[None, :]
        mask = idx >= 0
        idx_c = torch.where(mask, idx, 0).reshape(-1)
        kg = k[:, :, idx_c, :].reshape(B, H, S, W, Dh)
        vg = v[:, :, idx_c, :].reshape(B, H, S, W, Dh)
        logits = torch.einsum("bhsd,bhswd->bhsw", q, kg) * scale
        logits = torch.where(mask[None, None, :, :], logits, -1e9)
        attn = _softmax(logits, axis=-1)
        o = torch.einsum("bhsw,bhswd->bhsd", attn, vg)
        return (lambda x, *d: x.permute(*d))(o, 0, 2, 1, 3).reshape(B, S, D) @ wo

    def _B_ref(u, log_A_real, A_imag, C_real, C_imag, log_dt, D_skip):
        L = u.shape[1]
        dt = torch.exp(log_dt)
        A = -torch.exp(log_A_real) + 1j * A_imag
        C = (C_real + 1j * C_imag) * dt[:, None]
        pos = (lambda x, dt: x.to(dt))(torch.arange(L), log_dt.dtype)
        P = torch.exp((A * dt[:, None])[:, :, None] * pos[None, None, :])
        K = 2.0 * torch.einsum("dn,dnl->dl", C, P).real
        L2 = 2 * L
        u_t = (lambda x, *d: x.permute(*d))(u, 0, 2, 1)
        Kf = (lambda x, *a, **k: torch.fft.rfft(x.float(), *a, **k))(K, L2)
        Uf = (lambda x, *a, **k: torch.fft.rfft(x.float(), *a, **k))(u_t, L2)
        y = (lambda x, *a, **k: torch.fft.irfft(x, *a, **k).to(torch.bfloat16))(Uf * Kf[None, :, :], L2)[:, :, :L]
        y = (lambda x, *d: x.permute(*d))(y, 0, 2, 1)
        return y + u * D_skip[None, None, :]

    x = A_x
    x = x + _A_ref(_layernorm(x, axis=-1), A_wqkv, A_wo)
    x = x + _B_ref(_layernorm(x, axis=-1), B_log_A_real, B_A_imag, B_C_real, B_C_imag, B_log_dt, B_D_skip)
    x = x + _A_ref(_layernorm(x, axis=-1), A_wqkv, A_wo)
    x = x + _B_ref(_layernorm(x, axis=-1), B_log_A_real, B_A_imag, B_C_real, B_C_imag, B_log_dt, B_D_skip)
    return x
```

```cpp
#include <hip/hip_runtime.h>
#include <hip/hip_bf16.h>
#include <stdint.h>
typedef unsigned short u16;
static __device__ __forceinline__ float b2f(u16 v){ return __uint_as_float(((unsigned)v)<<16); }
static __device__ __forceinline__ u16 f2b(float f){ __hip_bfloat16 h = __float2bfloat16(f); return *(u16*)&h; }
static __device__ __forceinline__ float rb(float f){ return b2f(f2b(f)); }
#define S 2048
#define D 512
__global__ void k_cvt(const u16* x, float* o, int n){ int i=blockIdx.x*256+threadIdx.x; if(i<n) o[i]=b2f(x[i]); }
__global__ void k_out(const float* x, u16* o, int n){ int i=blockIdx.x*256+threadIdx.x; if(i<n) o[i]=f2b(x[i]); }
__global__ void k_ln(const float* x, float* o){
  int r=blockIdx.x; int t=threadIdx.x;
  float v[8]; float s=0; for(int i=0;i<8;i++){ v[i]=x[r*D+t+64*i]; s+=v[i]; }
  for(int m=32;m;m>>=1) s+=__shfl_xor(s,m); float mu=s/D; float q=0;
  for(int i=0;i<8;i++){ v[i]-=mu; q+=v[i]*v[i]; }
  for(int m=32;m;m>>=1) q+=__shfl_xor(q,m); float rs=rsqrtf(q/D+1e-5f);
  for(int i=0;i<8;i++) o[r*D+t+64*i]=rb(v[i]*rs);
}
__global__ void k_gemm(const float* a, const u16* w, float* o, const float* res, int N){
  int r=blockIdx.y; int c=blockIdx.x*256+threadIdx.x; float s=0;
  for(int k=0;k<D;k++) s+=a[r*D+k]*b2f(w[k*N+c]);
  if(res) s+=res[r*N+c];
  o[r*N+c]=rb(s);
}
__global__ void k_attn(const float* qkv, float* o){
  int s=blockIdx.x; int h=threadIdx.x; if(h>=32) return;
  float lg[8]; float mx=-1e30f;
  #pragma unroll
  for(int w=0;w<8;w++){ int j=s-2*w; float a=-1e9f; if(j>=0){ a=0; for(int d=0;d<16;d++) a+=qkv[s*1536+h*16+d]*qkv[j*1536+512+h*16+d]; a*=0.25f;} lg[w]=a; mx=fmaxf(mx,a);}
  float sum=0;
  #pragma unroll
  for(int w=0;w<8;w++){ lg[w]=__expf(lg[w]-mx); sum+=lg[w]; }
  for(int d=0;d<16;d++){ float a=0;
    #pragma unroll
    for(int w=0;w<8;w++){ int j=s-2*w; if(j<0) j=0; a+=lg[w]*qkv[j*1536+1024+h*16+d]; }
    o[s*D+h*16+d]=rb(a/sum); }
}
__global__ void k_kgen(const u16* lar, const u16* aim, const u16* cr, const u16* ci, const u16* ldt, float* K){
  int d=blockIdx.y; int l=blockIdx.x*256+threadIdx.x;
  float dt=rb(expf(b2f(ldt[d])));
  float p=rb((float)l);
  float acc=0;
  for(int n=0;n<128;n++){
    float ar=-rb(expf(b2f(lar[d*128+n])))*dt, ai=b2f(aim[d*128+n])*dt;
    float c_r=b2f(cr[d*128+n])*dt, c_i=b2f(ci[d*128+n])*dt;
    float e=expf(ar*p); float sn,cs; sn=__sinf(ai*p); cs=__cosf(ai*p);
    acc+=e*(c_r*cs-c_i*sn);
  }
  K[l*D+d]=2*acc;
}
__global__ void k_conv(const float* u, const float* K, const u16* dsk, const float* res, float* o){
  int t=blockIdx.y; int d=blockIdx.x*256+threadIdx.x; float s=0;
  for(int l=0;l<=t;l++) s+=K[l*D+d]*u[(t-l)*D+d];
  o[t*D+d]=rb(res[t*D+d]+s+u[t*D+d]*b2f(dsk[d]));
}
extern "C" void kernel_launch(void* const* d_in, const int* in_sizes, int n_in, void* d_out, int out_size, void* d_ws, size_t ws_size, hipStream_t st) {
  const u16* x=(const u16*)d_in[0]; const u16* wqkv=(const u16*)d_in[1]; const u16* wo=(const u16*)d_in[2];
  float* ws=(float*)d_ws; float* xa=ws; float* xb=xa+S*D; float* xn=xb+S*D; float* o=xn+S*D; float* K=o+S*D; float* qkv=K+S*D;
  k_cvt<<<S*D/256,256,0,st>>>(x,xa,S*D);
  k_kgen<<<dim3(S/256,D),256,0,st>>>((const u16*)d_in[4],(const u16*)d_in[5],(const u16*)d_in[6],(const u16*)d_in[7],(const u16*)d_in[8],K);
  for(int l=0;l<2;l++){
    k_ln<<<S,64,0,st>>>(xa,xn);
    k_gemm<<<dim3(6,S),256,0,st>>>(xn,wqkv,qkv,nullptr,1536);
    k_attn<<<S,64,0,st>>>(qkv,o);
    k_gemm<<<dim3(2,S),256,0,st>>>(o,wo,xb,xa,512);
    k_ln<<<S,64,0,st>>>(xb,xn);
    k_conv<<<dim3(2,S),256,0,st>>>(xn,K,(const u16*)d_in[9],xb,xa);
  }
  k_out<<<S*D/256,256,0,st>>>(xa,(u16*)d_out,S*D);
}
```

```cpp
#include <hip/hip_runtime.h>

__global__ void __launch_bounds__(256, 1) __attribute__((aligned(4096))) fused_layers_kernel(
        const void* x, unsigned nx, unsigned fx, const void* wqkv, unsigned nw, unsigned fw,
        const void* wo, unsigned no, unsigned fo, void* ws, unsigned ns, unsigned fs,
        void* out, unsigned nout, unsigned fout, const void* lar, const void* aim, const void* cr,
        const void* ci, const void* ldt, const void* dsk) {
#if defined(__HIP_DEVICE_COMPILE__)
    __shared__ unsigned char lds[163840];
    auto ka = __builtin_amdgcn_kernarg_segment_ptr();
    asm volatile(
        "s_load_dwordx16 s[8:23], %0, 0\n"
        "s_load_dwordx16 s[24:39], %0, 64\n"
        "s_lshr_b32 s41, %1, 3\n"
        "s_lshl_b32 s42, s41, 7\n"
        "v_lshl_add_u32 v195, v0, 12, s42\n"
        "s_getpc_b64 s[92:93]\n"
        "L_pc_%=:\n"
        "s_mov_b32 s94, L_end_%=-L_pc_%=\n"
        "s_mov_b32 s95, 0x20000\n"
        "s_and_b32 s93, s93, 0xffff\n"
        "buffer_load_dword v192, v195, s[92:95], 0 offen\n"
        "s_and_b32 s40, %1, 7\n"
        "s_lshl_b32 s50, s40, 5\n"
        "s_add_u32 s50, s50, s41\n"
        "v_lshrrev_b32 v3, 6, v0\n"
        "v_and_b32 v1, 63, v0\n"
        "v_readfirstlane_b32 s51, v3\n"
        "s_lshr_b32 s52, s50, 3\n"
        "s_and_b32 s53, s50, 7\n"
        "s_and_b32 s42, s52, 3\n"
        "s_and_b32 s43, s40, 3\n"
        "s_lshl2_add_u32 s56, s42, s43\n"
        "s_lshr_b32 s54, s52, 1\n"
        "s_and_b32 s55, s52, 1\n"
        "v_lshlrev_b32 v4, 4, v1\n"
        "s_lshl_b32 s41, s50, 9\n"
        "v_lshl_add_u32 v198, v0, 1, s41\n"
        "s_lshl_b32 s42, s54, 6\n"
        "s_add_u32 s42, s42, s55\n"
        "s_lshl_b32 s42, s42, 10\n"
        "s_lshl_b32 s79, s51, 11\n"
        "s_add_u32 s77, s42, s79\n"
        "s_and_b32 s41, s53, 7\n"
        "v_lshl_add_u32 v200, v0, 3, s41\n"
        "v_lshrrev_b32 v201, 3, v200\n"
        "v_and_b32 v200, 7, v200\n"
        "s_sub_u32 s41, s42, 0x4000\n"
        "v_cmp_gt_u32 vcc, 40, v201\n"
        "v_add_u32 v202, 0x1d8, v201\n"
        "v_lshl_add_u32 v199, v200, 7, s41\n"
        "v_cndmask_b32 v202, v202, v201, vcc\n"
        "v_cmp_gt_u32 vcc, 48, v201\n"
        "v_lshl_add_u32 v199, v202, 11, v199\n"
        "s_mul_i32 s80, s51, 0xc00\n"
        "v_cndmask_b32 v199, -16, v199, vcc\n"
        "v_lshrrev_b32 v200, 3, v0\n"
        "v_mul_u32_u24 v197, 0xc00, v200\n"
        "v_and_b32 v201, 16, v0\n"
        "v_and_b32 v202, 64, v0\n"
        "v_lshl_or_b32 v201, v201, 1, v202\n"
        "v_and_b32 v202, 0x70, v4\n"
        "v_xor_b32 v201, v201, v202\n"
        "s_lshl_b32 s42, s53, 7\n"
        "v_add3_u32 v197, v197, v201, s42\n"
        "s_mul_i32 s78, s51, 0x420\n"
        "s_lshl_b32 s81, s51, 12\n"
        "s_lshl_b32 s42, s56, 6\n"
        "v_add_u32_e64 v196, s42, v4\n"
        "v_and_b32 v196, 0x3ff, v196\n"
        "s_lshl_b32 s46, s50, 2\n"
        "s_waitcnt lgkmcnt(0)\n"
        "global_load_ushort v204, v198, s[28:29]\n"
        "global_load_ushort v205, v198, s[30:31]\n"
        "global_load_ushort v206, v198, s[32:33]\n"
        "global_load_ushort v207, v198, s[34:35]\n"
        "s_load_dword s91, s[36:37], s46\n"
        "s_load_dword s73, s[38:39], s46\n"
        "buffer_load_dwordx2 v[208:209], off, s[20:23], 0 sc1\n"
        "s_cmp_eq_u32 s51, 0\n"
        "s_cselect_b32 m0, 0x145e0, s78\n"
        "s_add_i32 s46, s77, 0xfc000\n"
        "buffer_load_dwordx4 v196, s[8:11], s46 offen lds\n"
        "s_add_u32 m0, s78, 0x1080\n"
        "s_add_i32 s46, s77, 0xfe000\n"
        "buffer_load_dwordx4 v196, s[8:11], s46 offen lds\n"
        "s_add_u32 m0, s78, 0x2100\n"
        "s_add_i32 s46, s77, -0x4000\n"
        "s_max_i32 s46, s46, 0\n"
        "buffer_load_dwordx4 v196, s[8:11], s46 offen lds\n"
        "s_add_u32 m0, s78, 0x3180\n"
        "s_add_i32 s46, s77, -0x2000\n"
        "s_max_i32 s46, s46, 0\n"
        "buffer_load_dwordx4 v196, s[8:11], s46 offen lds\n"
        "s_add_u32 m0, s78, 0x4200\n"
        "s_add_i32 s46, s77, 0\n"
        "buffer_load_dwordx4 v196, s[8:11], s46 offen lds\n"
        "s_add_u32 m0, s78, 0x5280\n"
        "s_add_i32 s46, s77, 0x2000\n"
        "buffer_load_dwordx4 v196, s[8:11], s46 offen lds\n"
        "s_add_u32 m0, s78, 0x6300\n"
        "s_add_i32 s46, s77, 0x4000\n"
        "buffer_load_dwordx4 v196, s[8:11], s46 offen lds\n"
        "s_add_u32 m0, s78, 0x7380\n"
        "s_add_i32 s46, s77, 0x6000\n"
        "buffer_load_dwordx4 v196, s[8:11], s46 offen lds\n"
        "s_add_u32 m0, s78, 0x8400\n"
        "s_add_i32 s46, s77, 0x8000\n"
        "buffer_load_dwordx4 v196, s[8:11], s46 offen lds\n"
        "s_add_u32 m0, s78, 0x9480\n"
        "s_add_i32 s46, s77, 0xa000\n"
        "buffer_load_dwordx4 v196, s[8:11], s46 offen lds\n"
        "s_add_u32 m0, s78, 0xa500\n"
        "s_add_i32 s46, s77, 0xc000\n"
        "buffer_load_dwordx4 v196, s[8:11], s46 offen lds\n"
        "v_and_b32 v2, 15, v1\n"
        "v_lshrrev_b32 v3, 4, v1\n"
        "v_mov_b32 v6, 0x3f803f80\n"
        "v_mov_b32 v7, 0x3f803f80\n"
        "v_mov_b32 v8, 0x3f803f80\n"
        "v_mov_b32 v9, 0x3f803f80\n"
        "ds_write_b128 v4, v[6:9]\n"
        "v_lshlrev_b32_e64 v200, 4, v3\n"
        "s_mov_b32 s40, 0x420\n"
        "v_mad_u32_u24 v32, v2, s40, v200\n"
        "v_bfe_u32 v202, v1, 3, 2\n"
        "v_xor_b32 v202, s51, v202\n"
        "v_lshlrev_b32 v202, 5, v202\n"
        "v_mul_u32_u24 v203, 0xc00, v3\n"
        "v_add_u32 v203, v203, v202\n"
        "v_bfe_u32 v202, v1, 2, 2\n"
        "v_lshl_add_u32 v203, v202, 7, v203\n"
        "v_and_b32 v202, 3, v1\n"
        "v_lshl_add_u32 v203, v202, 3, v203\n"
        "v_add_u32 v214, 0x14a00, v203\n"
        "v_add_u32 v203, 0xc600, v203\n"
        "s_mov_b32 s64, 0x22222222\n"
        "s_mov_b32 s65, 0x22222222\n"
        "s_mov_b32 s66, 0x44444444\n"
        "s_mov_b32 s67, 0x44444444\n"
        "s_mov_b32 s68, 0x88888888\n"
        "s_mov_b32 s69, 0x88888888\n"
        "s_mov_b32 s70, 0xf0000f\n"
        "s_mov_b32 s71, 0xf0000f00\n"
        "s_mov_b32 s60, 0x3b000000\n"
        "s_mov_b32 s61, 0x3727c5ac\n"
        "s_and_b32 s57, s53, 1\n"
        "s_lshr_b32 s58, s53, 1\n"
        "v_lshrrev_b32 v200, 1, v3\n"
        "v_and_b32 v201, 1, v3\n"
        "s_lshl_b32 s44, s51, 16\n"
        "v_lshl_add_u32 v36, v3, 13, s44\n"
        "v_lshl_add_u32 v36, v2, 2, v36\n"
        "s_lshl1_add_u32 s44, s54, s57\n"
        "s_lshl_b32 s45, s55, 5\n"
        "s_add_u32 s44, s44, s45\n"
        "s_lshl_b32 s44, s44, 6\n"
        "s_lshl_b32 s45, s58, 18\n"
        "s_add_u32 s34, s44, s45\n"
        "s_lshl_b32 s45, s58, 13\n"
        "s_add_u32 s35, s44, s45\n"
        "v_lshlrev_b32 v37, 2, v2\n"
        "v_lshl_add_u32 v37, v200, 12, v37\n"
        "s_lshl_b32 s44, s51, 28\n"
        "v_or_b32 v37, s44, v37\n"
        "v_lshl_or_b32 v37, v201, 30, v37\n"
        "s_lshl2_add_u32 s75, s58, s51\n"
        "s_sub_u32 s75, s75, s56\n"
        "s_and_b32 s75, s75, 15\n"
        "s_mul_i32 s44, s57, 0x4200\n"
        "s_lshl_b32 s45, s75, 6\n"
        "s_add_u32 s44, s44, s45\n"
        "v_lshlrev_b32 v202, 3, v3\n"
        "v_sub_u32 v35, v32, v202\n"
        "v_add_u32 v35, s44, v35\n"
        "v_lshrrev_b32 v200, 4, v0\n"
        "v_lshlrev_b32 v38, 11, v200\n"
        "v_bfe_u32 v200, v0, 4, 1\n"
        "v_bfe_u32 v201, v0, 6, 1\n"
        "v_lshl_or_b32 v200, v201, 1, v200\n"
        "v_lshlrev_b32 v200, 6, v200\n"
        "v_and_b32 v201, 0xf0, v4\n"
        "v_xor_b32 v200, v200, v201\n"
        "s_lshl_b32 s44, s58, 8\n"
        "v_add3_u32 v38, v38, v200, s44\n"
        "v_xor_b32 v39, 32, v38\n"
        "s_cmp_eq_u32 s54, 0\n"
        "s_cselect_b32 s74, 0xf149f2ca, 0\n"
        "v_lshlrev_b32 v200, 2, v3\n"
        "v_sub_u32 v200, v2, v200\n"
        "v_mov_b32 v202, 0xf149f2ca\n"
        "s_mov_b32 s44, 0xffff\n"
        "v_subrev_u32 v201, 0, v200\n"
        "v_cmp_gt_u32 s[38:39], 8, v201\n"
        "s_nop 1\n"
        "v_cndmask_b32 v26, v202, 0, s[38:39]\n"
        "v_cmp_gt_i32 s[38:39], -8, v201\n"
        "s_nop 1\n"
        "v_cndmask_b32 v22, v202, 0, s[38:39]\n"
        "v_cndmask_b32 v30, 0, -1, s[38:39]\n"
        "v_subrev_u32_e64 v201, 1, v200\n"
        "v_cmp_gt_u32 s[38:39], 8, v201\n"
        "s_nop 1\n"
        "v_cndmask_b32 v27, v202, 0, s[38:39]\n"
        "v_cmp_gt_i32 s[38:39], -8, v201\n"
        "s_nop 1\n"
        "v_cndmask_b32 v23, v202, 0, s[38:39]\n"
        "v_cndmask_b32 v201, 0, -1, s[38:39]\n"
        "s_mul_i32 s41, s51, 0x1200\n"
        "s_add_u32 s41, s41, 0x14a00\n"
        "v_add_u32 v219, s41, v4\n"
        "v_and_b32 v216, 60, v1\n"
        "v_lshl_add_u32 v220, v216, 2, s41\n"
        "v_and_b32 v216, 1, v1\n"
        "v_lshl_add_u32 v220, v216, 3, v220\n"
        "v_and_b32 v216, 2, v1\n"
        "v_mul_u32_u24 v216, 0x240, v216\n"
        "v_add_u32 v220, v220, v216\n"
        "s_mul_i32 s49, s51, 0xd00\n"
        "s_add_u32 s49, s49, 0x23c00\n"
        "v_and_b32 v216, 7, v1\n"
        "v_lshl_add_u32 v216, v216, 2, s49\n"
        "v_mov_b32 v217, 0\n"
        "ds_write_b32 v216, v217\n"
        "v_lshrrev_b32 v216, 5, v1\n"
        "v_and_b32 v217, 1, v3\n"
        "v_lshlrev_b32 v221, 3, v2\n"
        "v_lshl_add_u32 v221, v217, 2, v221\n"
        "v_lshl_add_u32 v221, v216, 7, v221\n"
        "v_sub_u32 v217, 2, v216\n"
        "v_lshlrev_b32 v222, v217, v2\n"
        "v_lshl_add_u32 v224, v3, 6, v2\n"
        "v_lshl_add_u32 v222, v3, 6, v222\n"
        "v_add_u32 v222, 0x100, v222\n"
        "v_add_u32 v224, 0x200, v224\n"
        "v_lshrrev_b32 v217, 4, v221\n"
        "v_add_u32 v221, v221, v217\n"
        "v_lshl_add_u32 v221, v221, 2, s49\n"
        "v_lshrrev_b32 v217, 4, v222\n"
        "v_add_u32 v222, v222, v217\n"
        "v_lshl_add_u32 v222, v222, 2, s49\n"
        "v_lshrrev_b32 v217, 4, v224\n"
        "v_add_u32 v224, v224, v217\n"
        "v_lshl_add_u32 v224, v224, 2, s49\n"
        "v_lshl_add_u32 v223, v216, 7, v222\n"
        "s_mov_b64 exec, 0\n"
        "s_cmp_eq_u32 s51, 0\n"
        "s_cbranch_scc1 L_pg1_%=\n"
        "s_cmp_eq_u32 s51, 1\n"
        "s_cbranch_scc1 L_pg2_%=\n"
        "s_cmp_eq_u32 s51, 2\n"
        "s_cbranch_scc1 L_pg3_%=\n"
        "s_branch L_pg4_%=\n"
        "L_pgret_%=:\n"
        "s_mov_b64 exec, -1\n"
        "s_waitcnt lgkmcnt(0)\n"
        "s_lshl_b32 s40, s91, 16\n"
        "s_and_b32 s41, s91, 0xffff0000\n"
        "s_cmp_lt_u32 s51, 2\n"
        "s_cselect_b32 s40, s40, s41\n"
        "v_mov_b32 v215, s40\n"
        "s_mov_b32 s40, 0x3fb8aa3b\n"
        "v_mul_f32 v216, s40, v215\n"
        "v_fma_f32 v217, v215, s40, -v216\n"
        "v_fmac_f32 v217, 0x32a57060, v215\n"
        "v_exp_f32 v216, v216\n"
        "v_mul_f32 v217, 0x3f317218, v217\n"
        "v_fma_f32 v216, v216, v217, v216\n"
        "v_cvt_pk_bf16_f32 v216, v216, v216\n"
        "v_lshlrev_b32 v215, 16, v216\n"
        "s_waitcnt vmcnt(15)\n"
        "v_lshlrev_b32_e64 v204, 16, v204\n"
        "s_add_u32 m0, s78, 0xb580\n"
        "s_add_i32 s46, s77, 0xe000\n"
        "s_mov_b32 s40, 0x3fb8aa3b\n"
        "v_mul_f32_e64 v216, s40, v204\n"
        "buffer_load_dwordx4 v196, s[8:11], s46 offen lds\n"
        "v_fma_f32 v217, v204, s40, -v216\n"
        "v_fmac_f32 v217, 0x32a57060, v204\n"
        "v_exp_f32_e64 v216, v216\n"
        "v_mul_f32 v217, 0x3f317218, v217\n"
        "v_fma_f32 v216, v216, v217, v216\n"
        "v_cvt_pk_bf16_f32 v216, v216, v216\n"
        "v_lshlrev_b32 v204, 16, v216\n"
        "s_waitcnt vmcnt(15)\n"
        "v_lshlrev_b32 v205, 16, v205\n"
        "v_mul_f32 v204, v204, v215\n"
        "v_mul_f32 v205, v205, v215\n"
        "v_mul_f32 v204, 0xbfb8aa3b, v204\n"
        "v_mul_f32 v205, 0x3e22f983, v205\n"
        "s_waitcnt vmcnt(14)\n"
        "v_lshlrev_b32 v226, 16, v206\n"
        "s_waitcnt vmcnt(13)\n"
        "v_lshlrev_b32 v227, 16, v207\n"
        "v_max_f32 v218, |v226|, |v227|\n"
        "v_mul_f32 v216, 0x41000000, v204\n"
        "v_mul_f32 v217, 0x41000000, v205\n"
        "v_max_f32_dpp v218, v218, v218 row_ror:8 row_mask:0xf bank_mask:0xf\n"
        "v_exp_f32 v216, v216\n"
        "v_fract_f32 v217, v217\n"
        "v_max_f32_dpp v218, v218, v218 row_ror:4 row_mask:0xf bank_mask:0xf\n"
        "v_cos_f32 v230, v217\n"
        "v_sin_f32 v231, v217\n"
        "v_max_f32_dpp v218, v218, v218 row_ror:2 row_mask:0xf bank_mask:0xf\n"
        "v_mul_f32 v230, v230, v216\n"
        "v_mul_f32 v231, v231, v216\n"
        "v_max_f32_dpp v218, v218, v218 row_ror:1 row_mask:0xf bank_mask:0xf\n"
        "v_mul_f32 v216, 1.0, v204\n"
        "v_mul_f32 v217, 1.0, v205\n"
        "v_mov_b32 v206, v218\n"
        "v_exp_f32 v216, v216\n"
        "v_fract_f32 v217, v217\n"
        "v_permlane16_swap_b32 v218, v206\n"
        "s_add_u32 s46, s56, 0\n"
        "s_and_b32 s46, s46, 15\n"
        "s_add_u32 m0, s80, 0x1aa00\n"
        "s_mul_i32 s46, s46, 0x18000\n"
        "v_cos_f32 v234, v217\n"
        "buffer_load_dwordx4 v197, s[12:15], s46 offen lds\n"
        "v_sin_f32 v235, v217\n"
        "v_max_f32 v218, v218, v206\n"
        "v_mul_f32 v234, v234, v216\n"
        "v_mul_f32 v235, -v235, v216\n"
        "v_mov_b32 v206, v218\n"
        "v_mul_f32 v216, 0x43000000, v204\n"
        "v_mul_f32 v217, 0x43000000, v205\n"
        "v_permlane32_swap_b32 v218, v206\n"
        "v_exp_f32 v216, v216\n"
        "v_fract_f32 v217, v217\n"
        "v_max_f32 v218, v218, v206\n"
        "v_cos_f32 v232, v217\n"
        "v_sin_f32 v233, v217\n"
        "v_mul_f32 v232, v232, v216\n"
        "v_mul_f32 v233, -v233, v216\n"
        "v_and_b32 v218, 0x7f800000, v218\n"
        "v_max_u32 v218, 0x800000, v218\n"
        "v_min_u32 v218, 0x7e800000, v218\n"
        "v_sub_u32 v216, 0x7f000000, v218\n"
        "v_mul_f32 v226, v226, v216\n"
        "v_mul_f32 v227, v227, v216\n"
        "v_add_f32 v215, v215, v215\n"
        "v_mul_f32 v218, v218, v215\n"
        "v_pk_mul_f32 v[216:217], v[226:227], v[230:231] op_sel:[1,1] op_sel_hi:[1,0]\n"
        "v_pk_fma_f32 v[228:229], v[226:227], v[230:231], v[216:217] op_sel:[0,0,0] op_sel_hi:[0,1,1] neg_lo:[0,0,1]\n"
        "v_cvt_pk_f16_f32 v204, v226, v228\n"
        "v_cvt_pk_f16_f32 v248, v227, v229\n"
        "v_pk_mul_f32 v[216:217], v[228:229], v[230:231] op_sel:[1,1] op_sel_hi:[1,0]\n"
        "v_pk_fma_f32 v[226:227], v[228:229], v[230:231], v[216:217] op_sel:[0,0,0] op_sel_hi:[0,1,1] neg_lo:[0,0,1]\n"
        "v_pk_mul_f32 v[216:217], v[226:227], v[230:231] op_sel:[1,1] op_sel_hi:[1,0]\n"
        "v_pk_fma_f32 v[228:229], v[226:227], v[230:231], v[216:217] op_sel:[0,0,0] op_sel_hi:[0,1,1] neg_lo:[0,0,1]\n"
        "v_cvt_pk_f16_f32 v205, v226, v228\n"
        "v_cvt_pk_f16_f32 v249, v227, v229\n"
        "v_pk_mul_f32 v[216:217], v[228:229], v[230:231] op_sel:[1,1] op_sel_hi:[1,0]\n"
        "v_pk_fma_f32 v[226:227], v[228:229], v[230:231], v[216:217] op_sel:[0,0,0] op_sel_hi:[0,1,1] neg_lo:[0,0,1]\n"
        "buffer_load_dwordx4 v197, s[12:15], s46 offen offset:1024 lds\n"
        "v_pk_mul_f32 v[216:217], v[226:227], v[230:231] op_sel:[1,1] op_sel_hi:[1,0]\n"
        "v_pk_fma_f32 v[228:229], v[226:227], v[230:231], v[216:217] op_sel:[0,0,0] op_sel_hi:[0,1,1] neg_lo:[0,0,1]\n"
        "v_cvt_pk_f16_f32 v206, v226, v228\n"
        "v_cvt_pk_f16_f32 v250, v227, v229\n"
        "v_pk_mul_f32 v[216:217], v[228:229], v[230:231] op_sel:[1,1] op_sel_hi:[1,0]\n"
        "v_pk_fma_f32 v[226:227], v[228:229], v[230:231], v[216:217] op_sel:[0,0,0] op_sel_hi:[0,1,1] neg_lo:[0,0,1]\n"
        "v_pk_mul_f32 v[216:217], v[226:227], v[230:231] op_sel:[1,1] op_sel_hi:[1,0]\n"
        "v_pk_fma_f32 v[228:229], v[226:227], v[230:231], v[216:217] op_sel:[0,0,0] op_sel_hi:[0,1,1] neg_lo:[0,0,1]\n"
        "v_cvt_pk_f16_f32 v207, v226, v228\n"
        "v_cvt_pk_f16_f32 v251, v227, v229\n"
        "ds_write_b128 v219, v[204:207]\n"
        "ds_write_b128 v219, v[248:251] offset:2304\n"
        "v_pk_mul_f32 v[216:217], v[228:229], v[230:231] op_sel:[1,1] op_sel_hi:[1,0]\n"
        "v_pk_fma_f32 v[226:227], v[228:229], v[230:231], v[216:217] op_sel:[0,0,0] op_sel_hi:[0,1,1] neg_lo:[0,0,1]\n"
        "v_pk_mul_f32 v[216:217], v[226:227], v[230:231] op_sel:[1,1] op_sel_hi:[1,0]\n"
        "v_pk_fma_f32 v[228:229], v[226:227], v[230:231], v[216:217] op_sel:[0,0,0] op_sel_hi:[0,1,1] neg_lo:[0,0,1]\n"
        "v_cvt_pk_f16_f32 v204, v226, v228\n"
        "v_cvt_pk_f16_f32 v248, v227, v229\n"
        "v_pk_mul_f32 v[216:217], v[228:229], v[230:231] op_sel:[1,1] op_sel_hi:[1,0]\n"
        "v_pk_fma_f32 v[226:227], v[228:229], v[230:231], v[216:217] op_sel:[0,0,0] op_sel_hi:[0,1,1] neg_lo:[0,0,1]\n"
        "v_pk_mul_f32 v[216:217], v[226:227], v[230:231] op_sel:[1,1] op_sel_hi:[1,0]\n"
        "v_pk_fma_f32 v[228:229], v[226:227], v[230:231], v[216:217] op_sel:[0,0,0] op_sel_hi:[0,1,1] neg_lo:[0,0,1]\n"
        "v_cvt_pk_f16_f32 v205, v226, v228\n"
        "v_cvt_pk_f16_f32 v249, v227, v229\n"
        "v_pk_mul_f32 v[216:217], v[228:229], v[230:231] op_sel:[1,1] op_sel_hi:[1,0]\n"
        "v_pk_fma_f32 v[226:227], v[228:229], v[230:231], v[216:217] op_sel:[0,0,0] op_sel_hi:[0,1,1] neg_lo:[0,0,1]\n"
        "v_pk_mul_f32 v[216:217], v[226:227], v[230:231] op_sel:[1,1] op_sel_hi:[1,0]\n"
        "v_pk_fma_f32 v[228:229], v[226:227], v[230:231], v[216:217] op_sel:[0,0,0] op_sel_hi:[0,1,1] neg_lo:[0,0,1]\n"
        "v_cvt_pk_f16_f32 v206, v226, v228\n"
        "v_cvt_pk_f16_f32 v250, v227, v229\n"
        "buffer_load_dwordx4 v197, s[12:15], s46 offen offset:2048 lds\n"
        "v_pk_mul_f32 v[216:217], v[228:229], v[230:231] op_sel:[1,1] op_sel_hi:[1,0]\n"
        "v_pk_fma_f32 v[226:227], v[228:229], v[230:231], v[216:217] op_sel:[0,0,0] op_sel_hi:[0,1,1] neg_lo:[0,0,1]\n"
        "v_pk_mul_f32 v[216:217], v[226:227], v[230:231] op_sel:[1,1] op_sel_hi:[1,0]\n"
        "v_pk_fma_f32 v[228:229], v[226:227], v[230:231], v[216:217] op_sel:[0,0,0] op_sel_hi:[0,1,1] neg_lo:[0,0,1]\n"
        "v_cvt_pk_f16_f32 v207, v226, v228\n"
        "v_cvt_pk_f16_f32 v251, v227, v229\n"
        "s_add_u32 s46, s56, 1\n"
        "s_and_b32 s46, s46, 15\n"
        "ds_write_b128 v219, v[204:207] offset:1152\n"
        "ds_write_b128 v219, v[248:251] offset:3456\n"
        "ds_read_b64_tr_b16 a[192:193], v220\n"
        "s_add_u32 m0, s80, 0x1da00\n"
        "ds_read_b64_tr_b16 a[194:195], v220 offset:2304\n"
        "s_mul_i32 s46, s46, 0x18000\n"
        "ds_read_b64_tr_b16 a[196:197], v220 offset:256\n"
        "ds_read_b64_tr_b16 a[198:199], v220 offset:2560\n"
        "ds_read_b64_tr_b16 a[200:201], v220 offset:512\n"
        "ds_read_b64_tr_b16 a[202:203], v220 offset:2816\n"
        "ds_read_b64_tr_b16 a[204:205], v220 offset:768\n"
        "ds_read_b64_tr_b16 a[206:207], v220 offset:3072\n"
        "v_cvt_pk_f16_f32 v204, 1.0, v234\n"
        "v_cvt_pk_f16_f32 v248, 0, v235\n"
        "v_pk_mul_f32 v[216:217], v[234:235], v[234:235] op_sel:[1,1] op_sel_hi:[1,0]\n"
        "v_pk_fma_f32 v[236:237], v[234:235], v[234:235], v[216:217] op_sel:[0,0,0] op_sel_hi:[0,1,1] neg_lo:[0,0,1]\n"
        "v_pk_mul_f32 v[216:217], v[236:237], v[234:235] op_sel:[1,1] op_sel_hi:[1,0]\n"
        "v_pk_fma_f32 v[238:239], v[236:237], v[234:235], v[216:217] op_sel:[0,0,0] op_sel_hi:[0,1,1] neg_lo:[0,0,1]\n"
        "v_cvt_pk_f16_f32 v205, v236, v238\n"
        "buffer_load_dwordx4 v197, s[12:15], s46 offen lds\n"
        "v_cvt_pk_f16_f32 v249, v237, v239\n"
        "v_pk_mul_f32 v[216:217], v[238:239], v[234:235] op_sel:[1,1] op_sel_hi:[1,0]\n"
        "v_pk_fma_f32 v[240:241], v[238:239], v[234:235], v[216:217] op_sel:[0,0,0] op_sel_hi:[0,1,1] neg_lo:[0,0,1]\n"
        "v_pk_mul_f32 v[216:217], v[240:241], v[234:235] op_sel:[1,1] op_sel_hi:[1,0]\n"
        "v_pk_fma_f32 v[242:243], v[240:241], v[234:235], v[216:217] op_sel:[0,0,0] op_sel_hi:[0,1,1] neg_lo:[0,0,1]\n"
        "v_cvt_pk_f16_f32 v206, v240, v242\n"
        "v_cvt_pk_f16_f32 v250, v241, v243\n"
        "v_pk_mul_f32 v[216:217], v[242:243], v[234:235] op_sel:[1,1] op_sel_hi:[1,0]\n"
        "v_pk_fma_f32 v[244:245], v[242:243], v[234:235], v[216:217] op_sel:[0,0,0] op_sel_hi:[0,1,1] neg_lo:[0,0,1]\n"
        "v_pk_mul_f32 v[216:217], v[244:245], v[234:235] op_sel:[1,1] op_sel_hi:[1,0]\n"
        "v_pk_fma_f32 v[246:247], v[244:245], v[234:235], v[216:217] op_sel:[0,0,0] op_sel_hi:[0,1,1] neg_lo:[0,0,1]\n"
        "v_cvt_pk_f16_f32 v207, v244, v246\n"
        "v_cvt_pk_f16_f32 v251, v245, v247\n"
        "ds_write_b128 v219, v[204:207]\n"
        "ds_write_b128 v219, v[248:251] offset:2304\n"
        "v_pk_mul_f32 v[216:217], v[232:233], v[234:235] op_sel:[1,1] op_sel_hi:[1,0]\n"
        "v_pk_fma_f32 v[228:229], v[232:233], v[234:235], v[216:217] op_sel:[0,0,0] op_sel_hi:[0,1,1] neg_lo:[0,0,1]\n"
        "v_cvt_pk_f16_f32 v204, v232, v228\n"
        "v_cvt_pk_f16_f32 v248, v233, v229\n"
        "v_pk_mul_f32 v[216:217], v[232:233], v[236:237] op_sel:[1,1] op_sel_hi:[1,0]\n"
        "v_pk_fma_f32 v[226:227], v[232:233], v[236:237], v[216:217] op_sel:[0,0,0] op_sel_hi:[0,1,1] neg_lo:[0,0,1]\n"
        "v_pk_mul_f32 v[216:217], v[232:233], v[238:239] op_sel:[1,1] op_sel_hi:[1,0]\n"
        "v_pk_fma_f32 v[228:229], v[232:233], v[238:239], v[216:217] op_sel:[0,0,0] op_sel_hi:[0,1,1] neg_lo:[0,0,1]\n"
        "v_cvt_pk_f16_f32 v205, v226, v228\n"
        "v_cvt_pk_f16_f32 v249, v227, v229\n"
        "v_pk_mul_f32 v[216:217], v[232:233], v[240:241] op_sel:[1,1] op_sel_hi:[1,0]\n"
        "v_pk_fma_f32 v[226:227], v[232:233], v[240:241], v[216:217] op_sel:[0,0,0] op_sel_hi:[0,1,1] neg_lo:[0,0,1]\n"
        "v_pk_mul_f32 v[216:217], v[232:233], v[242:243] op_sel:[1,1] op_sel_hi:[1,0]\n"
        "v_pk_fma_f32 v[228:229], v[232:233], v[242:243], v[216:217] op_sel:[0,0,0] op_sel_hi:[0,1,1] neg_lo:[0,0,1]\n"
        "v_cvt_pk_f16_f32 v206, v226, v228\n"
        "buffer_load_dwordx4 v197, s[12:15], s46 offen offset:1024 lds\n"
        "v_cvt_pk_f16_f32 v250, v227, v229\n"
        "v_pk_mul_f32 v[216:217], v[232:233], v[244:245] op_sel:[1,1] op_sel_hi:[1,0]\n"
        "v_pk_fma_f32 v[226:227], v[232:233], v[244:245], v[216:217] op_sel:[0,0,0] op_sel_hi:[0,1,1] neg_lo:[0,0,1]\n"
        "v_pk_mul_f32 v[216:217], v[232:233], v[246:247] op_sel:[1,1] op_sel_hi:[1,0]\n"
        "v_pk_fma_f32 v[228:229], v[232:233], v[246:247], v[216:217] op_sel:[0,0,0] op_sel_hi:[0,1,1] neg_lo:[0,0,1]\n"
        "v_cvt_pk_f16_f32 v207, v226, v228\n"
        "v_cvt_pk_f16_f32 v251, v227, v229\n"
        "ds_write_b128 v219, v[204:207] offset:1152\n"
        "ds_write_b128 v219, v[248:251] offset:3456\n"
        "ds_read_b64_tr_b16 a[208:209], v220\n"
        "ds_read_b64_tr_b16 a[210:211], v220 offset:2304\n"
        "ds_read_b64_tr_b16 a[212:213], v220 offset:256\n"
        "ds_read_b64_tr_b16 a[214:215], v220 offset:2560\n"
        "ds_read_b64_tr_b16 a[216:217], v220 offset:512\n"
        "ds_read_b64_tr_b16 a[218:219], v220 offset:2816\n"
        "ds_read_b64_tr_b16 a[220:221], v220 offset:768\n"
        "ds_read_b64_tr_b16 a[222:223], v220 offset:3072\n"
        "s_waitcnt lgkmcnt(6)\n"
        "v_mfma_f32_16x16x32_f16 v[252:255], a[208:211], a[192:195], 0\n"
        "buffer_load_dwordx4 v197, s[12:15], s46 offen offset:2048 lds\n"
        "s_waitcnt lgkmcnt(4)\n"
        "v_mfma_f32_16x16x32_f16 v[252:255], a[212:215], a[196:199], v[252:255]\n"
        "s_add_u32 s46, s56, 2\n"
        "s_and_b32 s46, s46, 15\n"
        "s_waitcnt lgkmcnt(2)\n"
        "v_mfma_f32_16x16x32_f16 v[252:255], a[216:219], a[200:203], v[252:255]\n"
        "s_add_u32 m0, s80, 0xc600\n"
        "s_mul_i32 s46, s46, 0x18000\n"
        "s_waitcnt lgkmcnt(0)\n"
        "v_mfma_f32_16x16x32_f16 v[252:255], a[220:223], a[204:207], v[252:255]\n"
        "v_pk_mul_f32 v[216:217], v[232:233], v[232:233] op_sel:[1,1] op_sel_hi:[1,0]\n"
        "v_pk_fma_f32 v[230:231], v[232:233], v[232:233], v[216:217] op_sel:[0,0,0] op_sel_hi:[0,1,1] neg_lo:[0,0,1]\n"
        "v_pk_mul_f32 v[216:217], v[230:231], v[236:237] op_sel:[1,1] op_sel_hi:[1,0]\n"
        "v_pk_fma_f32 v[228:229], v[230:231], v[236:237], v[216:217] op_sel:[0,0,0] op_sel_hi:[0,1,1] neg_lo:[0,0,1]\n"
        "v_cvt_pk_f16_f32 v204, v230, v228\n"
        "v_cvt_pk_f16_f32 v248, v231, v229\n"
        "v_pk_mul_f32 v[216:217], v[230:231], v[240:241] op_sel:[1,1] op_sel_hi:[1,0]\n"
        "v_pk_fma_f32 v[226:227], v[230:231], v[240:241], v[216:217] op_sel:[0,0,0] op_sel_hi:[0,1,1] neg_lo:[0,0,1]\n"
        "v_pk_mul_f32 v[216:217], v[230:231], v[244:245] op_sel:[1,1] op_sel_hi:[1,0]\n"
        "v_pk_fma_f32 v[228:229], v[230:231], v[244:245], v[216:217] op_sel:[0,0,0] op_sel_hi:[0,1,1] neg_lo:[0,0,1]\n"
        "v_cvt_pk_f16_f32 v205, v226, v228\n"
        "v_cvt_pk_f16_f32 v249, v227, v229\n"
        "v_pk_mul_f32 v[216:217], v[230:231], v[232:233] op_sel:[1,1] op_sel_hi:[1,0]\n"
        "v_pk_fma_f32 v[230:231], v[230:231], v[232:233], v[216:217] op_sel:[0,0,0] op_sel_hi:[0,1,1] neg_lo:[0,0,1]\n"
        "v_pk_mul_f32 v[216:217], v[230:231], v[236:237] op_sel:[1,1] op_sel_hi:[1,0]\n"
        "v_pk_fma_f32 v[228:229], v[230:231], v[236:237], v[216:217] op_sel:[0,0,0] op_sel_hi:[0,1,1] neg_lo:[0,0,1]\n"
        "v_cvt_pk_f16_f32 v206, v230, v228\n"
        "v_cvt_pk_f16_f32 v250, v231, v229\n"
        "v_pk_mul_f32 v[216:217], v[230:231], v[240:241] op_sel:[1,1] op_sel_hi:[1,0]\n"
        "v_pk_fma_f32 v[226:227], v[230:231], v[240:241], v[216:217] op_sel:[0,0,0] op_sel_hi:[0,1,1] neg_lo:[0,0,1]\n"
        "v_pk_mul_f32 v[216:217], v[230:231], v[244:245] op_sel:[1,1] op_sel_hi:[1,0]\n"
        "v_pk_fma_f32 v[228:229], v[230:231], v[244:245], v[216:217] op_sel:[0,0,0] op_sel_hi:[0,1,1] neg_lo:[0,0,1]\n"
        "v_cvt_pk_f16_f32 v207, v226, v228\n"
        "v_cvt_pk_f16_f32 v251, v227, v229\n"
        "buffer_load_dwordx4 v197, s[12:15], s46 offen lds\n"
        "ds_write_b128 v219, v[204:207]\n"
        "ds_write_b128 v219, v[248:251] offset:2304\n"
        "v_mul_f32 v252, v252, v218\n"
        "v_mul_f32 v253, v253, v218\n"
        "v_mul_f32 v254, v254, v218\n"
        "v_mul_f32 v255, v255, v218\n"
        "ds_write2_b32 v221, v252, v253 offset0:8 offset1:9\n"
        "ds_write2_b32 v221, v254, v255 offset0:10 offset1:11\n"
        "v_pk_mul_f32 v[216:217], v[230:231], v[232:233] op_sel:[1,1] op_sel_hi:[1,0]\n"
        "v_pk_fma_f32 v[230:231], v[230:231], v[232:233], v[216:217] op_sel:[0,0,0] op_sel_hi:[0,1,1] neg_lo:[0,0,1]\n"
        "v_pk_mul_f32 v[216:217], v[230:231], v[240:241] op_sel:[1,1] op_sel_hi:[1,0]\n"
        "v_pk_fma_f32 v[228:229], v[230:231], v[240:241], v[216:217] op_sel:[0,0,0] op_sel_hi:[0,1,1] neg_lo:[0,0,1]\n"
        "v_cvt_pk_f16_f32 v204, v230, v228\n"
        "v_cvt_pk_f16_f32 v248, v231, v229\n"
        "v_pk_mul_f32 v[216:217], v[230:231], v[232:233] op_sel:[1,1] op_sel_hi:[1,0]\n"
        "v_pk_fma_f32 v[230:231], v[230:231], v[232:233], v[216:217] op_sel:[0,0,0] op_sel_hi:[0,1,1] neg_lo:[0,0,1]\n"
        "v_pk_mul_f32 v[216:217], v[230:231], v[240:241] op_sel:[1,1] op_sel_hi:[1,0]\n"
        "v_pk_fma_f32 v[228:229], v[230:231], v[240:241], v[216:217] op_sel:[0,0,0] op_sel_hi:[0,1,1] neg_lo:[0,0,1]\n"
        "v_cvt_pk_f16_f32 v205, v230, v228\n"
        "v_cvt_pk_f16_f32 v249, v231, v229\n"
        "v_pk_mul_f32 v[216:217], v[230:231], v[232:233] op_sel:[1,1] op_sel_hi:[1,0]\n"
        "v_pk_fma_f32 v[230:231], v[230:231], v[232:233], v[216:217] op_sel:[0,0,0] op_sel_hi:[0,1,1] neg_lo:[0,0,1]\n"
        "v_pk_mul_f32 v[216:217], v[230:231], v[240:241] op_sel:[1,1] op_sel_hi:[1,0]\n"
        "v_pk_fma_f32 v[228:229], v[230:231], v[240:241], v[216:217] op_sel:[0,0,0] op_sel_hi:[0,1,1] neg_lo:[0,0,1]\n"
        "v_cvt_pk_f16_f32 v206, v230, v228\n"
        "v_cvt_pk_f16_f32 v250, v231, v229\n"
        "buffer_load_dwordx4 v197, s[12:15], s46 offen offset:1024 lds\n"
        "v_pk_mul_f32 v[216:217], v[230:231], v[232:233] op_sel:[1,1] op_sel_hi:[1,0]\n"
        "v_pk_fma_f32 v[230:231], v[230:231], v[232:233], v[216:217] op_sel:[0,0,0] op_sel_hi:[0,1,1] neg_lo:[0,0,1]\n"
        "v_pk_mul_f32 v[216:217], v[230:231], v[240:241] op_sel:[1,1] op_sel_hi:[1,0]\n"
        "v_pk_fma_f32 v[228:229], v[230:231], v[240:241], v[216:217] op_sel:[0,0,0] op_sel_hi:[0,1,1] neg_lo:[0,0,1]\n"
        "v_cvt_pk_f16_f32 v207, v230, v228\n"
        "v_cvt_pk_f16_f32 v251, v231, v229\n"
        "ds_write_b128 v219, v[204:207] offset:1152\n"
        "ds_write_b128 v219, v[248:251] offset:3456\n"
        "ds_read_b64_tr_b16 a[208:209], v220\n"
        "ds_read_b64_tr_b16 a[210:211], v220 offset:2304\n"
        "ds_read_b64_tr_b16 a[212:213], v220 offset:256\n"
        "ds_read_b64_tr_b16 a[214:215], v220 offset:2560\n"
        "ds_read_b64_tr_b16 a[216:217], v220 offset:512\n"
        "ds_read_b64_tr_b16 a[218:219], v220 offset:2816\n"
        "ds_read_b64_tr_b16 a[220:221], v220 offset:768\n"
        "ds_read_b64_tr_b16 a[222:223], v220 offset:3072\n"
        "s_waitcnt lgkmcnt(6)\n"
        "v_mfma_f32_16x16x32_f16 v[252:255], a[208:211], a[192:195], 0\n"
        "buffer_load_dwordx4 v197, s[12:15], s46 offen offset:2048 lds\n"
        "s_waitcnt lgkmcnt(4)\n"
        "v_mfma_f32_16x16x32_f16 v[252:255], a[212:215], a[196:199], v[252:255]\n"
        "s_add_u32 s46, s56, 3\n"
        "s_and_b32 s46, s46, 15\n"
        "s_waitcnt lgkmcnt(2)\n"
        "v_mfma_f32_16x16x32_f16 v[252:255], a[216:219], a[200:203], v[252:255]\n"
        "s_add_u32 m0, s80, 0xf600\n"
        "s_mul_i32 s46, s46, 0x18000\n"
        "s_waitcnt lgkmcnt(0)\n"
        "v_mfma_f32_16x16x32_f16 v[252:255], a[220:223], a[204:207], v[252:255]\n"
        "v_pk_mul_f32 v[216:217], v[230:231], v[232:233] op_sel:[1,1] op_sel_hi:[1,0]\n"
        "v_pk_fma_f32 v[226:227], v[230:231], v[232:233], v[216:217] op_sel:[0,0,0] op_sel_hi:[0,1,1] neg_lo:[0,0,1]\n"
        "v_pk_mul_f32 v[216:217], v[226:227], v[232:233] op_sel:[1,1] op_sel_hi:[1,0]\n"
        "v_pk_fma_f32 v[230:231], v[226:227], v[232:233], v[216:217] op_sel:[0,0,0] op_sel_hi:[0,1,1] neg_lo:[0,0,1]\n"
        "v_cvt_pk_f16_f32 v204, v226, v230\n"
        "v_cvt_pk_f16_f32 v248, v227, v231\n"
        "v_pk_mul_f32 v[216:217], v[230:231], v[232:233] op_sel:[1,1] op_sel_hi:[1,0]\n"
        "v_pk_fma_f32 v[226:227], v[230:231], v[232:233], v[216:217] op_sel:[0,0,0] op_sel_hi:[0,1,1] neg_lo:[0,0,1]\n"
        "v_pk_mul_f32 v[216:217], v[226:227], v[232:233] op_sel:[1,1] op_sel_hi:[1,0]\n"
        "v_pk_fma_f32 v[230:231], v[226:227], v[232:233], v[216:217] op_sel:[0,0,0] op_sel_hi:[0,1,1] neg_lo:[0,0,1]\n"
        "v_cvt_pk_f16_f32 v205, v226, v230\n"
        "v_cvt_pk_f16_f32 v249, v227, v231\n"
        "v_pk_mul_f32 v[216:217], v[230:231], v[232:233] op_sel:[1,1] op_sel_hi:[1,0]\n"
        "v_pk_fma_f32 v[226:227], v[230:231], v[232:233], v[216:217] op_sel:[0,0,0] op_sel_hi:[0,1,1] neg_lo:[0,0,1]\n"
        "v_pk_mul_f32 v[216:217], v[226:227], v[232:233] op_sel:[1,1] op_sel_hi:[1,0]\n"
        "v_pk_fma_f32 v[230:231], v[226:227], v[232:233], v[216:217] op_sel:[0,0,0] op_sel_hi:[0,1,1] neg_lo:[0,0,1]\n"
        "v_cvt_pk_f16_f32 v206, v226, v230\n"
        "v_cvt_pk_f16_f32 v250, v227, v231\n"
        "v_pk_mul_f32 v[216:217], v[230:231], v[232:233] op_sel:[1,1] op_sel_hi:[1,0]\n"
        "v_pk_fma_f32 v[226:227], v[230:231], v[232:233], v[216:217] op_sel:[0,0,0] op_sel_hi:[0,1,1] neg_lo:[0,0,1]\n"
        "v_pk_mul_f32 v[216:217], v[226:227], v[232:233] op_sel:[1,1] op_sel_hi:[1,0]\n"
        "v_pk_fma_f32 v[230:231], v[226:227], v[232:233], v[216:217] op_sel:[0,0,0] op_sel_hi:[0,1,1] neg_lo:[0,0,1]\n"
        "v_cvt_pk_f16_f32 v207, v226, v230\n"
        "v_cvt_pk_f16_f32 v251, v227, v231\n"
        "buffer_load_dwordx4 v197, s[12:15], s46 offen lds\n"
        "ds_write_b128 v219, v[204:207]\n"
        "ds_write_b128 v219, v[248:251] offset:2304\n"
        "v_mul_f32 v252, v252, v218\n"
        "v_mul_f32 v253, v253, v218\n"
        "v_mul_f32 v254, v254, v218\n"
        "v_mul_f32 v255, v255, v218\n"
        "ds_write2_b32 v222, v252, v253 offset0:8 offset1:9\n"
        "ds_write2_b32 v223, v254, v255 offset0:10 offset1:11\n"
        "v_pk_mul_f32 v[216:217], v[230:231], v[232:233] op_sel:[1,1] op_sel_hi:[1,0]\n"
        "v_pk_fma_f32 v[226:227], v[230:231], v[232:233], v[216:217] op_sel:[0,0,0] op_sel_hi:[0,1,1] neg_lo:[0,0,1]\n"
        "v_cvt_pk_f16_f32 v204, v226, v226\n"
        "v_cvt_pk_f16_f32 v248, v227, v227\n"
        "ds_write_b128 v219, v[204:207] offset:1152\n"
        "ds_write_b128 v219, v[248:251] offset:3456\n"
        "ds_read_b64_tr_b16 a[208:209], v220\n"
        "ds_read_b64_tr_b16 a[210:211], v220 offset:2304\n"
        "buffer_load_dwordx4 v197, s[12:15], s46 offen offset:1024 lds\n"
        "ds_read_b64_tr_b16 a[212:213], v220 offset:256\n"
        "ds_read_b64_tr_b16 a[214:215], v220 offset:2560\n"
        "ds_read_b64_tr_b16 a[216:217], v220 offset:512\n"
        "ds_read_b64_tr_b16 a[218:219], v220 offset:2816\n"
        "ds_read_b64_tr_b16 a[220:221], v220 offset:768\n"
        "ds_read_b64_tr_b16 a[222:223], v220 offset:3072\n"
        "s_waitcnt lgkmcnt(6)\n"
        "v_mfma_f32_16x16x32_f16 v[252:255], a[208:211], a[192:195], 0\n"
        "s_waitcnt lgkmcnt(4)\n"
        "v_mfma_f32_16x16x32_f16 v[252:255], a[212:215], a[196:199], v[252:255]\n"
        "s_waitcnt lgkmcnt(2)\n"
        "v_mfma_f32_16x16x32_f16 v[252:255], a[216:219], a[200:203], v[252:255]\n"
        "s_waitcnt lgkmcnt(0)\n"
        "v_mfma_f32_16x16x32_f16 v[252:255], a[220:223], a[204:207], v[252:255]\n"
        "s_nop 7\n"
        "v_mul_f32 v252, v252, v218\n"
        "buffer_load_dwordx4 v197, s[12:15], s46 offen offset:2048 lds\n"
        "v_mul_f32 v253, v253, v218\n"
        "v_mul_f32 v254, v254, v218\n"
        "v_mul_f32_e64 v255, v255, v218\n"
        "s_add_u32 s46, s56, 4\n"
        "s_and_b32 s46, s46, 15\n"
        "ds_write2_b32 v224, v252, v253 offset0:8 offset1:25\n"
        "ds_write2_b32 v224, v254, v255 offset0:42 offset1:59\n"
        "s_add_u32 m0, s80, 0x20a00\n"
        "L_pg1_%=:\n"
        "s_cbranch_execz L_pg5_%=\n"
        "s_mul_i32 s46, s46, 0x18000\n"
        "s_mul_i32 s40, s51, 64\n"
        "v_add_u32 v182, s40, v32\n"
        "buffer_load_dwordx4 v197, s[12:15], s46 offen lds\n"
        "s_waitcnt vmcnt(13)\n"
        "s_waitcnt vmcnt(7)\n"
        "s_barrier\n"
        "ds_read_b128 v[94:97], v182\n"
        "v_bfi_b32 v30, s44, v30, v201\n"
        "v_subrev_u32 v201, 2, v200\n"
        "v_cmp_gt_u32 s[38:39], 8, v201\n"
        "ds_read_b128 v[98:101], v182 offset:16896\n"
        "s_nop 0\n"
        "v_cndmask_b32 v28, v202, 0, s[38:39]\n"
        "v_cmp_gt_i32 s[38:39], -8, v201\n"
        "buffer_load_dwordx4 v197, s[12:15], s46 offen offset:1024 lds\n"
        "ds_read_b128 v[102:105], v182 offset:33792\n"
        "v_cndmask_b32 v24, v202, 0, s[38:39]\n"
        "v_cndmask_b32 v31, 0, -1, s[38:39]\n"
        "v_subrev_u32_e64 v201, 3, v200\n"
        "v_cmp_gt_u32 s[38:39], 8, v201\n"
        "ds_read_b128 v[106:109], v182 offset:256\n"
        "s_waitcnt lgkmcnt(3)\n"
        "v_mfma_f32_16x16x32_bf16 v[126:129], v[94:97], v[94:97], 0\n"
        "ds_read_b128 v[110:113], v182 offset:17152\n"
        "v_cndmask_b32 v29, v202, 0, s[38:39]\n"
        "v_mfma_f32_16x16x32_bf16 v[138:141], v[6:9], v[94:97], 0\n"
        "s_waitcnt lgkmcnt(3)\n"
        "ds_read_b128 v[114:117], v182 offset:34048\n"
        "buffer_load_dwordx4 v197, s[12:15], s46 offen offset:2048 lds\n"
        "v_mfma_f32_16x16x32_bf16 v[130:133], v[98:101], v[98:101], 0\n"
        "ds_read_b128 v[118:121], v182 offset:512\n"
        "v_mov_b32 v210, -1\n"
        "v_mfma_f32_16x16x32_bf16 v[142:145], v[6:9], v[98:101], 0\n"
        "s_waitcnt lgkmcnt(4)\n"
        "ds_read_b128 v[122:125], v182 offset:17408\n"
        "v_mov_b32 v211, -1\n"
        "v_mfma_f32_16x16x32_bf16 v[134:137], v[102:105], v[102:105], 0\n"
        "ds_read_b128 v[94:97], v182 offset:34304\n"
        "v_mov_b32 v212, -1\n"
        "v_mfma_f32_16x16x32_bf16 v[146:149], v[6:9], v[102:105], 0\n"
        "s_waitcnt lgkmcnt(5)\n"
        "ds_read_b128 v[98:101], v182 offset:768\n"
        "v_mov_b32 v213, -1\n"
        "v_mfma_f32_16x16x32_bf16 v[126:129], v[106:109], v[106:109], v[126:129]\n"
        "s_lshl4_add_u32 s82, s52, s53\n"
        "ds_read_b128 v[102:105], v182 offset:17664\n"
        "v_cmp_gt_i32 s[38:39], -8, v201\n"
        "v_mfma_f32_16x16x32_bf16 v[138:141], v[6:9], v[106:109], v[138:141]\n"
        "s_waitcnt lgkmcnt(5)\n"
        "ds_read_b128 v[106:109], v182 offset:34560\n"
        "v_cndmask_b32 v25, v202, 0, s[38:39]\n"
        "v_mfma_f32_16x16x32_bf16 v[130:133], v[110:113], v[110:113], v[130:133]\n"
        "s_lshl_b32 s82, s82, 11\n"
        "v_lshrrev_b32_e64 v50, 5, v1\n"
        "v_mul_u32_u24 v50, 0x3e00, v50\n"
        "v_mfma_f32_16x16x32_bf16 v[142:145], v[6:9], v[110:113], v[142:145]\n"
        "s_lshl_b32 s46, s51, 9\n"
        "ds_read_b128 v[110:113], v32\n"
        "v_add3_u32 v50, v50, v4, s46\n"
        "v_mfma_f32_16x16x32_bf16 v[134:137], v[114:117], v[114:117], v[134:137]\n"
        "s_add_u32 s46, s82, 0x40000\n"
        "buffer_store_dwordx4 v[210:213], v50, s[20:23], s46 offen sc1\n"
        "v_cndmask_b32 v201, 0, -1, s[38:39]\n"
        "v_mfma_f32_16x16x32_bf16 v[146:149], v[6:9], v[114:117], v[146:149]\n"
        "s_waitcnt lgkmcnt(3)\n"
        "ds_read_b64_tr_b16 a[0:1], v214 offset:24576\n"
        "v_bfi_b32 v31, s44, v31, v201\n"
        "v_mfma_f32_16x16x32_bf16 v[126:129], v[118:121], v[118:121], v[126:129]\n"
        "s_add_u32 s46, s82, 0x140000\n"
        "ds_read_b64_tr_b16 a[2:3], v214 offset:25088\n"
        "v_lshrrev_b32 v200, 1, v3\n"
        "v_mfma_f32_16x16x32_bf16 v[138:141], v[6:9], v[118:121], v[138:141]\n"
        "ds_read_b64_tr_b16 a[4:5], v214 offset:25600\n"
        "v_and_b32_e64 v201, 1, v3\n"
        "v_mfma_f32_16x16x32_bf16 v[130:133], v[122:125], v[122:125], v[130:133]\n"
        "ds_read_b64_tr_b16 a[6:7], v214 offset:26112\n"
        "buffer_store_dwordx4 v[210:213], v50, s[20:23], s46 offen sc1\n"
        "v_mfma_f32_16x16x32_bf16 v[142:145], v[6:9], v[122:125], v[142:145]\n"
        "ds_read_b64_tr_b16 a[8:9], v214 offset:26624\n"
        "v_lshrrev_b32 v50, 2, v0\n"
        "v_mfma_f32_16x16x32_bf16 v[134:137], v[94:97], v[94:97], v[134:137]\n"
        "ds_read_b64_tr_b16 a[10:11], v214 offset:27136\n"
        "v_lshlrev_b32 v50, 12, v50\n"
        "v_mfma_f32_16x16x32_bf16 v[146:149], v[6:9], v[94:97], v[146:149]\n"
        "ds_read_b128 v[114:117], v32 offset:16896\n"
        "v_and_or_b32 v50, v4, 48, v50\n"
        "v_mfma_f32_16x16x32_bf16 v[126:129], v[98:101], v[98:101], v[126:129]\n"
        "s_add_u32 s46, s34, 0x240000\n"
        "ds_read_b64_tr_b16 a[12:13], v214 offset:36864\n"
        "buffer_store_dwordx4 v[210:213], v50, s[20:23], s46 offen sc1\n"
        "v_mfma_f32_16x16x32_bf16 v[138:141], v[6:9], v[98:101], v[138:141]\n"
        "s_waitcnt lgkmcnt(5)\n"
        "ds_read_b64_tr_b16 a[14:15], v214 offset:37376\n"
        "v_lshlrev_b32 v34, 8, v200\n"
        "v_mfma_f32_16x16x32_bf16 v[130:133], v[102:105], v[102:105], v[130:133]\n"
        "s_add_u32 s46, s34, 0x340000\n"
        "ds_read_b128 v[118:121], v32 offset:33792\n"
        "v_lshl_add_u32 v34, v2, 4, v34\n"
        "v_mfma_f32_16x16x32_bf16 v[142:145], v[6:9], v[102:105], v[142:145]\n"
        "ds_read_b64_tr_b16 a[16:17], v214 offset:37888\n"
        "v_lshl_add_u32 v34, v201, 3, v34\n"
        "v_mfma_f32_16x16x32_bf16 v[134:137], v[106:109], v[106:109], v[134:137]\n"
        "s_lshl_b32 s44, s51, 9\n"
        "ds_read_b64_tr_b16 a[18:19], v214 offset:38400\n"
        "buffer_store_dwordx4 v[210:213], v50, s[20:23], s46 offen sc1\n"
        "v_mfma_f32_16x16x32_bf16 v[146:149], v[6:9], v[106:109], v[146:149]\n"
        "s_add_u32 s46, s35, 0x10000\n"
        "ds_read_b64_tr_b16 a[20:21], v214 offset:38912\n"
        "buffer_store_dword v210, v37, s[20:23], s46 offen sc1\n"
        "v_mfma_f32_16x16x32_bf16 v[10:13], a[0:3], v[6:9], 0\n"
        "s_waitcnt lgkmcnt(5)\n"
        "ds_read_b128 v[122:125], v32 offset:64\n"
        "v_cndmask_b32 v126, v126, v127, s[64:65]\n"
        "v_mfma_f32_16x16x32_bf16 v[62:65], a[4:7], v[110:113], 0\n"
        "s_add_u32 s46, s35, 0x18000\n"
        "ds_read_b64_tr_b16 a[22:23], v214 offset:39424\n"
        "buffer_store_dword v210, v37, s[20:23], s46 offen sc1\n"
        "v_mfma_f32_16x16x32_bf16 v[66:69], v[110:113], a[8:11], 0\n"
        "s_waitcnt vmcnt(9)\n"
        "ds_read_b128 v[94:97], v32 offset:16960\n"
        "v_cndmask_b32 v126, v126, v128, s[66:67]\n"
        "v_mfma_f32_16x16x32_bf16 v[70:73], a[0:3], v[114:117], 0\n"
        "s_barrier\n"
        "ds_read_b64_tr_b16 a[24:25], v203\n"
        "v_cndmask_b32 v139, v126, v129, s[68:69]\n"
        "s_add_u32 s46, s56, 5\n"
        "v_mfma_f32_16x16x32_bf16 v[74:77], a[4:7], v[114:117], 0\n"
        "s_and_b32 s46, s46, 15\n"
        "ds_read_b64_tr_b16 a[26:27], v203 offset:512\n"
        "v_cndmask_b32 v130, v130, v131, s[64:65]\n"
        "v_mfma_f32_16x16x32_bf16 v[78:81], v[114:117], a[8:11], 0\n"
        "s_waitcnt lgkmcnt(5)\n"
        "ds_read_b128 v[98:101], v32 offset:33856\n"
        "v_cndmask_b32 v130, v130, v132, s[66:67]\n"
        "v_mfma_f32_16x16x32_bf16 v[82:85], a[0:3], v[118:121], 0\n"
        "s_add_u32 m0, s80, 0x14a00\n"
        "ds_read_b64_tr_b16 a[28:29], v203 offset:1024\n"
        "v_cndmask_b32 v143, v130, v133, s[68:69]\n"
        "v_mfma_f32_16x16x32_bf16 v[86:89], a[4:7], v[118:121], 0\n"
        "s_mul_i32 s46, s46, 0x18000\n"
        "ds_read_b64_tr_b16 a[30:31], v203 offset:1536\n"
        "buffer_load_dwordx4 v197, s[12:15], s46 offen lds\n"
        "v_mfma_f32_16x16x32_bf16 v[90:93], v[118:121], a[8:11], 0\n"
        "ds_read_b64_tr_b16 a[32:33], v203 offset:2048\n"
        "v_cndmask_b32 v134, v134, v135, s[64:65]\n"
        "v_mfma_f32_16x16x32_bf16 v[10:13], a[12:15], v[6:9], v[10:13]\n"
        "s_waitcnt lgkmcnt(5)\n"
        "ds_read_b128 v[102:105], v32 offset:128\n"
        "v_cndmask_b32 v134, v134, v136, s[66:67]\n"
        "v_mfma_f32_16x16x32_bf16 v[62:65], a[16:19], v[122:125], v[62:65]\n"
        "ds_read_b64_tr_b16 a[34:35], v203 offset:2560\n"
        "v_cndmask_b32 v147, v134, v137, s[68:69]\n"
        "v_mfma_f32_16x16x32_bf16 v[66:69], v[122:125], a[20:23], v[66:69]\n"
        "s_mul_i32 s48, s51, 0x180\n"
        "ds_read_b128 v[106:109], v32 offset:17024\n"
        "buffer_load_dwordx4 v197, s[12:15], s46 offen offset:1024 lds\n"
        "v_mfma_f32_16x16x32_bf16 v[70:73], a[12:15], v[94:97], v[70:73]\n"
        "s_add_u32 s48, s48, 0x27000\n"
        "ds_read_b64_tr_b16 a[36:37], v203 offset:12288\n"
        "v_lshl_add_u32 v184, v2, 3, s48\n"
        "v_mfma_f32_16x16x32_bf16 v[74:77], a[16:19], v[94:97], v[74:77]\n"
        "s_mov_b64 exec, s[70:71]\n"
        "ds_write_b64 v184, v[138:139]\n"
        "ds_write_b64 v184, v[142:143] offset:128\n"
        "ds_write_b64 v184, v[146:147] offset:256\n"
        "s_mov_b64 exec, -1\n"
        "buffer_load_dwordx4 v197, s[12:15], s46 offen offset:2048 lds\n"
        "v_mfma_f32_16x16x32_bf16 v[78:81], v[94:97], a[20:23], v[78:81]\n"
        "s_waitcnt lgkmcnt(5)\n"
        "ds_read_b128 v[110:113], v32 offset:33920\n"
        "v_add_u32 v34, s44, v34\n"
        "v_mfma_f32_16x16x32_bf16 v[82:85], a[12:15], v[98:101], v[82:85]\n"
        "s_mov_b32 s48, 0x27000\n"
        "ds_read_b64_tr_b16 a[38:39], v203 offset:12800\n"
        "v_lshl_add_u32 v184, v1, 3, s48\n"
        "v_mfma_f32_16x16x32_bf16 v[86:89], a[16:19], v[98:101], v[86:89]\n"
        "s_lshl_b32 s48, s51, 9\n"
        "ds_read_b64_tr_b16 a[40:41], v203 offset:13312\n"
        "v_bfe_u32 v200, v1, 2, 3\n"
        "v_mfma_f32_16x16x32_bf16 v[90:93], v[98:101], a[20:23], v[90:93]\n"
        "s_add_u32 s48, s48, 0x27800\n"
        "ds_read_b64_tr_b16 a[42:43], v203 offset:13824\n"
        "v_lshl_add_u32 v51, v1, 2, s48\n"
        "v_mfma_f32_16x16x32_bf16 v[10:13], a[24:27], v[6:9], v[10:13]\n"
        "s_add_u32 s46, s56, 6\n"
        "ds_read_b128 v[114:117], v32 offset:192\n"
        "v_lshl_add_u32 v182, v2, 2, s48\n"
        "v_mfma_f32_16x16x32_bf16 v[62:65], a[28:31], v[102:105], v[62:65]\n"
        "s_and_b32 s46, s46, 15\n"
        "ds_read_b64_tr_b16 a[44:45], v203 offset:14336\n"
        "v_lshl_add_u32 v183, v3, 4, s48\n"
        "v_mfma_f32_16x16x32_bf16 v[66:69], v[102:105], a[32:35], v[66:69]\n"
        "s_waitcnt lgkmcnt(5)\n"
        "ds_read_b128 v[118:121], v32 offset:17088\n"
        "s_barrier\n"
        "v_mfma_f32_16x16x32_bf16 v[70:73], a[24:27], v[106:109], v[70:73]\n"
        "s_add_u32 m0, s80, 0x17a00\n"
        "ds_read_b64_tr_b16 a[46:47], v203 offset:14848\n"
        "s_mul_i32 s46, s46, 0x18000\n"
        "v_mfma_f32_16x16x32_bf16 v[74:77], a[28:31], v[106:109], v[74:77]\n"
        "s_lshl_b32 s44, s51, 1\n"
        "ds_read_b64 v[126:127], v184\n"
        "buffer_load_dwordx4 v197, s[12:15], s46 offen lds\n"
        "v_mfma_f32_16x16x32_bf16 v[78:81], v[106:109], a[32:35], v[78:81]\n"
        "ds_read_b128 v[122:125], v32 offset:33984\n"
        "v_xor_b32 v200, s44, v200\n"
        "v_mfma_f32_16x16x32_bf16 v[82:85], a[24:27], v[110:113], v[82:85]\n"
        "ds_read_b64 v[128:129], v184 offset:384\n"
        "v_lshlrev_b32_e64 v200, 5, v200\n"
        "v_mfma_f32_16x16x32_bf16 v[86:89], a[28:31], v[110:113], v[86:89]\n"
        "ds_read_b64 v[130:131], v184 offset:768\n"
        "v_lshl_add_u32 v200, v3, 11, v200\n"
        "v_mfma_f32_16x16x32_bf16 v[90:93], v[110:113], a[32:35], v[90:93]\n"
        "s_waitcnt lgkmcnt(5)\n"
        "ds_read_b64 v[132:133], v184 offset:1152\n"
        "buffer_load_dwordx4 v197, s[12:15], s46 offen offset:1024 lds\n"
        "v_mfma_f32_16x16x32_bf16 v[10:13], a[36:39], v[6:9], v[10:13]\n"
        "ds_read_b128 v[94:97], v32 offset:256\n"
        "v_bfe_u32 v201, v1, 2, 1\n"
        "v_mfma_f32_16x16x32_bf16 v[62:65], a[40:43], v[114:117], v[62:65]\n"
        "s_waitcnt lgkmcnt(5)\n"
        "ds_read_b64 v[58:59], v35 offset:16896\n"
        "v_lshl_add_u32 v200, v201, 10, v200\n"
        "v_mfma_f32_16x16x32_bf16 v[66:69], v[114:117], a[44:47], v[66:69]\n"
        "ds_read_b128 v[98:101], v32 offset:17152\n"
        "v_bfe_u32 v201, v1, 3, 1\n"
        "v_mfma_f32_16x16x32_bf16 v[70:73], a[36:39], v[118:121], v[70:73]\n"
        "ds_read_b64 v[60:61], v35 offset:16928\n"
        "buffer_load_dwordx4 v197, s[12:15], s46 offen offset:2048 lds\n"
        "v_mfma_f32_16x16x32_bf16 v[74:77], a[40:43], v[118:121], v[74:77]\n"
        "s_waitcnt lgkmcnt(5)\n"
        "ds_read_b128 v[102:105], v32 offset:34048\n"
        "v_add_f32 v126, v126, v128\n"
        "v_mfma_f32_16x16x32_bf16 v[78:81], v[118:121], a[44:47], v[78:81]\n"
        "s_waitcnt lgkmcnt(5)\n"
        "ds_read_b128 v[106:109], v32 offset:320\n"
        "v_add_f32 v130, v130, v132\n"
        "v_mfma_f32_16x16x32_bf16 v[82:85], a[36:39], v[122:125], v[82:85]\n"
        "s_add_u32 s46, s56, 7\n"
        "ds_read_b128 v[110:113], v32 offset:17216\n"
        "v_add_f32 v127, v127, v129\n"
        "v_mfma_f32_16x16x32_bf16 v[86:89], a[40:43], v[122:125], v[86:89]\n"
        "s_and_b32 s46, s46, 15\n"
        "ds_read_b128 v[114:117], v32 offset:34112\n"
        "v_add_f32 v131, v131, v133\n"
        "v_mfma_f32_16x16x32_bf16 v[90:93], v[122:125], a[44:47], v[90:93]\n"
        "s_add_u32 m0, s80, 0x1aa00\n"
        "ds_read_b128 v[118:121], v32 offset:384\n"
        "v_add_f32 v126, v126, v130\n"
        "v_add_f32 v127, v127, v131\n"
        "v_mul_f32 v126, s60, v126\n"
        "v_mul_f32 v127, s60, v127\n"
        "v_fma_f32 v127, -v126, v126, v127\n"
        "v_max_f32 v127, 0, v127\n"
        "v_add_f32 v127, s61, v127\n"
        "v_rsq_f32 v127, v127\n"
        "v_lshl_add_u32 v200, v201, 8, v200\n"
        "v_mul_f32 v126, -v126, v127\n"
        "v_and_b32 v201, 3, v1\n"
        "v_lshl_add_u32 v40, v201, 3, v200\n"
        "v_xor_b32 v42, 32, v40\n"
        "v_add_u32 v41, 0x14a00, v40\n"
        "v_add_u32 v43, 0x14a00, v42\n"
        "s_mul_i32 s46, s46, 0x18000\n"
        "s_mul_i32 s32, s52, 0xa000\n"
        "ds_write2_b32 v51, v127, v126 offset1:64\n"
        "ds_read2_b32 v[134:135], v182 offset0:0 offset1:64\n"
        "s_cmp_eq_u32 s54, 0\n"
        "s_cselect_b32 s44, 0x14000, 0\n"
        "s_add_u32 s33, s32, s44\n"
        "ds_read2_b32 v[136:137], v182 offset0:16 offset1:80\n"
        "s_lshl1_add_u32 s45, s52, s57\n"
        "s_lshl_b32 s45, s45, 14\n"
        "s_lshl2_add_u32 s44, s51, s56\n"
        "ds_read2_b32 v[130:131], v182 offset0:32 offset1:96\n"
        "s_add_u32 s44, s44, 0\n"
        "s_and_b32 s44, s44, 15\n"
        "s_lshl_b32 s44, s44, 10\n"
        "ds_read_b128 v[150:153], v183 offset:256\n"
        "s_add_u32 s83, s44, s45\n"
        "s_lshl1_add_u32 s28, s44, s32\n"
        "s_lshl2_add_u32 s44, s51, s56\n"
        "ds_read_b128 v[138:141], v183\n"
        "s_add_u32 s44, s44, 1\n"
        "s_and_b32 s44, s44, 15\n"
        "s_lshl_b32 s44, s44, 10\n"
        "buffer_load_dwordx4 v197, s[12:15], s46 offen lds\n"
        "ds_read_b128 v[154:157], v183 offset:320\n"
        "s_add_u32 s84, s44, s45\n"
        "s_lshl1_add_u32 s29, s44, s32\n"
        "s_lshl2_add_u32 s44, s51, s56\n"
        "ds_read_b128 v[142:145], v183 offset:64\n"
        "s_add_u32 s44, s44, 2\n"
        "s_and_b32 s44, s44, 15\n"
        "s_lshl_b32 s44, s44, 10\n"
        "ds_read_b128 v[158:161], v183 offset:384\n"
        "s_add_u32 s85, s44, s45\n"
        "s_lshl1_add_u32 s30, s44, s32\n"
        "s_lshl2_add_u32 s44, s51, s56\n"
        "ds_read_b128 v[146:149], v183 offset:128\n"
        "s_mov_b32 s48, 0x3eb8aa3b\n"
        "s_add_u32 s44, s44, 3\n"
        "s_and_b32 s44, s44, 15\n"
        "buffer_load_dwordx4 v197, s[12:15], s46 offen offset:1024 lds\n"
        "ds_read_b128 v[122:125], v32 offset:17280\n"
        "s_lshl_b32 s44, s44, 10\n"
        "s_add_u32 s86, s44, s45\n"
        "s_lshl1_add_u32 s31, s44, s32\n"
        "s_mov_b32 s59, 0x20000\n"
        "s_mov_b32 s76, 0\n"
        "s_lshl_b32 s72, s73, 16\n"
        "s_and_b32 s73, s73, 0xffff0000\n"
        "s_waitcnt lgkmcnt(3)\n"
        "v_mul_f32 v126, s48, v136\n"
        "v_mul_f32 v127, s48, v137\n"
        "v_mul_f32 v128, s48, v130\n"
        "v_mul_f32_e64 v129, s48, v131\n"
        "buffer_load_dwordx4 v197, s[12:15], s46 offen offset:2048 lds\n"
        "s_waitcnt vmcnt(6)\n"
        "s_barrier\n"
        "s_add_u32 s46, s56, 8\n"
        "s_and_b32 s46, s46, 15\n"
        "s_add_u32 m0, s80, 0x1da00\n"
        "s_mul_i32 s46, s46, 0x18000\n"
        "ds_read_b64_tr_b16 a[48:49], v214 offset:49152\n"
        "buffer_load_dwordx4 v197, s[12:15], s46 offen lds\n"
        "ds_read_b64_tr_b16 a[50:51], v214 offset:49664\n"
        "ds_read_b64_tr_b16 a[52:53], v214 offset:50176\n"
        "ds_read_b64_tr_b16 a[54:55], v214 offset:50688\n"
        "ds_read_b64_tr_b16 a[56:57], v214 offset:51200\n"
        "ds_read_b64_tr_b16 a[58:59], v214 offset:51712\n"
        "v_readfirstlane_b32 s62, v208\n"
        "v_readfirstlane_b32 s63, v209\n"
        "ds_read_b64_tr_b16 a[60:61], v214\n"
        "buffer_load_dwordx4 v197, s[12:15], s46 offen offset:1024 lds\n"
        "ds_read_b64_tr_b16 a[62:63], v214 offset:512\n"
        "ds_read_b64_tr_b16 a[64:65], v214 offset:1024\n"
        "ds_read_b64_tr_b16 a[66:67], v214 offset:1536\n"
        "s_waitcnt lgkmcnt(8)\n"
        "v_mfma_f32_16x16x32_bf16 v[10:13], a[48:51], v[6:9], v[10:13]\n"
        "s_add_u32 s62, s62, 1\n"
        "s_addc_u32 s63, s63, 0\n"
        "s_waitcnt lgkmcnt(6)\n"
        "v_mfma_f32_16x16x32_bf16 v[62:65], a[52:55], v[94:97], v[62:65]\n"
        "ds_read_b64_tr_b16 a[68:69], v214 offset:2048\n"
        "s_waitcnt lgkmcnt(5)\n"
        "v_mfma_f32_16x16x32_bf16 v[66:69], v[94:97], a[56:59], v[66:69]\n"
        "ds_read_b64_tr_b16 a[70:71], v214 offset:2560\n"
        "buffer_load_dwordx4 v197, s[12:15], s46 offen offset:2048 lds\n"
        "v_mfma_f32_16x16x32_bf16 v[70:73], a[48:51], v[98:101], v[70:73]\n"
        "s_add_u32 s46, s56, 9\n"
        "ds_read_b128 v[94:97], v32 offset:34176\n"
        "v_mov_b32 v48, s62\n"
        "v_mfma_f32_16x16x32_bf16 v[74:77], a[52:55], v[98:101], v[74:77]\n"
        "s_and_b32 s46, s46, 15\n"
        "v_mov_b32 v49, s63\n"
        "v_lshlrev_b32 v50, 24, v0\n"
        "v_mfma_f32_16x16x32_bf16 v[78:81], v[98:101], a[56:59], v[78:81]\n"
        "s_add_u32 m0, s80, 0xc600\n"
        "ds_read_b128 v[98:101], v32 offset:448\n"
        "s_mul_i32 s46, s46, 0x18000\n"
        "v_mfma_f32_16x16x32_bf16 v[82:85], a[48:51], v[102:105], v[82:85]\n"
        "s_lshl3_add_u32 s96, s52, s53\n"
        "s_lshl_b32 s96, s96, 6\n"
        "buffer_load_dwordx4 v197, s[12:15], s46 offen lds\n"
        "v_mfma_f32_16x16x32_bf16 v[86:89], a[52:55], v[102:105], v[86:89]\n"
        "s_add_u32 s96, s96, 0x1000\n"
        "v_mfma_f32_16x16x32_bf16 v[90:93], v[102:105], a[56:59], v[90:93]\n"
        "s_waitcnt lgkmcnt(2)\n"
        "ds_read_b128 v[102:105], v32 offset:17344\n"
        "v_mfma_f32_16x16x32_bf16 v[10:13], a[60:63], v[6:9], v[10:13]\n"
        "v_mfma_f32_16x16x32_bf16 v[62:65], a[64:67], v[106:109], v[62:65]\n"
        "buffer_load_dwordx4 v197, s[12:15], s46 offen offset:1024 lds\n"
        "v_mfma_f32_16x16x32_bf16 v[66:69], v[106:109], a[68:71], v[66:69]\n"
        "ds_read_b128 v[106:109], v32 offset:34240\n"
        "v_mfma_f32_16x16x32_bf16 v[70:73], a[60:63], v[110:113], v[70:73]\n"
        "v_mfma_f32_16x16x32_bf16 v[74:77], a[64:67], v[110:113], v[74:77]\n"
        "v_mfma_f32_16x16x32_bf16 v[78:81], v[110:113], a[68:71], v[78:81]\n"
        "ds_read_b128 v[110:113], v32 offset:512\n"
        "buffer_load_dwordx4 v197, s[12:15], s46 offen offset:2048 lds\n"
        "v_mfma_f32_16x16x32_bf16 v[82:85], a[60:63], v[114:117], v[82:85]\n"
        "v_mfma_f32_16x16x32_bf16 v[86:89], a[64:67], v[114:117], v[86:89]\n"
        "s_waitcnt vmcnt(6)\n"
        "s_barrier\n"
        "v_mfma_f32_16x16x32_bf16 v[90:93], v[114:117], a[68:71], v[90:93]\n"
        "s_add_u32 s46, s56, 10\n"
        "ds_read_b64_tr_b16 a[72:73], v214 offset:12288\n"
        "s_and_b32 s46, s46, 15\n"
        "ds_read_b64_tr_b16 a[74:75], v214 offset:12800\n"
        "s_add_u32 m0, s80, 0xf600\n"
        "ds_read_b64_tr_b16 a[76:77], v214 offset:13312\n"
        "s_mul_i32 s46, s46, 0x18000\n"
        "ds_read_b64_tr_b16 a[78:79], v214 offset:13824\n"
        "buffer_load_dwordx4 v197, s[12:15], s46 offen lds\n"
        "ds_read_b64_tr_b16 a[80:81], v214 offset:14336\n"
        "ds_read_b64_tr_b16 a[82:83], v214 offset:14848\n"
        "ds_read_b64_tr_b16 a[84:85], v214 offset:24576\n"
        "ds_read_b64_tr_b16 a[86:87], v214 offset:25088\n"
        "ds_read_b64_tr_b16 a[88:89], v214 offset:25600\n"
        "ds_read_b64_tr_b16 a[90:91], v214 offset:26112\n"
        "ds_read_b64_tr_b16 a[92:93], v214 offset:26624\n"
        "buffer_load_dwordx4 v197, s[12:15], s46 offen offset:1024 lds\n"
        "ds_read_b64_tr_b16 a[94:95], v214 offset:27136\n"
        "s_waitcnt lgkmcnt(10)\n"
        "v_mfma_f32_16x16x32_bf16 v[10:13], a[72:75], v[6:9], v[10:13]\n"
        "ds_read_b128 v[114:117], v32 offset:17408\n"
        "s_waitcnt lgkmcnt(9)\n"
        "v_mfma_f32_16x16x32_bf16 v[62:65], a[76:79], v[118:121], v[62:65]\n"
        "s_waitcnt lgkmcnt(7)\n"
        "v_mfma_f32_16x16x32_bf16 v[66:69], v[118:121], a[80:83], v[66:69]\n"
        "ds_read_b128 v[118:121], v32 offset:34304\n"
        "v_mfma_f32_16x16x32_bf16 v[70:73], a[72:75], v[122:125], v[70:73]\n"
        "buffer_load_dwordx4 v197, s[12:15], s46 offen offset:2048 lds\n"
        "s_add_u32 s46, s56, 11\n"
        "v_mfma_f32_16x16x32_bf16 v[74:77], a[76:79], v[122:125], v[74:77]\n"
        "s_and_b32 s46, s46, 15\n"
        "s_add_u32 m0, s80, 0x20a00\n"
        "s_mul_i32 s46, s46, 0x18000\n"
        "v_mfma_f32_16x16x32_bf16 v[78:81], v[122:125], a[80:83], v[78:81]\n"
        "ds_read_b128 v[122:125], v32 offset:576\n"
        "v_mfma_f32_16x16x32_bf16 v[82:85], a[72:75], v[94:97], v[82:85]\n"
        "v_mfma_f32_16x16x32_bf16 v[86:89], a[76:79], v[94:97], v[86:89]\n"
        "buffer_load_dwordx4 v197, s[12:15], s46 offen lds\n"
        "v_mfma_f32_16x16x32_bf16 v[90:93], v[94:97], a[80:83], v[90:93]\n"
        "s_waitcnt lgkmcnt(1)\n"
        "ds_read_b128 v[94:97], v32 offset:17472\n"
        "v_mfma_f32_16x16x32_bf16 v[10:13], a[84:87], v[6:9], v[10:13]\n"
        "v_mfma_f32_16x16x32_bf16 v[62:65], a[88:91], v[98:101], v[62:65]\n"
        "v_mfma_f32_16x16x32_bf16 v[66:69], v[98:101], a[92:95], v[66:69]\n"
        "ds_read_b128 v[98:101], v32 offset:34368\n"
        "buffer_load_dwordx4 v197, s[12:15], s46 offen offset:1024 lds\n"
        "v_mfma_f32_16x16x32_bf16 v[70:73], a[84:87], v[102:105], v[70:73]\n"
        "v_mfma_f32_16x16x32_bf16 v[74:77], a[88:91], v[102:105], v[74:77]\n"
        "v_mfma_f32_16x16x32_bf16 v[78:81], v[102:105], a[92:95], v[78:81]\n"
        "ds_read_b128 v[102:105], v32 offset:640\n"
        "v_mfma_f32_16x16x32_bf16 v[82:85], a[84:87], v[106:109], v[82:85]\n"
        "buffer_load_dwordx4 v197, s[12:15], s46 offen offset:2048 lds\n"
        "v_mfma_f32_16x16x32_bf16 v[86:89], a[88:91], v[106:109], v[86:89]\n"
        "v_mfma_f32_16x16x32_bf16 v[90:93], v[106:109], a[92:95], v[90:93]\n"
        "s_waitcnt vmcnt(6)\n"
        "s_barrier\n"
        "ds_read_b64_tr_b16 a[96:97], v214 offset:36864\n"
        "s_add_u32 s46, s56, 12\n"
        "ds_read_b64_tr_b16 a[98:99], v214 offset:37376\n"
        "s_and_b32 s46, s46, 15\n"
        "ds_read_b64_tr_b16 a[100:101], v214 offset:37888\n"
        "s_add_u32 m0, s80, 0x14a00\n"
        "ds_read_b64_tr_b16 a[102:103], v214 offset:38400\n"
        "s_mul_i32 s46, s46, 0x18000\n"
        "ds_read_b64_tr_b16 a[104:105], v214 offset:38912\n"
        "buffer_load_dwordx4 v197, s[12:15], s46 offen lds\n"
        "ds_read_b64_tr_b16 a[106:107], v214 offset:39424\n"
        "ds_read_b64_tr_b16 a[108:109], v203\n"
        "ds_read_b64_tr_b16 a[110:111], v203 offset:512\n"
        "ds_read_b64_tr_b16 a[112:113], v203 offset:1024\n"
        "ds_read_b64_tr_b16 a[114:115], v203 offset:1536\n"
        "ds_read_b64_tr_b16 a[116:117], v203 offset:2048\n"
        "ds_read_b64_tr_b16 a[118:119], v203 offset:2560\n"
        "s_waitcnt lgkmcnt(10)\n"
        "buffer_load_dwordx4 v197, s[12:15], s46 offen offset:1024 lds\n"
        "v_mfma_f32_16x16x32_bf16 v[10:13], a[96:99], v[6:9], v[10:13]\n"
        "ds_read_b128 v[106:109], v32 offset:17536\n"
        "s_waitcnt lgkmcnt(9)\n"
        "v_mfma_f32_16x16x32_bf16 v[62:65], a[100:103], v[110:113], v[62:65]\n"
        "s_waitcnt lgkmcnt(7)\n"
        "v_mfma_f32_16x16x32_bf16 v[66:69], v[110:113], a[104:107], v[66:69]\n"
        "ds_read_b128 v[110:113], v32 offset:34432\n"
        "v_mfma_f32_16x16x32_bf16 v[70:73], a[96:99], v[114:117], v[70:73]\n"
        "buffer_load_dwordx4 v197, s[12:15], s46 offen offset:2048 lds\n"
        "s_add_u32 s46, s56, 13\n"
        "v_mfma_f32_16x16x32_bf16 v[74:77], a[100:103], v[114:117], v[74:77]\n"
        "s_and_b32 s46, s46, 15\n"
        "s_add_u32 m0, s80, 0x17a00\n"
        "s_mul_i32 s46, s46, 0x18000\n"
        "v_mfma_f32_16x16x32_bf16 v[78:81], v[114:117], a[104:107], v[78:81]\n"
        "ds_read_b128 v[114:117], v32 offset:704\n"
        "v_mfma_f32_16x16x32_bf16 v[82:85], a[96:99], v[118:121], v[82:85]\n"
        "v_mfma_f32_16x16x32_bf16 v[86:89], a[100:103], v[118:121], v[86:89]\n"
        "buffer_load_dwordx4 v197, s[12:15], s46 offen lds\n"
        "v_mfma_f32_16x16x32_bf16 v[90:93], v[118:121], a[104:107], v[90:93]\n"
        "s_waitcnt lgkmcnt(1)\n"
        "ds_read_b128 v[118:121], v32 offset:17600\n"
        "v_mfma_f32_16x16x32_bf16 v[10:13], a[108:111], v[6:9], v[10:13]\n"
        "v_mfma_f32_16x16x32_bf16 v[62:65], a[112:115], v[122:125], v[62:65]\n"
        "v_mfma_f32_16x16x32_bf16 v[66:69], v[122:125], a[116:119], v[66:69]\n"
        "ds_read_b128 v[122:125], v32 offset:34496\n"
        "buffer_load_dwordx4 v197, s[12:15], s46 offen offset:1024 lds\n"
        "v_mfma_f32_16x16x32_bf16 v[70:73], a[108:111], v[94:97], v[70:73]\n"
        "v_mfma_f32_16x16x32_bf16 v[74:77], a[112:115], v[94:97], v[74:77]\n"
        "v_mfma_f32_16x16x32_bf16 v[78:81], v[94:97], a[116:119], v[78:81]\n"
        "ds_read_b128 v[94:97], v32 offset:768\n"
        "v_mfma_f32_16x16x32_bf16 v[82:85], a[108:111], v[98:101], v[82:85]\n"
        "buffer_load_dwordx4 v197, s[12:15], s46 offen offset:2048 lds\n"
        "v_mfma_f32_16x16x32_bf16 v[86:89], a[112:115], v[98:101], v[86:89]\n"
        "v_mfma_f32_16x16x32_bf16 v[90:93], v[98:101], a[116:119], v[90:93]\n"
        "s_waitcnt vmcnt(6)\n"
        "s_barrier\n"
        "ds_read_b64_tr_b16 a[120:121], v203 offset:12288\n"
        "s_add_u32 s46, s56, 14\n"
        "ds_read_b64_tr_b16 a[122:123], v203 offset:12800\n"
        "s_and_b32 s46, s46, 15\n"
        "ds_read_b64_tr_b16 a[124:125], v203 offset:13312\n"
        "s_add_u32 m0, s80, 0x1aa00\n"
        "ds_read_b64_tr_b16 a[126:127], v203 offset:13824\n"
        "s_mul_i32 s46, s46, 0x18000\n"
        "ds_read_b64_tr_b16 a[128:129], v203 offset:14336\n"
        "buffer_load_dwordx4 v197, s[12:15], s46 offen lds\n"
        "ds_read_b64_tr_b16 a[130:131], v203 offset:14848\n"
        "ds_read_b64_tr_b16 a[132:133], v214 offset:49152\n"
        "ds_read_b64_tr_b16 a[134:135], v214 offset:49664\n"
        "ds_read_b64_tr_b16 a[136:137], v214 offset:50176\n"
        "ds_read_b64_tr_b16 a[138:139], v214 offset:50688\n"
        "ds_read_b64_tr_b16 a[140:141], v214 offset:51200\n"
        "ds_read_b64_tr_b16 a[142:143], v214 offset:51712\n"
        "s_waitcnt lgkmcnt(10)\n"
        "buffer_load_dwordx4 v197, s[12:15], s46 offen offset:1024 lds\n"
        "v_mfma_f32_16x16x32_bf16 v[10:13], a[120:123], v[6:9], v[10:13]\n"
        "ds_read_b128 v[98:101], v32 offset:17664\n"
        "s_waitcnt lgkmcnt(9)\n"
        "v_mfma_f32_16x16x32_bf16 v[62:65], a[124:127], v[102:105], v[62:65]\n"
        "s_waitcnt lgkmcnt(7)\n"
        "v_mfma_f32_16x16x32_bf16 v[66:69], v[102:105], a[128:131], v[66:69]\n"
        "ds_read_b128 v[102:105], v32 offset:832\n"
        "v_mfma_f32_16x16x32_bf16 v[70:73], a[120:123], v[106:109], v[70:73]\n"
        "buffer_load_dwordx4 v197, s[12:15], s46 offen offset:2048 lds\n"
        "s_add_u32 s46, s56, 15\n"
        "v_mfma_f32_16x16x32_bf16 v[74:77], a[124:127], v[106:109], v[74:77]\n"
        "s_and_b32 s46, s46, 15\n"
        "s_add_u32 m0, s80, 0x1da00\n"
        "s_mul_i32 s46, s46, 0x18000\n"
        "v_mfma_f32_16x16x32_bf16 v[78:81], v[106:109], a[128:131], v[78:81]\n"
        "ds_read_b128 v[106:109], v32 offset:17728\n"
        "v_mfma_f32_16x16x32_bf16 v[82:85], a[120:123], v[110:113], v[82:85]\n"
        "v_mfma_f32_16x16x32_bf16 v[86:89], a[124:127], v[110:113], v[86:89]\n"
        "buffer_load_dwordx4 v197, s[12:15], s46 offen lds\n"
        "v_mfma_f32_16x16x32_bf16 v[90:93], v[110:113], a[128:131], v[90:93]\n"
        "s_waitcnt lgkmcnt(1)\n"
        "ds_read_b128 v[110:113], v32 offset:896\n"
        "v_mfma_f32_16x16x32_bf16 v[10:13], a[132:135], v[6:9], v[10:13]\n"
        "v_mfma_f32_16x16x32_bf16 v[62:65], a[136:139], v[114:117], v[62:65]\n"
        "v_mfma_f32_16x16x32_bf16 v[66:69], v[114:117], a[140:143], v[66:69]\n"
        "ds_read_b128 v[114:117], v32 offset:17792\n"
        "buffer_load_dwordx4 v197, s[12:15], s46 offen offset:1024 lds\n"
        "v_mfma_f32_16x16x32_bf16 v[70:73], a[132:135], v[118:121], v[70:73]\n"
        "v_mfma_f32_16x16x32_bf16 v[74:77], a[136:139], v[118:121], v[74:77]\n"
        "v_mfma_f32_16x16x32_bf16 v[78:81], v[118:121], a[140:143], v[78:81]\n"
        "ds_read_b128 v[118:121], v32 offset:960\n"
        "v_mfma_f32_16x16x32_bf16 v[82:85], a[132:135], v[122:125], v[82:85]\n"
        "buffer_load_dwordx4 v197, s[12:15], s46 offen offset:2048 lds\n"
        "s_add_u32 m0, s78, 0x12900\n"
        "v_mfma_f32_16x16x32_bf16 v[86:89], a[136:139], v[122:125], v[86:89]\n"
        "s_add_i32 s46, s77, 0x10c000\n"
        "v_mfma_f32_16x16x32_bf16 v[90:93], v[122:125], a[140:143], v[90:93]\n"
        "s_waitcnt vmcnt(6)\n"
        "s_barrier\n"
        "ds_read_b64_tr_b16 a[144:145], v214\n"
        "ds_read_b64_tr_b16 a[146:147], v214 offset:512\n"
        "ds_read_b64_tr_b16 a[148:149], v214 offset:1024\n"
        "ds_read_b64_tr_b16 a[150:151], v214 offset:1536\n"
        "buffer_load_dwordx4 v196, s[8:11], s46 offen lds\n"
        "ds_read_b64_tr_b16 a[152:153], v214 offset:2048\n"
        "s_add_u32 m0, s78, 0x13980\n"
        "ds_read_b64_tr_b16 a[154:155], v214 offset:2560\n"
        "s_add_i32 s46, s77, 0x10e000\n"
        "ds_read_b64_tr_b16 a[156:157], v214 offset:12288\n"
        "ds_read_b64_tr_b16 a[158:159], v214 offset:12800\n"
        "ds_read_b64_tr_b16 a[160:161], v214 offset:13312\n"
        "ds_read_b64_tr_b16 a[162:163], v214 offset:13824\n"
        "ds_read_b64_tr_b16 a[164:165], v214 offset:14336\n"
        "buffer_load_dwordx4 v196, s[8:11], s46 offen lds\n"
        "ds_read_b64_tr_b16 a[166:167], v214 offset:14848\n"
        "s_waitcnt lgkmcnt(10)\n"
        "v_mfma_f32_16x16x32_bf16 v[10:13], a[144:147], v[6:9], v[10:13]\n"
        "s_add_u32 m0, s78, 0xc600\n"
        "ds_read_b128 v[122:125], v32 offset:17856\n"
        "s_waitcnt lgkmcnt(9)\n"
        "v_mfma_f32_16x16x32_bf16 v[62:65], a[148:151], v[94:97], v[62:65]\n"
        "s_add_i32 s46, s77, 0x100000\n"
        "s_waitcnt lgkmcnt(7)\n"
        "v_mfma_f32_16x16x32_bf16 v[66:69], v[94:97], a[152:155], v[66:69]\n"
        "ds_read_b128 v[94:97], v32 offset:34560\n"
        "v_mfma_f32_16x16x32_bf16 v[70:73], a[144:147], v[98:101], v[70:73]\n"
        "buffer_load_dwordx4 v196, s[8:11], s46 offen lds\n"
        "s_add_u32 m0, s78, 0xd680\n"
        "v_mfma_f32_16x16x32_bf16 v[74:77], a[148:151], v[98:101], v[74:77]\n"
        "L_pg2_%=:\n"
        "s_cbranch_execz L_pg6_%=\n"
        "s_add_i32 s46, s77, 0x102000\n"
        "v_mfma_f32_16x16x32_bf16 v[78:81], v[98:101], a[152:155], v[78:81]\n"
        "s_waitcnt lgkmcnt(2)\n"
        "ds_read_b128 v[98:101], v32 offset:34624\n"
        "v_mfma_f32_16x16x32_bf16 v[10:13], a[156:159], v[6:9], v[10:13]\n"
        "v_mfma_f32_16x16x32_bf16 v[62:65], a[160:163], v[102:105], v[62:65]\n"
        "buffer_load_dwordx4 v196, s[8:11], s46 offen lds\n"
        "s_add_u32 m0, s78, 0xe700\n"
        "v_mfma_f32_16x16x32_bf16 v[66:69], v[102:105], a[164:167], v[66:69]\n"
        "s_add_i32 s46, s77, 0x104000\n"
        "ds_read_b128 v[102:105], v32 offset:34688\n"
        "v_mfma_f32_16x16x32_bf16 v[70:73], a[156:159], v[106:109], v[70:73]\n"
        "v_mfma_f32_16x16x32_bf16 v[74:77], a[160:163], v[106:109], v[74:77]\n"
        "v_mfma_f32_16x16x32_bf16 v[78:81], v[106:109], a[164:167], v[78:81]\n"
        "ds_read_b128 v[106:109], v32 offset:34752\n"
        "buffer_load_dwordx4 v196, s[8:11], s46 offen lds\n"
        "s_add_u32 m0, s78, 0xf780\n"
        "s_add_i32 s46, s77, 0x106000\n"
        "buffer_load_dwordx4 v196, s[8:11], s46 offen lds\n"
        "s_add_u32 m0, s78, 0x10800\n"
        "s_add_i32 s46, s77, 0x108000\n"
        "buffer_load_dwordx4 v196, s[8:11], s46 offen lds\n"
        "s_add_u32 m0, s78, 0x11880\n"
        "s_add_i32 s46, s77, 0x10a000\n"
        "s_waitcnt vmcnt(7)\n"
        "buffer_load_dwordx4 v196, s[8:11], s46 offen lds\n"
        "s_barrier\n"
        "ds_read_b64_tr_b16 a[168:169], v214 offset:24576\n"
        "buffer_store_dwordx2 v[48:49], v50, s[20:23], s96 offen sc1\n"
        "ds_read_b64_tr_b16 a[170:171], v214 offset:25088\n"
        "s_add_u32 s46, s56, 0\n"
        "ds_read_b64_tr_b16 a[172:173], v214 offset:25600\n"
        "s_and_b32 s46, s46, 15\n"
        "ds_read_b64_tr_b16 a[174:175], v214 offset:26112\n"
        "s_add_u32 m0, s79, 0x20a00\n"
        "ds_read_b64_tr_b16 a[176:177], v214 offset:26624\n"
        "s_lshl_b32 s46, s46, 15\n"
        "ds_read_b64_tr_b16 a[178:179], v214 offset:27136\n"
        "ds_read_b64_tr_b16 a[180:181], v214 offset:36864\n"
        "buffer_load_dwordx4 v38, s[16:19], s46 offen lds\n"
        "ds_read_b64_tr_b16 a[182:183], v214 offset:37376\n"
        "ds_read_b64_tr_b16 a[184:185], v214 offset:37888\n"
        "ds_read_b64_tr_b16 a[186:187], v214 offset:38400\n"
        "ds_read_b64_tr_b16 a[188:189], v214 offset:38912\n"
        "ds_read_b64_tr_b16 a[190:191], v214 offset:39424\n"
        "s_waitcnt lgkmcnt(10)\n"
        "v_mfma_f32_16x16x32_bf16 v[10:13], a[168:171], v[6:9], v[10:13]\n"
        "s_waitcnt lgkmcnt(8)\n"
        "v_mfma_f32_16x16x32_bf16 v[62:65], a[172:175], v[110:113], v[62:65]\n"
        "buffer_load_dwordx4 v39, s[16:19], s46 offen offset:1024 lds\n"
        "s_waitcnt lgkmcnt(6)\n"
        "v_mfma_f32_16x16x32_bf16 v[66:69], v[110:113], a[176:179], v[66:69]\n"
        "s_add_u32 s46, s56, 1\n"
        "s_and_b32 s46, s46, 15\n"
        "s_add_u32 m0, s79, 0x14a00\n"
        "v_mfma_f32_16x16x32_bf16 v[70:73], a[168:171], v[114:117], v[70:73]\n"
        "s_lshl_b32 s46, s46, 15\n"
        "v_mfma_f32_16x16x32_bf16 v[74:77], a[172:175], v[114:117], v[74:77]\n"
        "s_waitcnt lgkmcnt(0)\n"
        "v_mfma_f32_16x16x32_bf16 v[78:81], v[114:117], a[176:179], v[78:81]\n"
        "s_barrier\n"
        "buffer_load_dwordx4 v38, s[16:19], s46 offen lds\n"
        "v_mfma_f32_16x16x32_bf16 v[10:13], a[180:183], v[6:9], v[10:13]\n"
        "v_mfma_f32_16x16x32_bf16 v[62:65], a[184:187], v[118:121], v[62:65]\n"
        "v_mfma_f32_16x16x32_bf16 v[66:69], v[118:121], a[188:191], v[66:69]\n"
        "v_mfma_f32_16x16x32_bf16 v[70:73], a[180:183], v[122:125], v[70:73]\n"
        "buffer_load_dwordx4 v39, s[16:19], s46 offen offset:1024 lds\n"
        "s_add_u32 s46, s56, 2\n"
        "v_mfma_f32_16x16x32_bf16 v[74:77], a[184:187], v[122:125], v[74:77]\n"
        "s_and_b32 s46, s46, 15\n"
        "s_add_u32 m0, s79, 0x16a00\n"
        "s_lshl_b32 s46, s46, 15\n"
        "v_mfma_f32_16x16x32_bf16 v[78:81], v[122:125], a[188:191], v[78:81]\n"
        "s_barrier\n"
        "v_mov_b32 v18, v66\n"
        "v_mov_b32 v19, v66\n"
        "v_mfma_f32_16x16x32_bf16 v[82:85], a[144:147], v[94:97], v[82:85]\n"
        "v_mov_b32_dpp v14, v62 row_newbcast:0 row_mask:0xf bank_mask:0xf\n"
        "v_mov_b32_dpp v15, v63 row_newbcast:0 row_mask:0xf bank_mask:0xf\n"
        "v_mfma_f32_16x16x32_bf16 v[86:89], a[148:151], v[94:97], v[86:89]\n"
        "v_permlane16_swap_b32 v18, v19\n"
        "v_mfma_f32_16x16x32_bf16 v[90:93], v[94:97], a[152:155], v[90:93]\n"
        "buffer_load_dwordx4 v38, s[16:19], s46 offen lds\n"
        "v_mov_b32 v19, v18\n"
        "v_mfma_f32_16x16x32_bf16 v[82:85], a[156:159], v[98:101], v[82:85]\n"
        "v_mov_b32_dpp v16, v64 row_newbcast:0 row_mask:0xf bank_mask:0xf\n"
        "v_mov_b32_dpp v17, v65 row_newbcast:0 row_mask:0xf bank_mask:0xf\n"
        "v_mfma_f32_16x16x32_bf16 v[86:89], a[160:163], v[98:101], v[86:89]\n"
        "v_permlane32_swap_b32 v18, v19\n"
        "v_mfma_f32_16x16x32_bf16 v[90:93], v[98:101], a[164:167], v[90:93]\n"
        "v_mul_f32 v170, v135, v14\n"
        "v_mul_f32 v171, v135, v15\n"
        "v_mfma_f32_16x16x32_bf16 v[82:85], a[168:171], v[102:105], v[82:85]\n"
        "buffer_load_dwordx4 v39, s[16:19], s46 offen offset:1024 lds\n"
        "v_mul_f32 v172, v135, v16\n"
        "v_mfma_f32_16x16x32_bf16 v[86:89], a[172:175], v[102:105], v[86:89]\n"
        "s_add_u32 s46, s56, 3\n"
        "v_mul_f32 v173, v135, v17\n"
        "v_mul_f32 v150, v150, v18\n"
        "v_mfma_f32_16x16x32_bf16 v[90:93], v[102:105], a[176:179], v[90:93]\n"
        "s_and_b32 s46, s46, 15\n"
        "v_mul_f32 v151, v151, v18\n"
        "v_mul_f32 v152, v152, v18\n"
        "v_mfma_f32_16x16x32_bf16 v[82:85], a[180:183], v[106:109], v[82:85]\n"
        "s_add_u32 m0, s79, 0x18a00\n"
        "v_mul_f32 v153, v153, v18\n"
        "v_mul_f32 v174, v137, v14\n"
        "v_mfma_f32_16x16x32_bf16 v[86:89], a[184:187], v[106:109], v[86:89]\n"
        "s_lshl_b32 s46, s46, 15\n"
        "buffer_load_dwordx4 v38, s[16:19], s46 offen lds\n"
        "v_mul_f32 v175, v137, v15\n"
        "v_mfma_f32_16x16x32_bf16 v[90:93], v[106:109], a[188:191], v[90:93]\n"
        "v_mul_f32 v176, v137, v16\n"
        "v_mul_f32 v177, v137, v17\n"
        "v_mul_f32 v162, v127, v10\n"
        "v_mul_f32 v163, v127, v11\n"
        "v_mul_f32 v164, v127, v12\n"
        "v_mul_f32 v165, v127, v13\n"
        "v_mul_f32 v154, v154, v18\n"
        "v_mul_f32 v155, v155, v18\n"
        "v_mul_f32 v156, v156, v18\n"
        "v_mul_f32 v157, v157, v18\n"
        "buffer_load_dwordx4 v39, s[16:19], s46 offen offset:1024 lds\n"
        "v_mul_f32 v178, v131, v14\n"
        "v_mul_f32 v179, v131, v15\n"
        "v_mul_f32 v180, v131, v16\n"
        "v_mul_f32 v181, v131, v17\n"
        "v_mul_f32 v166, v129, v10\n"
        "v_mul_f32 v167, v129, v11\n"
        "v_mul_f32 v168, v129, v12\n"
        "v_mul_f32 v169, v129, v13\n"
        "v_mul_f32 v158, v158, v18\n"
        "v_mul_f32 v159, v159, v18\n"
        "v_mul_f32 v160, v160, v18\n"
        "s_add_u32 s46, s56, 4\n"
        "s_and_b32 s46, s46, 15\n"
        "s_add_u32 m0, s79, 0x1aa00\n"
        "s_lshl_b32 s46, s46, 15\n"
        "v_mul_f32 v161, v161, v18\n"
        "v_pk_fma_f32 v[166:167], v[82:83], v[128:129], v[166:167] op_sel_hi:[1,0,1]\n"
        "buffer_load_dwordx4 v38, s[16:19], s46 offen lds\n"
        "v_pk_fma_f32 v[168:169], v[84:85], v[128:129], v[168:169] op_sel_hi:[1,0,1]\n"
        "v_pk_fma_f32 v[178:179], v[86:87], v[130:131], v[178:179] op_sel_hi:[1,0,1]\n"
        "v_pk_fma_f32 v[180:181], v[88:89], v[130:131], v[180:181] op_sel_hi:[1,0,1]\n"
        "v_pk_fma_f32 v[158:159], v[90:91], v[146:147], v[158:159]\n"
        "v_pk_fma_f32 v[160:161], v[92:93], v[148:149], v[160:161]\n"
        "v_fmac_f32 v174, v74, v136\n"
        "v_fmac_f32 v175, v75, v136\n"
        "v_fmac_f32 v176, v76, v136\n"
        "v_fmac_f32 v177, v77, v136\n"
        "v_cvt_pk_bf16_f32 v174, v174, v175\n"
        "v_cvt_pk_bf16_f32 v175, v176, v177\n"
        "v_fmac_f32 v162, v70, v126\n"
        "v_fmac_f32 v163, v71, v126\n"
        "buffer_load_dwordx4 v39, s[16:19], s46 offen offset:1024 lds\n"
        "v_fmac_f32 v164, v72, v126\n"
        "v_fmac_f32 v165, v73, v126\n"
        "v_cvt_pk_bf16_f32 v162, v162, v163\n"
        "v_cvt_pk_bf16_f32 v163, v164, v165\n"
        "v_fmac_f32 v170, v62, v134\n"
        "v_fmac_f32 v171, v63, v134\n"
        "v_fmac_f32 v172, v64, v134\n"
        "v_fmac_f32 v173, v65, v134\n"
        "v_cvt_pk_bf16_f32 v170, v170, v171\n"
        "v_cvt_pk_bf16_f32 v171, v172, v173\n"
        "v_mfma_f32_16x16x16_bf16 v[74:77], v[174:175], v[162:163], v[26:29]\n"
        "s_add_u32 s46, s56, 5\n"
        "v_cvt_pk_bf16_f32 v166, v166, v167\n"
        "v_cvt_pk_bf16_f32 v167, v168, v169\n"
        "v_mfma_f32_16x16x16_bf16 v[62:65], v[170:171], v[162:163], v[22:25]\n"
        "s_and_b32 s46, s46, 15\n"
        "v_fmac_f32 v154, v78, v142\n"
        "v_fmac_f32 v155, v79, v143\n"
        "v_fmac_f32 v156, v80, v144\n"
        "v_fmac_f32 v157, v81, v145\n"
        "v_cvt_pk_bf16_f32 v54, v154, v155\n"
        "v_cvt_pk_bf16_f32 v55, v156, v157\n"
        "v_fmac_f32 v150, v66, v138\n"
        "v_fmac_f32 v151, v67, v139\n"
        "v_fmac_f32 v152, v68, v140\n"
        "v_fmac_f32 v153, v69, v141\n"
        "v_cvt_pk_bf16_f32 v52, v150, v151\n"
        "v_cvt_pk_bf16_f32 v53, v152, v153\n"
        "s_add_u32 s90, s82, 0x40000\n"
        "v_add_f32 v62, s74, v62\n"
        "v_add_f32 v63, s74, v63\n"
        "v_add_f32 v64, s74, v64\n"
        "v_add_f32 v65, s74, v65\n"
        "v_max_f32 v62, v62, v74\n"
        "v_max_f32 v63, v63, v75\n"
        "v_max_f32 v64, v64, v76\n"
        "v_max_f32 v65, v65, v77\n"
        "v_max3_f32 v172, v62, v63, v64\n"
        "v_max_f32 v173, v172, v65\n"
        "v_max_f32 v172, v172, v65\n"
        "v_cvt_pk_bf16_f32 v178, v178, v179\n"
        "v_cvt_pk_bf16_f32 v179, v180, v181\n"
        "v_permlane32_swap_b32 v172, v173\n"
        "v_max_f32 v74, v172, v173\n"
        "v_mfma_f32_16x16x16_bf16 v[86:89], v[178:179], v[166:167], v[26:29]\n"
        "s_add_u32 m0, s79, 0x1ca00\n"
        "v_max_f32 v172, v172, v173\n"
        "s_lshl_b32 s46, s46, 15\n"
        "v_mfma_f32_16x16x16_bf16 v[82:85], v[174:175], v[166:167], v[22:25]\n"
        "v_permlane16_swap_b32 v172, v74\n"
        "v_max_f32 v172, v172, v74\n"
        "v_sub_f32 v62, v62, v172\n"
        "v_sub_f32 v63, v63, v172\n"
        "v_sub_f32 v64, v64, v172\n"
        "v_sub_f32 v65, v65, v172\n"
        "v_exp_f32 v62, v62\n"
        "v_exp_f32 v63, v63\n"
        "v_exp_f32 v64, v64\n"
        "v_exp_f32 v65, v65\n"
        "v_cvt_pk_bf16_f32 v152, v62, v63\n"
        "v_cvt_pk_bf16_f32 v153, v64, v65\n"
        "v_and_b32 v150, v152, v30\n"
        "v_and_b32 v151, v153, v31\n"
        "v_xor_b32 v152, v152, v150\n"
        "v_xor_b32 v153, v153, v151\n"
        "v_cvt_pk_bf16_f32 v56, v158, v159\n"
        "v_cvt_pk_bf16_f32 v57, v160, v161\n"
        "v_mfma_f32_16x16x32_bf16 v[66:69], v[6:9], v[150:153], 0\n"
        "s_add_u32 s87, s82, 0x44000\n"
        "v_max_f32 v82, v82, v86\n"
        "v_max_f32 v83, v83, v87\n"
        "v_mfma_f32_16x16x32_bf16 v[70:73], v[52:55], v[150:153], 0\n"
        "v_max_f32 v84, v84, v88\n"
        "v_max_f32 v85, v85, v89\n"
        "v_max3_f32 v180, v82, v83, v84\n"
        "v_max_f32 v181, v180, v85\n"
        "v_rcp_f32 v66, v66\n"
        "v_max_f32 v180, v180, v85\n"
        "s_nop 1\n"
        "v_mul_f32 v70, v70, v66\n"
        "v_mul_f32 v71, v71, v66\n"
        "v_mul_f32 v72, v72, v66\n"
        "v_mul_f32 v73, v73, v66\n"
        "v_cvt_pk_bf16_f32 v70, v70, v71\n"
        "v_cvt_pk_bf16_f32 v71, v72, v73\n"
        "buffer_store_dwordx2 v[70:71], v34, s[20:23], s90 offen sc1\n"
        "buffer_load_dwordx4 v38, s[16:19], s46 offen lds\n"
        "v_mov_b32_dpp v44, v170 row_ror:8 row_mask:0xf bank_mask:0xf\n"
        "v_mov_b32 v46, v52\n"
        "v_mov_b32_dpp v45, v171 row_ror:8 row_mask:0xf bank_mask:0xf\n"
        "v_mov_b32 v47, v53\n"
        "v_permlane32_swap_b32 v46, v52\n"
        "s_nop 0\n"
        "v_permlane32_swap_b32 v47, v53\n"
        "v_add_u32 v32, 0x8400, v32\n"
        "v_add_u32 v35, 0x8400, v35\n"
        "s_mul_i32 s40, s51, 64\n"
        "v_add_u32 v182, s40, v32\n"
        "s_waitcnt vmcnt(13)\n"
        "s_barrier\n"
        "buffer_load_dwordx4 v39, s[16:19], s46 offen offset:1024 lds\n"
        "ds_read_b128 v[94:97], v182 offset:16896\n"
        "v_permlane32_swap_b32 v180, v181\n"
        "v_max_f32 v86, v180, v181\n"
        "v_max_f32 v180, v180, v181\n"
        "s_add_u32 s46, s56, 6\n"
        "s_and_b32 s46, s46, 15\n"
        "ds_read_b128 v[98:101], v182 offset:33792\n"
        "v_permlane16_swap_b32 v180, v86\n"
        "v_max_f32 v180, v180, v86\n"
        "v_sub_f32 v82, v82, v180\n"
        "v_sub_f32 v83, v83, v180\n"
        "s_add_u32 m0, s79, 0x1ea00\n"
        "s_lshl_b32 s46, s46, 15\n"
        "ds_read_b128 v[102:105], v182 offset:17152\n"
        "v_sub_f32 v84, v84, v180\n"
        "buffer_load_dwordx4 v38, s[16:19], s46 offen lds\n"
        "ds_read_b128 v[106:109], v182 offset:34048\n"
        "s_waitcnt lgkmcnt(3)\n"
        "v_mfma_f32_16x16x32_bf16 v[130:133], v[94:97], v[94:97], 0\n"
        "ds_read_b128 v[110:113], v182 offset:17408\n"
        "v_sub_f32 v85, v85, v180\n"
        "v_mfma_f32_16x16x32_bf16 v[142:145], v[6:9], v[94:97], 0\n"
        "ds_read_b128 v[114:117], v182 offset:34304\n"
        "s_waitcnt lgkmcnt(4)\n"
        "v_mfma_f32_16x16x32_bf16 v[134:137], v[98:101], v[98:101], 0\n"
        "ds_read_b128 v[118:121], v182 offset:17664\n"
        "s_waitcnt vmcnt(10)\n"
        "v_mfma_f32_16x16x32_bf16 v[146:149], v[6:9], v[98:101], 0\n"
        "s_barrier\n"
        "ds_read_b64_tr_b16 a[192:193], v41 offset:49152\n"
        "s_waitcnt lgkmcnt(5)\n"
        "v_mfma_f32_16x16x32_bf16 v[130:133], v[102:105], v[102:105], v[130:133]\n"
        "buffer_load_dwordx4 v39, s[16:19], s46 offen offset:1024 lds\n"
        "ds_read_b128 v[122:125], v182 offset:34560\n"
        "v_mfma_f32_16x16x32_bf16 v[142:145], v[6:9], v[102:105], v[142:145]\n"
        "s_waitcnt lgkmcnt(5)\n"
        "v_exp_f32 v82, v82\n"
        "v_mfma_f32_16x16x32_bf16 v[134:137], v[106:109], v[106:109], v[134:137]\n"
        "s_add_u32 s46, s56, 7\n"
        "ds_read_b128 v[94:97], v32 offset:16896\n"
        "v_and_b32 v50, 7, v1\n"
        "v_mfma_f32_16x16x32_bf16 v[146:149], v[6:9], v[106:109], v[146:149]\n"
        "s_waitcnt lgkmcnt(4)\n"
        "ds_read_b64_tr_b16 a[194:195], v41 offset:49664\n"
        "v_lshlrev_b32_e64 v50, 6, v50\n"
        "v_mfma_f32_16x16x32_bf16 v[130:133], v[110:113], v[110:113], v[130:133]\n"
        "s_and_b32 s46, s46, 15\n"
        "ds_read_b64_tr_b16 a[196:197], v43 offset:49152\n"
        "s_add_u32 m0, s79, 0\n"
        "v_mfma_f32_16x16x32_bf16 v[142:145], v[6:9], v[110:113], v[142:145]\n"
        "s_lshl_b32 s46, s46, 15\n"
        "ds_read_b128 v[98:101], v32 offset:16960\n"
        "buffer_load_dwordx4 v38, s[16:19], s46 offen lds\n"
        "v_mfma_f32_16x16x32_bf16 v[134:137], v[114:117], v[114:117], v[134:137]\n"
        "s_lshl_b32 s96, s52, 9\n"
        "ds_read_b64_tr_b16 a[198:199], v43 offset:49664\n"
        "s_add_u32 s96, s96, 0x1000\n"
        "v_mfma_f32_16x16x32_bf16 v[146:149], v[6:9], v[114:117], v[146:149]\n"
        "s_waitcnt lgkmcnt(5)\n"
        "ds_read_b64_tr_b16 a[200:201], v41\n"
        "buffer_load_dwordx2 v[48:49], v50, s[20:23], s96 offen sc1\n"
        "v_mfma_f32_16x16x32_bf16 v[130:133], v[118:121], v[118:121], v[130:133]\n"
        "ds_read_b128 v[102:105], v32 offset:17024\n"
        "v_mfma_f32_16x16x32_bf16 v[142:145], v[6:9], v[118:121], v[142:145]\n"
        "ds_read_b64_tr_b16 a[202:203], v41 offset:512\n"
        "buffer_load_dwordx4 v39, s[16:19], s46 offen offset:1024 lds\n"
        "v_mfma_f32_16x16x32_bf16 v[134:137], v[122:125], v[122:125], v[134:137]\n"
        "s_add_u32 s46, s56, 8\n"
        "ds_read_b64_tr_b16 a[204:205], v43\n"
        "s_and_b32 s46, s46, 15\n"
        "v_mfma_f32_16x16x32_bf16 v[146:149], v[6:9], v[122:125], v[146:149]\n"
        "s_waitcnt lgkmcnt(5)\n"
        "ds_read_b128 v[106:109], v32 offset:17088\n"
        "v_cndmask_b32 v130, v130, v131, s[64:65]\n"
        "v_mfma_f32_16x16x32_bf16 v[70:73], a[0:3], v[94:97], 0\n"
        "s_add_u32 m0, s79, 0x2000\n"
        "ds_read_b64_tr_b16 a[206:207], v43 offset:512\n"
        "v_cndmask_b32 v130, v130, v132, s[66:67]\n"
        "v_mfma_f32_16x16x32_bf16 v[74:77], a[4:7], v[94:97], 0\n"
        "s_lshl_b32 s46, s46, 15\n"
        "v_exp_f32 v83, v83\n"
        "v_mfma_f32_16x16x32_bf16 v[78:81], v[94:97], a[8:11], 0\n"
        "ds_read_b128 v[110:113], v32 offset:17152\n"
        "buffer_load_dwordx4 v38, s[16:19], s46 offen lds\n"
        "v_mfma_f32_16x16x32_bf16 v[70:73], a[12:15], v[98:101], v[70:73]\n"
        "s_waitcnt vmcnt(11)\n"
        "v_exp_f32 v84, v84\n"
        "v_mfma_f32_16x16x32_bf16 v[74:77], a[16:19], v[98:101], v[74:77]\n"
        "v_exp_f32 v85, v85\n"
        "v_mfma_f32_16x16x32_bf16 v[78:81], v[98:101], a[20:23], v[78:81]\n"
        "s_waitcnt lgkmcnt(2)\n"
        "ds_read_b128 v[114:117], v32 offset:17216\n"
        "v_cvt_pk_bf16_f32 v160, v82, v83\n"
        "v_mfma_f32_16x16x32_bf16 v[70:73], a[24:27], v[102:105], v[70:73]\n"
        "s_waitcnt lgkmcnt(2)\n"
        "ds_read_b128 v[118:121], v32 offset:17280\n"
        "buffer_load_dwordx4 v39, s[16:19], s46 offen offset:1024 lds\n"
        "v_mfma_f32_16x16x32_bf16 v[74:77], a[28:31], v[102:105], v[74:77]\n"
        "s_barrier\n"
        "ds_read_b64_tr_b16 a[208:209], v41 offset:8192\n"
        "v_cvt_pk_bf16_f32 v161, v84, v85\n"
        "v_mfma_f32_16x16x32_bf16 v[78:81], v[102:105], a[32:35], v[78:81]\n"
        "s_add_u32 s46, s56, 9\n"
        "ds_read_b64_tr_b16 a[210:211], v41 offset:8704\n"
        "v_and_b32 v158, v160, v30\n"
        "v_mfma_f32_16x16x32_bf16 v[70:73], a[36:39], v[106:109], v[70:73]\n"
        "s_and_b32 s46, s46, 15\n"
        "ds_read_b64_tr_b16 a[212:213], v43 offset:8192\n"
        "v_and_b32 v159, v161, v31\n"
        "v_mfma_f32_16x16x32_bf16 v[74:77], a[40:43], v[106:109], v[74:77]\n"
        "s_add_u32 m0, s79, 0x4000\n"
        "ds_read_b64_tr_b16 a[214:215], v43 offset:8704\n"
        "v_xor_b32 v160, v160, v158\n"
        "v_mfma_f32_16x16x32_bf16 v[78:81], v[106:109], a[44:47], v[78:81]\n"
        "s_waitcnt lgkmcnt(5)\n"
        "ds_read_b128 v[122:125], v32 offset:17344\n"
        "v_xor_b32_e64 v161, v161, v159\n"
        "v_mfma_f32_16x16x32_bf16 v[70:73], a[48:51], v[110:113], v[70:73]\n"
        "s_lshl_b32 s46, s46, 15\n"
        "ds_read_b64_tr_b16 a[216:217], v41 offset:16384\n"
        "buffer_load_dwordx4 v38, s[16:19], s46 offen lds\n"
        "v_mfma_f32_16x16x32_bf16 v[166:169], v[6:9], v[158:161], 0\n"
        "ds_read_b64_tr_b16 a[218:219], v41 offset:16896\n"
        "v_cndmask_b32 v143, v130, v133, s[68:69]\n"
        "v_mfma_f32_16x16x32_bf16 v[90:93], v[54:57], v[158:161], 0\n"
        "ds_read_b64_tr_b16 a[220:221], v43 offset:16384\n"
        "v_cndmask_b32 v134, v134, v135, s[64:65]\n"
        "v_mfma_f32_16x16x32_bf16 v[74:77], a[52:55], v[110:113], v[74:77]\n"
        "ds_read_b64_tr_b16 a[222:223], v43 offset:16896\n"
        "v_cndmask_b32 v134, v134, v136, s[66:67]\n"
        "v_mfma_f32_16x16x32_bf16 v[78:81], v[110:113], a[56:59], v[78:81]\n"
        "ds_read_b128 v[94:97], v32 offset:17408\n"
        "buffer_load_dwordx4 v39, s[16:19], s46 offen offset:1024 lds\n"
        "v_mfma_f32_16x16x32_bf16 v[70:73], a[60:63], v[114:117], v[70:73]\n"
        "s_add_u32 s46, s56, 10\n"
        "v_rcp_f32 v166, v166\n"
        "v_mfma_f32_16x16x32_bf16 v[74:77], a[64:67], v[114:117], v[74:77]\n"
        "s_and_b32 s46, s46, 15\n"
        "ds_read_b128 v[98:101], v32 offset:17472\n"
        "v_mul_f32 v90, v90, v166\n"
        "v_mfma_f32_16x16x32_bf16 v[78:81], v[114:117], a[68:71], v[78:81]\n"
        "s_waitcnt lgkmcnt(4)\n"
        "ds_read_b128 v[102:105], v32 offset:17536\n"
        "v_mul_f32 v91, v91, v166\n"
        "v_mfma_f32_16x16x32_bf16 v[70:73], a[72:75], v[118:121], v[70:73]\n"
        "s_add_u32 m0, s79, 0x6000\n"
        "ds_read_b128 v[106:109], v32 offset:17600\n"
        "v_mul_f32 v92, v92, v166\n"
        "v_mfma_f32_16x16x32_bf16 v[74:77], a[76:79], v[118:121], v[74:77]\n"
        "s_lshl_b32 s46, s46, 15\n"
        "ds_read_b128 v[110:113], v32 offset:17664\n"
        "v_mul_f32_e64 v93, v93, v166\n"
        "v_mfma_f32_16x16x32_bf16 v[78:81], v[118:121], a[80:83], v[78:81]\n"
        "ds_read_b128 v[114:117], v32 offset:17728\n"
        "v_cvt_pk_bf16_f32 v90, v90, v91\n"
        "v_mfma_f32_16x16x32_bf16 v[70:73], a[84:87], v[122:125], v[70:73]\n"
        "ds_read_b128 v[118:121], v32 offset:17792\n"
        "v_cvt_pk_bf16_f32 v91, v92, v93\n"
        "v_mfma_f32_16x16x32_bf16 v[74:77], a[88:91], v[122:125], v[74:77]\n"
        "buffer_store_dwordx2 v[90:91], v34, s[20:23], s87 offen sc1\n"
        "v_cndmask_b32 v147, v134, v137, s[68:69]\n"
        "v_mfma_f32_16x16x32_bf16 v[78:81], v[122:125], a[92:95], v[78:81]\n"
        "s_waitcnt lgkmcnt(4)\n"
        "ds_read_b128 v[122:125], v32 offset:17856\n"
        "s_mul_i32 s48, s51, 0x180\n"
        "v_mfma_f32_16x16x32_bf16 v[70:73], a[96:99], v[94:97], v[70:73]\n"
        "s_add_u32 s48, s48, 0x27000\n"
        "v_lshl_add_u32 v184, v2, 3, s48\n"
        "v_mfma_f32_16x16x32_bf16 v[74:77], a[100:103], v[94:97], v[74:77]\n"
        "s_mov_b64 exec, s[70:71]\n"
        "ds_write_b64 v184, v[142:143] offset:128\n"
        "ds_write_b64 v184, v[146:147] offset:256\n"
        "s_mov_b64 exec, -1\n"
        "v_mfma_f32_16x16x32_bf16 v[78:81], v[94:97], a[104:107], v[78:81]\n"
        "s_mov_b32 s48, 0x27000\n"
        "ds_read_b128 v[94:97], v32 offset:33792\n"
        "v_lshl_add_u32 v184, v1, 3, s48\n"
        "v_mfma_f32_16x16x32_bf16 v[70:73], a[108:111], v[98:101], v[70:73]\n"
        "s_lshl_b32 s48, s51, 9\n"
        "s_add_u32 s48, s48, 0x27800\n"
        "v_lshl_add_u32 v51, v1, 2, s48\n"
        "v_mfma_f32_16x16x32_bf16 v[74:77], a[112:115], v[98:101], v[74:77]\n"
        "v_lshl_add_u32 v182, v2, 2, s48\n"
        "v_lshl_add_u32 v183, v3, 4, s48\n"
        "v_mfma_f32_16x16x32_bf16 v[78:81], v[98:101], a[116:119], v[78:81]\n"
        "ds_read_b128 v[98:101], v32 offset:33856\n"
        "v_mfma_f32_16x16x32_bf16 v[70:73], a[120:123], v[102:105], v[70:73]\n"
        "s_waitcnt lgkmcnt(2)\n"
        "s_barrier\n"
        "ds_read_b64 v[126:127], v184\n"
        "v_mfma_f32_16x16x32_bf16 v[74:77], a[124:127], v[102:105], v[74:77]\n"
        "ds_read_b64 v[128:129], v184 offset:384\n"
        "v_mfma_f32_16x16x32_bf16 v[78:81], v[102:105], a[128:131], v[78:81]\n"
        "ds_read_b64 v[130:131], v184 offset:768\n"
        "v_mfma_f32_16x16x32_bf16 v[70:73], a[132:135], v[106:109], v[70:73]\n"
        "ds_read_b64 v[132:133], v184 offset:1152\n"
        "v_mfma_f32_16x16x32_bf16 v[74:77], a[136:139], v[106:109], v[74:77]\n"
        "ds_read_b64 v[186:187], v35 offset:16896\n"
        "v_mfma_f32_16x16x32_bf16 v[78:81], v[106:109], a[140:143], v[78:81]\n"
        "ds_read_b64 v[188:189], v35 offset:16928\n"
        "v_mfma_f32_16x16x32_bf16 v[70:73], a[144:147], v[110:113], v[70:73]\n"
        "ds_read_b128 v[102:105], v32 offset:33920\n"
        "v_mfma_f32_16x16x32_bf16 v[74:77], a[148:151], v[110:113], v[74:77]\n"
        "s_waitcnt lgkmcnt(5)\n"
        "ds_read_b128 v[106:109], v32 offset:33984\n"
        "buffer_load_dwordx4 v38, s[16:19], s46 offen lds\n"
        "v_mfma_f32_16x16x32_bf16 v[78:81], v[110:113], a[152:155], v[78:81]\n"
        "ds_read_b128 v[110:113], v32 offset:34048\n"
        "v_add_f32 v126, v126, v128\n"
        "v_mfma_f32_16x16x32_bf16 v[70:73], a[156:159], v[114:117], v[70:73]\n"
        "s_waitcnt lgkmcnt(5)\n"
        "v_add_f32 v130, v130, v132\n"
        "v_add_f32 v127, v127, v129\n"
        "v_mfma_f32_16x16x32_bf16 v[74:77], a[160:163], v[114:117], v[74:77]\n"
        "v_add_f32 v131, v131, v133\n"
        "v_add_f32 v126, v126, v130\n"
        "v_mfma_f32_16x16x32_bf16 v[78:81], v[114:117], a[164:167], v[78:81]\n"
        "s_waitcnt vmcnt(11)\n"
        "ds_read_b128 v[114:117], v32 offset:34112\n"
        "buffer_load_dwordx4 v39, s[16:19], s46 offen offset:1024 lds\n"
        "v_mfma_f32_16x16x32_bf16 v[70:73], a[168:171], v[118:121], v[70:73]\n"
        "s_barrier\n"
        "ds_read_b64_tr_b16 a[224:225], v41 offset:24576\n"
        "v_add_f32 v127, v127, v131\n"
        "v_mfma_f32_16x16x32_bf16 v[74:77], a[172:175], v[118:121], v[74:77]\n"
        "s_add_u32 s46, s56, 11\n"
        "ds_read_b64_tr_b16 a[226:227], v41 offset:25088\n"
        "v_mul_f32 v126, s60, v126\n"
        "v_mfma_f32_16x16x32_bf16 v[78:81], v[118:121], a[176:179], v[78:81]\n"
        "s_and_b32 s46, s46, 15\n"
        "ds_read_b64_tr_b16 a[228:229], v43 offset:24576\n"
        "v_mul_f32 v127, s60, v127\n"
        "v_mfma_f32_16x16x32_bf16 v[70:73], a[180:183], v[122:125], v[70:73]\n"
        "s_add_u32 m0, s79, 0x20a00\n"
        "ds_read_b64_tr_b16 a[230:231], v43 offset:25088\n"
        "v_fma_f32 v127, -v126, v126, v127\n"
        "v_mfma_f32_16x16x32_bf16 v[74:77], a[184:187], v[122:125], v[74:77]\n"
        "s_lshl_b32 s46, s46, 15\n"
        "ds_read_b64_tr_b16 a[232:233], v41 offset:32768\n"
        "buffer_load_dwordx4 v38, s[16:19], s46 offen lds\n"
        "v_mfma_f32_16x16x32_bf16 v[78:81], v[122:125], a[188:191], v[78:81]\n"
        "ds_read_b64_tr_b16 a[234:235], v41 offset:33280\n"
        "v_max_f32 v127, 0, v127\n"
        "v_mfma_f32_16x16x32_bf16 v[82:85], a[0:3], v[94:97], 0\n"
        "ds_read_b64_tr_b16 a[236:237], v43 offset:32768\n"
        "v_add_f32 v127, s61, v127\n"
        "v_mfma_f32_16x16x32_bf16 v[86:89], a[4:7], v[94:97], 0\n"
        "ds_read_b64_tr_b16 a[238:239], v43 offset:33280\n"
        "v_mfma_f32_16x16x32_bf16 v[90:93], v[94:97], a[8:11], 0\n"
        "v_rsq_f32 v127, v127\n"
        "v_mfma_f32_16x16x32_bf16 v[82:85], a[12:15], v[98:101], v[82:85]\n"
        "ds_read_b128 v[118:121], v32 offset:34176\n"
        "buffer_load_dwordx4 v39, s[16:19], s46 offen offset:1024 lds\n"
        "v_mfma_f32_16x16x32_bf16 v[86:89], a[16:19], v[98:101], v[86:89]\n"
        "s_add_u32 s46, s56, 12\n"
        "ds_read_b128 v[122:125], v32 offset:34240\n"
        "v_mul_f32 v126, -v126, v127\n"
        "v_mfma_f32_16x16x32_bf16 v[90:93], v[98:101], a[20:23], v[90:93]\n"
        "s_waitcnt lgkmcnt(4)\n"
        "ds_write2_b32 v51, v127, v126 offset1:64\n"
        "v_mfma_f32_16x16x32_bf16 v[82:85], a[24:27], v[102:105], v[82:85]\n"
        "s_and_b32 s46, s46, 15\n"
        "ds_read2_b32 v[136:137], v182 offset0:16 offset1:80\n"
        "s_add_u32 m0, s79, 0x14a00\n"
        "v_mfma_f32_16x16x32_bf16 v[86:89], a[28:31], v[102:105], v[86:89]\n"
        "s_lshl_b32 s46, s46, 15\n"
        "ds_read2_b32 v[130:131], v182 offset0:32 offset1:96\n"
        "buffer_load_dwordx4 v38, s[16:19], s46 offen lds\n"
        "v_mfma_f32_16x16x32_bf16 v[90:93], v[102:105], a[32:35], v[90:93]\n"
        "ds_read_b128 v[154:157], v183 offset:320\n"
        "v_mfma_f32_16x16x32_bf16 v[82:85], a[36:39], v[106:109], v[82:85]\n"
        "ds_read_b128 v[142:145], v183 offset:64\n"
        "s_waitcnt vmcnt(10)\n"
        "v_mfma_f32_16x16x32_bf16 v[86:89], a[40:43], v[106:109], v[86:89]\n"
        "s_waitcnt lgkmcnt(5)\n"
        "ds_read_b128 v[158:161], v183 offset:384\n"
        "s_barrier\n"
        "v_mfma_f32_16x16x32_bf16 v[90:93], v[106:109], a[44:47], v[90:93]\n"
        "ds_read_b64_tr_b16 a[240:241], v41 offset:40960\n"
        "buffer_load_dwordx4 v39, s[16:19], s46 offen offset:1024 lds\n"
        "v_mfma_f32_16x16x32_bf16 v[82:85], a[48:51], v[110:113], v[82:85]\n"
        "s_add_u32 s46, s56, 13\n"
        "ds_read_b64_tr_b16 a[242:243], v41 offset:41472\n"
        "s_and_b32 s46, s46, 15\n"
        "v_mfma_f32_16x16x32_bf16 v[86:89], a[52:55], v[110:113], v[86:89]\n"
        "s_add_u32 m0, s79, 0x16a00\n"
        "ds_read_b64_tr_b16 a[244:245], v43 offset:40960\n"
        "s_lshl_b32 s46, s46, 15\n"
        "v_mfma_f32_16x16x32_bf16 v[90:93], v[110:113], a[56:59], v[90:93]\n"
        "ds_read_b128 v[94:97], v32 offset:34304\n"
        "v_mfma_f32_16x16x32_bf16 v[82:85], a[60:63], v[114:117], v[82:85]\n"
        "ds_read_b64_tr_b16 a[246:247], v43 offset:41472\n"
        "buffer_load_dwordx4 v38, s[16:19], s46 offen lds\n"
        "v_mfma_f32_16x16x32_bf16 v[86:89], a[64:67], v[114:117], v[86:89]\n"
        "ds_read_b64_tr_b16 a[248:249], v40\n"
        "v_mfma_f32_16x16x32_bf16 v[90:93], v[114:117], a[68:71], v[90:93]\n"
        "ds_read_b128 v[98:101], v32 offset:34368\n"
        "v_mfma_f32_16x16x32_bf16 v[82:85], a[72:75], v[118:121], v[82:85]\n"
        "ds_read_b64_tr_b16 a[250:251], v40 offset:512\n"
        "v_mfma_f32_16x16x32_bf16 v[86:89], a[76:79], v[118:121], v[86:89]\n"
        "ds_read_b64_tr_b16 a[252:253], v42\n"
        "buffer_load_dwordx4 v39, s[16:19], s46 offen offset:1024 lds\n"
        "v_mfma_f32_16x16x32_bf16 v[90:93], v[118:121], a[80:83], v[90:93]\n"
        "s_add_u32 s46, s56, 14\n"
        "ds_read_b128 v[102:105], v32 offset:34432\n"
        "s_and_b32 s46, s46, 15\n"
        "v_mfma_f32_16x16x32_bf16 v[82:85], a[84:87], v[122:125], v[82:85]\n"
        "s_add_u32 m0, s79, 0x18a00\n"
        "ds_read_b64_tr_b16 a[254:255], v42 offset:512\n"
        "s_lshl_b32 s46, s46, 15\n"
        "v_mfma_f32_16x16x32_bf16 v[86:89], a[88:91], v[122:125], v[86:89]\n"
        "ds_read_b128 v[146:149], v183 offset:128\n"
        "s_mov_b32 s48, 0x3eb8aa3b\n"
        "v_mfma_f32_16x16x32_bf16 v[90:93], v[122:125], a[92:95], v[90:93]\n"
        "s_waitcnt lgkmcnt(5)\n"
        "ds_read_b128 v[106:109], v32 offset:34496\n"
        "buffer_load_dwordx4 v38, s[16:19], s46 offen lds\n"
        "v_mfma_f32_16x16x32_bf16 v[82:85], a[96:99], v[94:97], v[82:85]\n"
        "ds_read_b128 v[110:113], v32 offset:34560\n"
        "v_mul_f32 v126, s48, v136\n"
        "v_mfma_f32_16x16x32_bf16 v[86:89], a[100:103], v[94:97], v[86:89]\n"
        "ds_read_b128 v[114:117], v32 offset:34624\n"
        "v_mul_f32 v127, s48, v137\n"
        "v_mfma_f32_16x16x32_bf16 v[90:93], v[94:97], a[104:107], v[90:93]\n"
        "s_waitcnt vmcnt(10)\n"
        "ds_read_b128 v[118:121], v32 offset:34688\n"
        "v_mul_f32 v128, s48, v130\n"
        "v_mfma_f32_16x16x32_bf16 v[82:85], a[108:111], v[98:101], v[82:85]\n"
        "s_barrier\n"
        "ds_read_b64_tr_b16 v[192:193], v40 offset:8192\n"
        "buffer_load_dwordx4 v39, s[16:19], s46 offen offset:1024 lds\n"
        "v_mfma_f32_16x16x32_bf16 v[86:89], a[112:115], v[98:101], v[86:89]\n"
        "s_add_u32 s46, s56, 15\n"
        "ds_read_b64_tr_b16 v[194:195], v40 offset:8704\n"
        "v_mul_f32 v129, s48, v131\n"
        "v_mfma_f32_16x16x32_bf16 v[90:93], v[98:101], a[116:119], v[90:93]\n"
        "s_waitcnt lgkmcnt(5)\n"
        "ds_read_b64_tr_b16 v[196:197], v42 offset:8192\n"
        "v_mul_f32 v174, v137, v14\n"
        "v_mfma_f32_16x16x32_bf16 v[82:85], a[120:123], v[102:105], v[82:85]\n"
        "s_and_b32 s46, s46, 15\n"
        "ds_read_b64_tr_b16 v[198:199], v42 offset:8704\n"
        "v_mul_f32 v175, v137, v15\n"
        "v_mfma_f32_16x16x32_bf16 v[86:89], a[124:127], v[102:105], v[86:89]\n"
        "s_add_u32 m0, s79, 0x1aa00\n"
        "ds_read_b64_tr_b16 v[200:201], v40 offset:16384\n"
        "v_mul_f32 v176, v137, v16\n"
        "v_mfma_f32_16x16x32_bf16 v[90:93], v[102:105], a[128:131], v[90:93]\n"
        "s_lshl_b32 s46, s46, 15\n"
        "ds_read_b64_tr_b16 v[202:203], v40 offset:16896\n"
        "v_mul_f32 v177, v137, v17\n"
        "v_mfma_f32_16x16x32_bf16 v[82:85], a[132:135], v[106:109], v[82:85]\n"
        "ds_read_b64_tr_b16 v[204:205], v42 offset:16384\n"
        "v_mul_f32 v162, v127, v10\n"
        "v_mfma_f32_16x16x32_bf16 v[86:89], a[136:139], v[106:109], v[86:89]\n"
        "ds_read_b64_tr_b16 v[206:207], v42 offset:16896\n"
        "v_mul_f32 v163, v127, v11\n"
        "v_mfma_f32_16x16x32_bf16 v[90:93], v[106:109], a[140:143], v[90:93]\n"
        "s_waitcnt lgkmcnt(5)\n"
        "ds_read_b128 v[122:125], v32 offset:34752\n"
        "v_mul_f32 v164, v127, v12\n"
        "v_mfma_f32_16x16x32_bf16 v[82:85], a[144:147], v[110:113], v[82:85]\n"
        "v_cmp_ne_u64 vcc, v[48:49], s[62:63]\n"
        "s_cbranch_vccnz L_slow_flag0g_%=\n"
        "L_ret_flag0g_%=:\n"
        "v_mfma_f32_16x16x32_bf16 v[86:89], a[148:151], v[110:113], v[86:89]\n"
        "v_mul_f32 v165, v127, v13\n"
        "v_mul_f32 v154, v154, v18\n"
        "v_mfma_f32_16x16x32_bf16 v[90:93], v[110:113], a[152:155], v[90:93]\n"
        "v_mul_f32 v155, v155, v18\n"
        "v_mul_f32 v156, v156, v18\n"
        "v_mfma_f32_16x16x32_bf16 v[82:85], a[156:159], v[114:117], v[82:85]\n"
        "buffer_load_dwordx4 v38, s[16:19], s46 offen lds\n"
        "v_mul_f32 v157, v157, v18\n"
        "v_mfma_f32_16x16x32_bf16 v[86:89], a[160:163], v[114:117], v[86:89]\n"
        "v_mul_f32 v178, v131, v14\n"
        "v_mul_f32 v179, v131, v15\n"
        "v_mfma_f32_16x16x32_bf16 v[90:93], v[114:117], a[164:167], v[90:93]\n"
        "v_mul_f32 v180, v131, v16\n"
        "v_mul_f32 v181, v131, v17\n"
        "v_mfma_f32_16x16x32_bf16 v[82:85], a[168:171], v[118:121], v[82:85]\n"
        "v_mul_f32 v166, v129, v10\n"
        "v_mul_f32 v167, v129, v11\n"
        "L_pg3_%=:\n"
        "s_cbranch_execz L_pg7_%=\n"
        "v_mfma_f32_16x16x32_bf16 v[86:89], a[172:175], v[118:121], v[86:89]\n"
        "buffer_load_dwordx4 v39, s[16:19], s46 offen offset:1024 lds\n"
        "v_mul_f32 v168, v129, v12\n"
        "v_mfma_f32_16x16x32_bf16 v[90:93], v[118:121], a[176:179], v[90:93]\n"
        "s_waitcnt lgkmcnt(0)\n"
        "v_mul_f32 v169, v129, v13\n"
        "v_mul_f32 v158, v158, v18\n"
        "v_mfma_f32_16x16x32_bf16 v[82:85], a[180:183], v[122:125], v[82:85]\n"
        "s_add_u32 m0, s81, 0x8400\n"
        "v_mul_f32 v159, v159, v18\n"
        "v_mul_f32 v160, v160, v18\n"
        "v_mfma_f32_16x16x32_bf16 v[86:89], a[184:187], v[122:125], v[86:89]\n"
        "s_add_u32 s46, s83, 0x40000\n"
        "v_mul_f32 v161, v161, v18\n"
        "v_fmac_f32 v174, v74, v136\n"
        "v_mfma_f32_16x16x32_bf16 v[90:93], v[122:125], a[188:191], v[90:93]\n"
        "buffer_load_dwordx4 v4, s[20:23], s46 offen sc1 lds\n"
        "v_fmac_f32 v175, v75, v136\n"
        "v_fmac_f32 v176, v76, v136\n"
        "v_fmac_f32 v177, v77, v136\n"
        "v_cvt_pk_bf16_f32 v174, v174, v175\n"
        "v_pk_fma_f32 v[166:167], v[82:83], v[128:129], v[166:167] op_sel_hi:[1,0,1]\n"
        "v_pk_fma_f32 v[168:169], v[84:85], v[128:129], v[168:169] op_sel_hi:[1,0,1]\n"
        "v_pk_fma_f32 v[178:179], v[86:87], v[130:131], v[178:179] op_sel_hi:[1,0,1]\n"
        "v_pk_fma_f32 v[180:181], v[88:89], v[130:131], v[180:181] op_sel_hi:[1,0,1]\n"
        "v_pk_fma_f32 v[158:159], v[90:91], v[146:147], v[158:159]\n"
        "v_pk_fma_f32 v[160:161], v[92:93], v[148:149], v[160:161]\n"
        "v_cvt_pk_bf16_f32 v166, v166, v167\n"
        "s_add_u32 m0, s81, 0x8800\n"
        "s_add_u32 s46, s84, 0x40000\n"
        "v_cvt_pk_bf16_f32 v167, v168, v169\n"
        "v_cvt_pk_bf16_f32 v178, v178, v179\n"
        "buffer_load_dwordx4 v4, s[20:23], s46 offen sc1 lds\n"
        "v_cvt_pk_bf16_f32 v179, v180, v181\n"
        "v_cvt_pk_bf16_f32 v175, v176, v177\n"
        "v_fmac_f32 v162, v70, v126\n"
        "v_fmac_f32 v163, v71, v126\n"
        "v_fmac_f32 v164, v72, v126\n"
        "v_fmac_f32 v165, v73, v126\n"
        "v_cvt_pk_bf16_f32 v162, v162, v163\n"
        "v_cvt_pk_bf16_f32 v163, v164, v165\n"
        "s_waitcnt vmcnt(10)\n"
        "s_barrier\n"
        "v_mfma_f32_16x16x16_bf16 v[74:77], v[174:175], v[162:163], v[26:29]\n"
        "s_add_u32 m0, s81, 0x8c00\n"
        "ds_read_b64_tr_b16 v[208:209], v40 offset:24576\n"
        "s_add_u32 s46, s85, 0x40000\n"
        "v_mfma_f32_16x16x16_bf16 v[62:65], v[44:45], v[162:163], v[22:25]\n"
        "v_fmac_f32 v154, v78, v142\n"
        "v_fmac_f32 v155, v79, v143\n"
        "v_fmac_f32 v156, v80, v144\n"
        "v_fmac_f32 v157, v81, v145\n"
        "v_cvt_pk_bf16_f32 v54, v154, v155\n"
        "v_cvt_pk_bf16_f32 v55, v156, v157\n"
        "buffer_load_dwordx4 v4, s[20:23], s46 offen sc1 lds\n"
        "v_mfma_f32_16x16x16_bf16 v[86:89], v[178:179], v[166:167], v[26:29]\n"
        "s_add_u32 m0, s81, 0x9000\n"
        "ds_read_b64_tr_b16 v[210:211], v40 offset:25088\n"
        "v_mov_b32_e64 v52, v46\n"
        "v_mfma_f32_16x16x16_bf16 v[82:85], v[174:175], v[166:167], v[22:25]\n"
        "s_add_u32 s46, s86, 0x40000\n"
        "v_cvt_pk_bf16_f32 v56, v158, v159\n"
        "v_cvt_pk_bf16_f32 v57, v160, v161\n"
        "s_add_u32 s87, s82, 0x144000\n"
        "v_mov_b32 v53, v47\n"
        "s_add_u32 s90, s82, 0x140000\n"
        "v_max_f32 v62, v62, v74\n"
        "v_max_f32 v63, v63, v75\n"
        "v_max_f32 v82, v82, v86\n"
        "buffer_load_dwordx4 v4, s[20:23], s46 offen sc1 lds\n"
        "v_max_f32 v83, v83, v87\n"
        "v_max_f32 v84, v84, v88\n"
        "v_max_f32 v85, v85, v89\n"
        "v_max3_f32 v180, v82, v83, v84\n"
        "v_max_f32 v181, v180, v85\n"
        "v_max_f32 v180, v180, v85\n"
        "v_max_f32 v64, v64, v76\n"
        "v_max_f32 v65, v65, v77\n"
        "v_permlane32_swap_b32 v180, v181\n"
        "v_max_f32 v86, v180, v181\n"
        "v_max_f32 v180, v180, v181\n"
        "v_max3_f32 v172, v62, v63, v64\n"
        "v_max_f32 v173, v172, v65\n"
        "v_permlane16_swap_b32 v180, v86\n"
        "v_max_f32 v180, v180, v86\n"
        "v_sub_f32 v82, v82, v180\n"
        "v_sub_f32 v83, v83, v180\n"
        "v_sub_f32 v84, v84, v180\n"
        "v_sub_f32 v85, v85, v180\n"
        "v_exp_f32 v82, v82\n"
        "v_exp_f32 v83, v83\n"
        "v_exp_f32 v84, v84\n"
        "v_exp_f32 v85, v85\n"
        "v_cvt_pk_bf16_f32 v160, v82, v83\n"
        "v_cvt_pk_bf16_f32 v161, v84, v85\n"
        "v_and_b32 v158, v160, v30\n"
        "v_and_b32 v159, v161, v31\n"
        "v_xor_b32 v160, v160, v158\n"
        "v_xor_b32 v161, v161, v159\n"
        "v_max_f32 v172, v172, v65\n"
        "s_nop 0\n"
        "v_mfma_f32_16x16x32_bf16 v[166:169], v[6:9], v[158:161], 0\n"
        "ds_read_b64_tr_b16 v[212:213], v42 offset:24576\n"
        "v_mfma_f32_16x16x32_bf16 v[90:93], v[54:57], v[158:161], 0\n"
        "ds_read_b64_tr_b16 v[214:215], v42 offset:25088\n"
        "v_permlane32_swap_b32 v172, v173\n"
        "v_max_f32 v74, v172, v173\n"
        "v_max_f32 v172, v172, v173\n"
        "s_nop 1\n"
        "v_rcp_f32_e64 v166, v166\n"
        "s_nop 0\n"
        "v_pk_mul_f32 v[90:91], v[90:91], v[166:167] op_sel_hi:[1,0]\n"
        "v_pk_mul_f32 v[92:93], v[92:93], v[166:167] op_sel_hi:[1,0]\n"
        "v_cvt_pk_bf16_f32 v90, v90, v91\n"
        "v_cvt_pk_bf16_f32 v91, v92, v93\n"
        "buffer_store_dwordx2 v[90:91], v34, s[20:23], s87 offen sc1\n"
        "v_permlane16_swap_b32 v172, v74\n"
        "v_max_f32 v172, v172, v74\n"
        "v_sub_f32 v62, v62, v172\n"
        "v_sub_f32 v63, v63, v172\n"
        "v_sub_f32 v64, v64, v172\n"
        "v_sub_f32 v65, v65, v172\n"
        "v_exp_f32 v62, v62\n"
        "v_exp_f32 v63, v63\n"
        "v_exp_f32 v64, v64\n"
        "v_exp_f32 v65, v65\n"
        "v_cvt_pk_bf16_f32 v152, v62, v63\n"
        "v_cvt_pk_bf16_f32 v153, v64, v65\n"
        "v_and_b32 v150, v152, v30\n"
        "v_and_b32 v151, v153, v31\n"
        "v_xor_b32 v152, v152, v150\n"
        "v_xor_b32_e64 v153, v153, v151\n"
        "s_nop 1\n"
        "v_mfma_f32_16x16x32_bf16 v[66:69], v[6:9], v[150:153], 0\n"
        "ds_read_b64_tr_b16 v[216:217], v41 offset:49152\n"
        "v_mfma_f32_16x16x32_bf16 v[70:73], v[52:55], v[150:153], 0\n"
        "ds_read_b64_tr_b16 v[218:219], v41 offset:49664\n"
        "ds_read_b64_tr_b16 v[220:221], v43 offset:49152\n"
        "ds_read_b64_tr_b16 v[222:223], v43 offset:49664\n"
        "s_waitcnt vmcnt(9)\n"
        "s_barrier\n"
        "ds_read_b64_tr_b16 v[224:225], v41\n"
        "v_rcp_f32 v66, v66\n"
        "s_nop 0\n"
        "v_mul_f32 v70, v70, v66\n"
        "v_mul_f32 v71, v71, v66\n"
        "v_mul_f32 v72, v72, v66\n"
        "v_mul_f32 v73, v73, v66\n"
        "v_cvt_pk_bf16_f32 v70, v70, v71\n"
        "v_cvt_pk_bf16_f32 v71, v72, v73\n"
        "buffer_store_dwordx2 v[70:71], v34, s[20:23], s90 offen sc1\n"
        "ds_read_b64_tr_b16 v[226:227], v41 offset:512\n"
        "ds_read_b64_tr_b16 v[228:229], v43\n"
        "ds_read_b64_tr_b16 v[230:231], v43 offset:512\n"
        "ds_read_b64_tr_b16 v[232:233], v41 offset:8192\n"
        "ds_read_b64_tr_b16 v[234:235], v41 offset:8704\n"
        "ds_read_b64_tr_b16 v[236:237], v43 offset:8192\n"
        "ds_read_b64_tr_b16 v[238:239], v43 offset:8704\n"
        "s_waitcnt vmcnt(6)\n"
        "s_barrier\n"
        "ds_read_b64_tr_b16 v[240:241], v41 offset:16384\n"
        "ds_read_b64_tr_b16 v[242:243], v41 offset:16896\n"
        "ds_read_b64_tr_b16 v[244:245], v43 offset:16384\n"
        "ds_read_b64_tr_b16 v[246:247], v43 offset:16896\n"
        "ds_read_b64_tr_b16 v[248:249], v41 offset:24576\n"
        "ds_read_b64_tr_b16 v[250:251], v41 offset:25088\n"
        "ds_read_b64_tr_b16 v[252:253], v43 offset:24576\n"
        "ds_read_b64_tr_b16 v[254:255], v43 offset:25088\n"
        "s_mov_b32 s43, 0xffff0000\n"
        "v_lshlrev_b32 v106, 16, v58\n"
        "v_and_b32 v107, s43, v58\n"
        "v_lshlrev_b32 v108, 16, v59\n"
        "v_and_b32 v109, s43, v59\n"
        "v_lshlrev_b32 v110, 16, v60\n"
        "v_and_b32 v111, s43, v60\n"
        "v_lshlrev_b32 v112, 16, v61\n"
        "v_and_b32 v113, s43, v61\n"
        "v_lshlrev_b32 v45, 6, v0\n"
        "s_mov_b32 s91, 0x1000\n"
        "buffer_load_dwordx2 v[46:47], v45, s[20:23], s91 offen sc1\n"
        "s_waitcnt vmcnt(3)\n"
        "s_lshl_b32 s43, s51, 12\n"
        "s_add_u32 s43, s43, 0x8400\n"
        "v_add_u32 v75, s43, v4\n"
        "ds_read_b128 v[78:81], v75\n"
        "ds_read_b128 v[94:97], v75 offset:1024\n"
        "ds_read_b128 v[98:101], v75 offset:2048\n"
        "ds_read_b128 v[102:105], v75 offset:3072\n"
        "s_waitcnt lgkmcnt(3)\n"
        "v_max3_u32 v74, v78, v79, v80\n"
        "s_waitcnt lgkmcnt(2)\n"
        "v_max3_u32 v74, v74, v81, v94\n"
        "v_max3_u32 v74, v74, v95, v96\n"
        "s_waitcnt lgkmcnt(1)\n"
        "v_max3_u32 v74, v74, v97, v98\n"
        "v_max3_u32 v74, v74, v99, v100\n"
        "s_waitcnt lgkmcnt(0)\n"
        "v_max3_u32 v74, v74, v101, v102\n"
        "v_max3_u32 v74, v74, v103, v104\n"
        "v_max_u32 v74, v74, v105\n"
        "v_cmp_eq_u32 vcc, -1, v74\n"
        "s_cbranch_vccnz L_slow_o00_%=\n"
        "L_ret_o00_%=:\n"
        "s_barrier\n"
        "ds_read_b128 v[62:65], v4 offset:33792\n"
        "s_add_u32 s92, s34, 0x240000\n"
        "s_add_u32 s93, s34, 0x241000\n"
        "s_add_u32 s94, s34, 0x248000\n"
        "ds_read_b128 v[66:69], v4 offset:34816\n"
        "s_add_u32 s95, s34, 0x249000\n"
        "s_lshl_b32 s48, s51, 8\n"
        "s_add_u32 s48, s48, 0x27000\n"
        "ds_read_b128 v[70:73], v4 offset:35840\n"
        "v_lshl_add_u32 v115, v1, 2, s48\n"
        "s_mov_b32 s48, 0x27000\n"
        "v_lshl_add_u32 v114, v1, 2, s48\n"
        "s_add_u32 s40, s35, 0x10000\n"
        "ds_read_b128 v[74:77], v4 offset:36864\n"
        "ds_read_b128 v[78:81], v4 offset:37888\n"
        "s_waitcnt lgkmcnt(4)\n"
        "v_mfma_f32_16x16x32_bf16 v[106:109], a[192:195], v[62:65], v[106:109]\n"
        "ds_read_b128 v[94:97], v4 offset:38912\n"
        "v_mfma_f32_16x16x32_bf16 v[110:113], a[196:199], v[62:65], v[110:113]\n"
        "s_waitcnt lgkmcnt(4)\n"
        "ds_read_b128 v[98:101], v4 offset:39936\n"
        "v_mfma_f32_16x16x32_bf16 v[106:109], a[200:203], v[66:69], v[106:109]\n"
        "ds_read_b128 v[102:105], v4 offset:40960\n"
        "v_mfma_f32_16x16x32_bf16 v[110:113], a[204:207], v[66:69], v[110:113]\n"
        "s_waitcnt lgkmcnt(4)\n"
        "ds_read_b128 v[62:65], v4 offset:41984\n"
        "v_lshrrev_b32 v48, 1, v3\n"
        "v_mfma_f32_16x16x32_bf16 v[106:109], a[208:211], v[70:73], v[106:109]\n"
        "ds_read_b128 v[66:69], v4 offset:43008\n"
        "v_and_b32_e64 v49, 1, v3\n"
        "v_mfma_f32_16x16x32_bf16 v[110:113], a[212:215], v[70:73], v[110:113]\n"
        "s_lshl_b32 s44, s51, 7\n"
        "ds_read_b128 v[70:73], v4 offset:44032\n"
        "v_lshl_add_u32 v50, v2, 3, s44\n"
        "v_mfma_f32_16x16x32_bf16 v[106:109], a[216:219], v[74:77], v[106:109]\n"
        "v_lshl_add_u32 v50, v48, 2, v50\n"
        "v_lshlrev_b32 v19, 2, v50\n"
        "v_mfma_f32_16x16x32_bf16 v[110:113], a[220:223], v[74:77], v[110:113]\n"
        "s_waitcnt lgkmcnt(4)\n"
        "ds_read_b128 v[74:77], v4 offset:45056\n"
        "v_lshl_add_u32 v19, v49, 11, v19\n"
        "v_mfma_f32_16x16x32_bf16 v[106:109], a[224:227], v[78:81], v[106:109]\n"
        "s_mov_b32 s44, 0x24d00\n"
        "v_lshl_add_u32 v20, v50, 1, s44\n"
        "s_mov_b32 s44, 0x900\n"
        "v_mfma_f32_16x16x32_bf16 v[110:113], a[228:231], v[78:81], v[110:113]\n"
        "ds_read_b128 v[78:81], v4 offset:46080\n"
        "v_mad_u32_u24 v20, v49, s44, v20\n"
        "v_mfma_f32_16x16x32_bf16 v[106:109], a[232:235], v[94:97], v[106:109]\n"
        "v_lshrrev_b32 v50, 2, v2\n"
        "s_lshl_b32 s44, s51, 3\n"
        "v_mfma_f32_16x16x32_bf16 v[110:113], a[236:239], v[94:97], v[110:113]\n"
        "ds_read_b128 v[94:97], v4 offset:47104\n"
        "v_lshl_add_u32 v50, v50, 1, s44\n"
        "v_mfma_f32_16x16x32_bf16 v[106:109], a[240:243], v[98:101], v[106:109]\n"
        "v_add_u32 v50, v50, v49\n"
        "s_mov_b32 s44, 0xa000\n"
        "v_mfma_f32_16x16x32_bf16 v[110:113], a[244:247], v[98:101], v[110:113]\n"
        "s_waitcnt lgkmcnt(2)\n"
        "ds_read_b128 v[98:101], v4 offset:48128\n"
        "v_lshlrev_b32 v42, 3, v48\n"
        "v_mfma_f32_16x16x32_bf16 v[106:109], a[248:251], v[102:105], v[106:109]\n"
        "v_mad_u32_u24 v42, v50, s44, v42\n"
        "v_and_b32 v50, 3, v2\n"
        "v_mfma_f32_16x16x32_bf16 v[110:113], a[252:255], v[102:105], v[110:113]\n"
        "ds_read_b128 v[102:105], v4 offset:49152\n"
        "v_lshlrev_b32 v41, 4, v50\n"
        "v_mfma_f32_16x16x32_bf16 v[106:109], v[192:195], v[62:65], v[106:109]\n"
        "s_lshl_b32 s44, s50, 3\n"
        "s_and_b32 s44, s44, 32\n"
        "v_xor_b32 v41, s44, v41\n"
        "v_mfma_f32_16x16x32_bf16 v[110:113], v[196:199], v[62:65], v[110:113]\n"
        "s_lshl_b32 s44, s50, 7\n"
        "v_add3_u32 v41, v41, v42, s44\n"
        "v_add_u32 v50, v2, v3\n"
        "v_mfma_f32_16x16x32_bf16 v[106:109], v[200:203], v[66:69], v[106:109]\n"
        "s_mul_i32 s44, s51, 0x900\n"
        "s_add_u32 s44, s44, 0x24c10\n"
        "v_lshl_add_u32 v21, v50, 4, s44\n"
        "v_mfma_f32_16x16x32_bf16 v[110:113], v[204:207], v[66:69], v[110:113]\n"
        "v_bfe_u32 v48, v1, 2, 2\n"
        "v_and_b32 v49, 1, v1\n"
        "v_mfma_f32_16x16x32_bf16 v[106:109], v[208:211], v[70:73], v[106:109]\n"
        "s_and_b32 s45, s51, 1\n"
        "v_xor_b32 v50, s45, v49\n"
        "v_and_b32 v51, s45, v50\n"
        "v_mfma_f32_16x16x32_bf16 v[110:113], v[212:215], v[70:73], v[110:113]\n"
        "v_add_u32 v48, v48, v51\n"
        "v_lshl_add_u32 v50, v50, 2, v48\n"
        "v_mfma_f32_16x16x32_bf16 v[106:109], v[216:219], v[74:77], v[106:109]\n"
        "s_lshr_b32 s44, s51, 1\n"
        "s_mul_i32 s44, s44, 0x49a0\n"
        "s_add_u32 s44, s44, 0x14a38\n"
        "v_mfma_f32_16x16x32_bf16 v[110:113], v[220:223], v[74:77], v[110:113]\n"
        "s_waitcnt lgkmcnt(0)\n"
        "v_mul_u32_u24 v38, 0x820, v50\n"
        "v_and_b32 v51, 2, v1\n"
        "v_mfma_f32_16x16x32_bf16 v[106:109], v[224:227], v[78:81], v[106:109]\n"
        "v_lshl_add_u32 v38, v51, 2, v38\n"
        "v_and_b32 v51, 4, v48\n"
        "v_mfma_f32_16x16x32_bf16 v[110:113], v[228:231], v[78:81], v[110:113]\n"
        "v_lshl_add_u32 v38, v51, 5, v38\n"
        "v_lshlrev_b32 v51, 4, v3\n"
        "v_mfma_f32_16x16x32_bf16 v[106:109], v[232:235], v[94:97], v[106:109]\n"
        "v_sub_u32 v38, v38, v51\n"
        "v_add_u32 v38, s44, v38\n"
        "v_mfma_f32_16x16x32_bf16 v[110:113], v[236:239], v[94:97], v[110:113]\n"
        "s_add_u32 s44, s81, 0x1dd40\n"
        "v_add_u32 v39, s44, v4\n"
        "s_lshl_b32 s44, s51, 10\n"
        "v_mfma_f32_16x16x32_bf16 v[106:109], v[240:243], v[98:101], v[106:109]\n"
        "s_add_u32 s44, s44, 0x1dd40\n"
        "v_add_u32 v40, s44, v4\n"
        "v_mfma_f32_16x16x32_bf16 v[110:113], v[244:247], v[98:101], v[110:113]\n"
        "v_mfma_f32_16x16x32_bf16 v[106:109], v[248:251], v[102:105], v[106:109]\n"
        "v_mfma_f32_16x16x32_bf16 v[110:113], v[252:255], v[102:105], v[110:113]\n"
        "v_mov_b32 v1, v1\n"
        "s_nop 3\n"
        "s_nop 1\n"
        "v_pk_add_f32 v[66:67], v[106:107], v[108:109]\n"
        "v_pk_mul_f32 v[68:69], v[106:107], v[106:107]\n"
        "v_pk_add_f32 v[66:67], v[66:67], v[110:111]\n"
        "v_pk_fma_f32 v[68:69], v[108:109], v[108:109], v[68:69]\n"
        "v_pk_add_f32 v[66:67], v[66:67], v[112:113]\n"
        "v_pk_fma_f32 v[68:69], v[110:111], v[110:111], v[68:69]\n"
        "v_add_f32 v66, v66, v67\n"
        "v_pk_fma_f32 v[68:69], v[112:113], v[112:113], v[68:69]\n"
        "v_add_f32 v67, v68, v69\n"
        "v_cvt_pk_bf16_f32 v62, v106, v107\n"
        "v_cvt_pk_bf16_f32 v63, v108, v109\n"
        "v_permlane32_swap_b32 v66, v67\n"
        "v_add_f32 v66, v66, v67\n"
        "v_cvt_pk_bf16_f32 v64, v110, v111\n"
        "v_cvt_pk_bf16_f32 v65, v112, v113\n"
        "ds_write_b32 v115, v66\n"
        "buffer_store_dword v62, v36, s[20:23], s92 offen sc1\n"
        "buffer_store_dword v63, v36, s[20:23], s93 offen sc1\n"
        "buffer_store_dword v64, v36, s[20:23], s94 offen sc1\n"
        "buffer_store_dword v65, v36, s[20:23], s95 offen sc1\n"
        "s_waitcnt lgkmcnt(0)\n"
        "s_barrier\n"
        "ds_read2st64_b32 v[70:71], v114 offset1:1\n"
        "ds_read2st64_b32 v[72:73], v114 offset0:2 offset1:3\n"
        "s_waitcnt lgkmcnt(0)\n"
        "v_pk_add_f32 v[70:71], v[70:71], v[72:73]\n"
        "v_add_f32 v72, v70, v71\n"
        "v_add_f32 v70, v70, v71\n"
        "s_nop 1\n"
        "v_permlane16_swap_b32 v70, v72\n"
        "v_add_f32 v70, v70, v72\n"
        "buffer_store_dword v70, v37, s[20:23], s40 offen sc1\n"
        "v_and_b32 v57, 0x7f, v0\n"
        "v_lshlrev_b32 v57, 4, v57\n"
        "v_cvt_f32_u32 v106, v57\n"
        "s_mov_b32 s40, 0x43000000\n"
        "s_mov_b32 s43, 0x82800000\n"
        "v_add_f32 v108, -1.0, v106\n"
        "v_max_f32 v107, s40, v106\n"
        "v_and_b32 v107, 0x7f800000, v107\n"
        "v_sub_u32 v107, s43, v107\n"
        "v_max_f32 v108, s40, v108\n"
        "v_and_b32 v108, 0x7f800000, v108\n"
        "v_sub_u32 v108, s43, v108\n"
        "v_lshrrev_b32 v106, 16, v106\n"
        "v_subrev_u32 v106, 0x4280, v106\n"
        "v_min_i32 v62, 0x80, v57\n"
        "v_max_i32 v106, v106, v62\n"
        "v_add_u32 v106, 0x4b400000, v106\n"
        "s_lshr_b32 s41, s51, 1\n"
        "s_mul_i32 s42, s41, 0x1a00\n"
        "s_add_u32 s42, s42, 0xfc323c20\n"
        "v_fmamk_f32 v62, v108, 0xc0c00000, v106\n"
        "v_lshrrev_b32 v63, 4, v62\n"
        "v_add_u32 v62, v63, v62\n"
        "v_mad_u32_u24 v62, v62, 4, s42\n"
        "v_fmamk_f32 v64, v108, 0xc0a00000, v106\n"
        "v_lshrrev_b32 v65, 4, v64\n"
        "v_add_u32 v64, v65, v64\n"
        "v_mad_u32_u24 v64, v64, 4, s42\n"
        "v_fmamk_f32 v66, v108, 0xc0800000, v106\n"
        "v_lshrrev_b32 v67, 4, v66\n"
        "v_add_u32 v66, v67, v66\n"
        "v_mad_u32_u24 v66, v66, 4, s42\n"
        "v_fmamk_f32 v68, v108, 0xc0400000, v106\n"
        "v_lshrrev_b32 v69, 4, v68\n"
        "v_add_u32 v68, v69, v68\n"
        "v_mad_u32_u24 v68, v68, 4, s42\n"
        "ds_read2st64_b32 v[62:63], v62 offset1:13\n"
        "ds_read2st64_b32 v[64:65], v64 offset1:13\n"
        "ds_read2st64_b32 v[66:67], v66 offset1:13\n"
        "ds_read2st64_b32 v[68:69], v68 offset1:13\n"
        "s_add_u32 m0, s81, 0\n"
        "s_add_u32 s46, s83, 0x140000\n"
        "v_fmamk_f32 v70, v108, 0xc0000000, v106\n"
        "v_lshrrev_b32 v71, 4, v70\n"
        "buffer_load_dwordx4 v4, s[20:23], s46 offen sc1 lds\n"
        "v_add_u32 v70, v71, v70\n"
        "v_mad_u32_u24 v70, v70, 4, s42\n"
        "v_fmamk_f32 v72, v108, 0xbf800000, v106\n"
        "v_lshrrev_b32 v73, 4, v72\n"
        "v_add_u32 v72, v73, v72\n"
        "v_mad_u32_u24 v72, v72, 4, s42\n"
        "v_lshrrev_b32 v75, 4, v106\n"
        "v_add_u32 v74, v75, v106\n"
        "v_mad_u32_u24 v74, v74, 4, s42\n"
        "v_fmamk_f32 v76, v107, 0x3f800000, v106\n"
        "v_lshrrev_b32 v77, 4, v76\n"
        "s_add_u32 m0, s81, 0x400\n"
        "s_add_u32 s46, s84, 0x140000\n"
        "v_add_u32_e64 v76, v77, v76\n"
        "v_mad_u32_u24 v76, v76, 4, s42\n"
        "buffer_load_dwordx4 v4, s[20:23], s46 offen sc1 lds\n"
        "ds_read2st64_b32 v[70:71], v70 offset1:13\n"
        "s_add_u32 m0, s81, 0x800\n"
        "s_add_u32 s46, s85, 0x140000\n"
        "ds_read2st64_b32 v[72:73], v72 offset1:13\n"
        "ds_read2st64_b32 v[74:75], v74 offset1:13\n"
        "ds_read2st64_b32 v[76:77], v76 offset1:13\n"
        "buffer_load_dwordx4 v4, s[20:23], s46 offen sc1 lds\n"
        "v_fmamk_f32 v78, v107, 0x40000000, v106\n"
        "v_lshrrev_b32 v79, 4, v78\n"
        "v_add_u32 v78, v79, v78\n"
        "v_mad_u32_u24 v78, v78, 4, s42\n"
        "v_fmamk_f32 v80, v107, 0x40400000, v106\n"
        "v_lshrrev_b32 v81, 4, v80\n"
        "v_add_u32 v80, v81, v80\n"
        "v_mad_u32_u24 v80, v80, 4, s42\n"
        "v_fmamk_f32 v82, v107, 0x40800000, v106\n"
        "v_lshrrev_b32 v83, 4, v82\n"
        "v_add_u32 v82, v83, v82\n"
        "s_add_u32 m0, s81, 0xc00\n"
        "s_add_u32 s46, s86, 0x140000\n"
        "v_mad_u32_u24 v82, v82, 4, s42\n"
        "v_fmamk_f32 v84, v107, 0x40a00000, v106\n"
        "buffer_load_dwordx4 v4, s[20:23], s46 offen sc1 lds\n"
        "v_lshrrev_b32 v85, 4, v84\n"
        "v_add_u32 v84, v85, v84\n"
        "v_mad_u32_u24 v84, v84, 4, s42\n"
        "ds_read2st64_b32 v[78:79], v78 offset1:13\n"
        "v_mov_b32 v58, -1\n"
        "ds_read2st64_b32 v[80:81], v80 offset1:13\n"
        "v_mov_b32 v59, -1\n"
        "v_mov_b32 v60, -1\n"
        "v_mov_b32 v61, -1\n"
        "v_lshrrev_b32 v49, 3, v0\n"
        "v_mul_u32_u24 v49, 0xa000, v49\n"
        "v_and_b32_e64 v48, 7, v1\n"
        "v_lshl_add_u32 v49, v48, 2, v49\n"
        "v_mad_u32_u24 v48, v48, 12, v49\n"
        "s_lshl_b32 s46, s50, 7\n"
        "s_add_u32 s46, s46, 0x840000\n"
        "ds_read2st64_b32 v[82:83], v82 offset1:13\n"
        "buffer_store_dwordx4 v[58:61], v48, s[20:23], s46 offen sc1\n"
        "s_lshl_b32 s46, s50, 7\n"
        "s_add_u32 s46, s46, 0x980000\n"
        "ds_read2st64_b32 v[84:85], v84 offset1:13\n"
        "v_fmamk_f32 v86, v107, 0x40c00000, v106\n"
        "v_lshrrev_b32 v87, 4, v86\n"
        "v_add_u32 v86, v87, v86\n"
        "v_mad_u32_u24 v86, v86, 4, s42\n"
        "v_fmamk_f32 v88, v107, 0x40e00000, v106\n"
        "v_lshrrev_b32 v89, 4, v88\n"
        "v_add_u32 v88, v89, v88\n"
        "v_mad_u32_u24 v88, v88, 4, s42\n"
        "v_fmamk_f32 v90, v107, 0x41000000, v106\n"
        "v_lshrrev_b32 v91, 4, v90\n"
        "buffer_store_dwordx4 v[58:61], v48, s[20:23], s46 offen sc1\n"
        "v_add_u32 v90, v91, v90\n"
        "v_mad_u32_u24 v90, v90, 4, s42\n"
        "v_fmamk_f32 v92, v107, 0x41100000, v106\n"
        "v_lshrrev_b32 v93, 4, v92\n"
        "v_add_u32_e64 v92, v93, v92\n"
        "v_mad_u32_u24 v92, v92, 4, s42\n"
        "ds_read2st64_b32 v[86:87], v86 offset1:13\n"
        "ds_read2st64_b32 v[88:89], v88 offset1:13\n"
        "ds_read2st64_b32 v[90:91], v90 offset1:13\n"
        "ds_read2st64_b32 v[92:93], v92 offset1:13\n"
        "v_fmamk_f32 v94, v107, 0x41200000, v106\n"
        "v_lshrrev_b32 v95, 4, v94\n"
        "v_add_u32 v94, v95, v94\n"
        "v_mad_u32_u24 v94, v94, 4, s42\n"
        "v_fmamk_f32 v96, v107, 0x41300000, v106\n"
        "v_lshrrev_b32 v97, 4, v96\n"
        "v_add_u32 v96, v97, v96\n"
        "v_mad_u32_u24 v96, v96, 4, s42\n"
        "v_fmamk_f32 v98, v107, 0x41400000, v106\n"
        "v_lshrrev_b32 v99, 4, v98\n"
        "v_add_u32 v98, v99, v98\n"
        "v_mad_u32_u24 v98, v98, 4, s42\n"
        "v_fmamk_f32 v100, v107, 0x41500000, v106\n"
        "v_lshrrev_b32 v101, 4, v100\n"
        "v_add_u32 v100, v101, v100\n"
        "v_mad_u32_u24 v100, v100, 4, s42\n"
        "ds_read2st64_b32 v[94:95], v94 offset1:13\n"
        "ds_read2st64_b32 v[96:97], v96 offset1:13\n"
        "ds_read2st64_b32 v[98:99], v98 offset1:13\n"
        "ds_read2st64_b32 v[100:101], v100 offset1:13\n"
        "v_fmamk_f32 v102, v107, 0x41600000, v106\n"
        "v_lshrrev_b32 v103, 4, v102\n"
        "v_add_u32 v102, v103, v102\n"
        "v_mad_u32_u24 v102, v102, 4, s42\n"
        "v_fmamk_f32 v104, v107, 0x41700000, v106\n"
        "v_lshrrev_b32 v105, 4, v104\n"
        "v_add_u32 v104, v105, v104\n"
        "v_mad_u32_u24 v104, v104, 4, s42\n"
        "ds_read2st64_b32 v[102:103], v102 offset1:13\n"
        "ds_read2st64_b32 v[104:105], v104 offset1:13\n"
        "s_mul_i32 s42, s41, 0x49a0\n"
        "s_add_u32 s42, s42, 0x14a10\n"
        "v_add_u32 v57, s42, v57\n"
        "v_mov_b32 v52, 0\n"
        "v_mov_b32 v53, 0\n"
        "v_mov_b32 v54, 0\n"
        "v_mov_b32 v55, 0\n"
        "v_min_u32 v56, 17, v1\n"
        "v_lshrrev_b32 v106, 1, v56\n"
        "v_and_b32 v56, 1, v56\n"
        "v_lshl_add_u32 v56, v56, 3, s42\n"
        "s_mov_b32 s40, 0x820\n"
        "v_mad_u32_u24 v56, v106, s40, v56\n"
        "v_and_b32 v106, 8, v106\n"
        "v_mad_u32_u24 v56, v106, 17, v56\n"
        "v_subrev_u32 v56, 16, v56\n"
        "s_waitcnt lgkmcnt(1)\n"
        "v_add_f32 v62, v62, v63\n"
        "v_add_f32 v64, v64, v65\n"
        "v_add_f32 v66, v66, v67\n"
        "v_add_f32 v68, v68, v69\n"
        "v_add_f32 v70, v70, v71\n"
        "v_add_f32 v72, v72, v73\n"
        "v_add_f32 v74, v74, v75\n"
        "v_add_f32 v76, v76, v77\n"
        "v_add_f32 v78, v78, v79\n"
        "v_add_f32 v80, v80, v81\n"
        "v_add_f32 v82, v82, v83\n"
        "v_add_f32 v84, v84, v85\n"
        "v_add_f32 v86, v86, v87\n"
        "v_add_f32 v88, v88, v89\n"
        "v_add_f32 v90, v90, v91\n"
        "v_add_f32 v92, v92, v93\n"
        "v_add_f32 v94, v94, v95\n"
        "v_add_f32 v96, v96, v97\n"
        "v_add_f32 v98, v98, v99\n"
        "v_add_f32 v100, v100, v101\n"
        "v_add_f32 v102, v102, v103\n"
        "s_waitcnt lgkmcnt(0)\n"
        "v_add_f32 v104, v104, v105\n"
        "ds_write_b64 v56, v[52:53]\n"
        "v_cvt_pk_bf16_f32 v110, v74, v78\n"
        "v_cvt_pk_bf16_f32 v111, v82, v86\n"
        "v_cvt_pk_bf16_f32 v112, v90, v94\n"
        "v_cvt_pk_bf16_f32 v113, v98, v102\n"
        "v_cvt_pk_bf16_f32 v114, v70, v74\n"
        "v_cvt_pk_bf16_f32 v115, v78, v82\n"
        "v_cvt_pk_bf16_f32 v116, v86, v90\n"
        "v_cvt_pk_bf16_f32 v117, v94, v98\n"
        "ds_write_b128 v57, v[110:113]\n"
        "ds_write_b128 v57, v[114:117] offset:2080\n"
        "v_cvt_pk_bf16_f32 v110, v66, v70\n"
        "v_cvt_pk_bf16_f32 v111, v74, v78\n"
        "v_cvt_pk_bf16_f32 v112, v82, v86\n"
        "v_cvt_pk_bf16_f32 v113, v90, v94\n"
        "v_cvt_pk_bf16_f32 v114, v62, v66\n"
        "v_cvt_pk_bf16_f32 v115, v70, v74\n"
        "v_cvt_pk_bf16_f32 v116, v78, v82\n"
        "v_cvt_pk_bf16_f32 v117, v86, v90\n"
        "ds_write_b128 v57, v[110:113] offset:4160\n"
        "ds_write_b128 v57, v[114:117] offset:6240\n"
        "v_cvt_pk_bf16_f32 v110, v76, v80\n"
        "v_cvt_pk_bf16_f32 v111, v84, v88\n"
        "v_cvt_pk_bf16_f32 v112, v92, v96\n"
        "v_cvt_pk_bf16_f32 v113, v100, v104\n"
        "v_cvt_pk_bf16_f32 v114, v72, v76\n"
        "v_cvt_pk_bf16_f32 v115, v80, v84\n"
        "v_cvt_pk_bf16_f32 v116, v88, v92\n"
        "v_cvt_pk_bf16_f32 v117, v96, v100\n"
        "ds_write_b128 v57, v[110:113] offset:8320\n"
        "ds_write_b64 v57, v[110:111] offset:16776\n"
        "ds_write_b64 v57, v[112:113] offset:16784\n"
        "ds_write_b128 v57, v[114:117] offset:10400\n"
        "v_cvt_pk_bf16_f32 v110, v68, v72\n"
        "v_cvt_pk_bf16_f32 v111, v76, v80\n"
        "v_cvt_pk_bf16_f32 v112, v84, v88\n"
        "v_cvt_pk_bf16_f32 v113, v92, v96\n"
        "v_cvt_pk_bf16_f32 v114, v64, v68\n"
        "v_cvt_pk_bf16_f32 v115, v72, v76\n"
        "v_cvt_pk_bf16_f32 v116, v80, v84\n"
        "v_cvt_pk_bf16_f32 v117, v88, v92\n"
        "ds_write_b128 v57, v[110:113] offset:12480\n"
        "ds_write_b128 v57, v[114:117] offset:14560\n"
        "v_mul_u32_u24 v56, 0x900, v3\n"
        "v_lshl_add_u32 v56, v2, 4, v56\n"
        "v_add_u32 v56, 0x24c00, v56\n"
        "s_mov_b32 s43, 0xffff0000\n"
        "v_lshlrev_b32 v158, 16, v186\n"
        "v_and_b32 v159, s43, v186\n"
        "v_lshlrev_b32 v160, 16, v187\n"
        "v_and_b32 v161, s43, v187\n"
        "v_lshlrev_b32 v162, 16, v188\n"
        "v_and_b32 v163, s43, v188\n"
        "v_lshlrev_b32 v164, 16, v189\n"
        "v_and_b32 v165, s43, v189\n"
        "s_waitcnt vmcnt(11)\n"
        "v_cmp_ne_u64 vcc, v[46:47], s[62:63]\n"
        "s_cbranch_vccnz L_slow_flag0_%=\n"
        "L_ret_flag0_%=:\n"
        "s_waitcnt lgkmcnt(0)\n"
        "s_waitcnt vmcnt(2)\n"
        "s_lshl_b32 s43, s51, 12\n"
        "s_add_u32 s43, s43, 0\n"
        "v_add_u32_e64 v139, s43, v4\n"
        "ds_read_b128 v[142:145], v139\n"
        "ds_read_b128 v[146:149], v139 offset:1024\n"
        "ds_read_b128 v[150:153], v139 offset:2048\n"
        "ds_read_b128 v[154:157], v139 offset:3072\n"
        "s_waitcnt lgkmcnt(3)\n"
        "v_max3_u32 v138, v142, v143, v144\n"
        "s_waitcnt lgkmcnt(2)\n"
        "v_max3_u32 v138, v138, v145, v146\n"
        "v_max3_u32 v138, v138, v147, v148\n"
        "s_waitcnt lgkmcnt(1)\n"
        "v_max3_u32 v138, v138, v149, v150\n"
        "v_max3_u32 v138, v138, v151, v152\n"
        "s_waitcnt lgkmcnt(0)\n"
        "v_max3_u32 v138, v138, v153, v154\n"
        "v_max3_u32 v138, v138, v155, v156\n"
        "v_max_u32 v138, v138, v157\n"
        "v_cmp_eq_u32 vcc, -1, v138\n"
        "s_cbranch_vccnz L_slow_o01_%=\n"
        "L_ret_o01_%=:\n"
        "s_barrier\n"
        "ds_read_b128 v[126:129], v4\n"
        "s_add_u32 s92, s34, 0x340000\n"
        "s_add_u32 s93, s34, 0x341000\n"
        "s_add_u32 s94, s34, 0x348000\n"
        "ds_read_b128 v[130:133], v4 offset:1024\n"
        "s_add_u32 s95, s34, 0x349000\n"
        "s_lshl_b32 s48, s51, 8\n"
        "s_add_u32 s48, s48, 0x27400\n"
        "ds_read_b128 v[134:137], v4 offset:2048\n"
        "v_lshl_add_u32 v167, v1, 2, s48\n"
        "s_mov_b32 s48, 0x27400\n"
        "v_lshl_add_u32 v166, v1, 2, s48\n"
        "s_add_u32 s40, s35, 0x18000\n"
        "ds_read_b128 v[138:141], v4 offset:3072\n"
        "L_pg4_%=:\n"
        "s_cbranch_execz L_pg8_%=\n"
        "ds_read_b128 v[142:145], v4 offset:4096\n"
        "s_waitcnt lgkmcnt(4)\n"
        "v_mfma_f32_16x16x32_bf16 v[158:161], a[192:195], v[126:129], v[158:161]\n"
        "ds_read_b128 v[146:149], v4 offset:5120\n"
        "v_mfma_f32_16x16x32_bf16 v[162:165], a[196:199], v[126:129], v[162:165]\n"
        "s_waitcnt lgkmcnt(4)\n"
        "ds_read_b128 v[150:153], v4 offset:6144\n"
        "v_mfma_f32_16x16x32_bf16 v[158:161], a[200:203], v[130:133], v[158:161]\n"
        "ds_read_b128 v[154:157], v4 offset:7168\n"
        "v_mfma_f32_16x16x32_bf16 v[162:165], a[204:207], v[130:133], v[162:165]\n"
        "s_waitcnt lgkmcnt(4)\n"
        "ds_read_b128 v[126:129], v4 offset:8192\n"
        "s_mov_b32 s46, 0x10000\n"
        "v_mfma_f32_16x16x32_bf16 v[158:161], a[208:211], v[134:137], v[158:161]\n"
        "ds_read_b128 v[130:133], v4 offset:9216\n"
        "buffer_load_dwordx4 v[62:65], v19, s[20:23], s46 offen sc1\n"
        "v_mfma_f32_16x16x32_bf16 v[162:165], a[212:215], v[134:137], v[162:165]\n"
        "s_mov_b32 s46, 0x11000\n"
        "ds_read_b128 v[134:137], v4 offset:10240\n"
        "v_mfma_f32_16x16x32_bf16 v[158:161], a[216:219], v[138:141], v[158:161]\n"
        "ds_write_b128 v56, v[52:55]\n"
        "v_mfma_f32_16x16x32_bf16 v[162:165], a[220:223], v[138:141], v[162:165]\n"
        "s_waitcnt lgkmcnt(4)\n"
        "ds_read_b128 v[138:141], v4 offset:11264\n"
        "v_and_b32_e64 v48, 3, v1\n"
        "v_mfma_f32_16x16x32_bf16 v[158:161], a[224:227], v[142:145], v[158:161]\n"
        "ds_read_b64_tr_b16 v[94:95], v38 offset:8\n"
        "buffer_load_dwordx4 v[66:69], v19, s[20:23], s46 offen sc1\n"
        "v_mfma_f32_16x16x32_bf16 v[162:165], a[228:231], v[142:145], v[162:165]\n"
        "s_mov_b32 s46, 0x12000\n"
        "ds_read_b128 v[142:145], v4 offset:12288\n"
        "v_lshlrev_b32_e64 v48, 3, v48\n"
        "v_mfma_f32_16x16x32_bf16 v[158:161], a[232:235], v[146:149], v[158:161]\n"
        "ds_read_b64_tr_b16 v[96:97], v38\n"
        "v_bfe_u32 v49, v1, 2, 2\n"
        "v_mfma_f32_16x16x32_bf16 v[162:165], a[236:239], v[146:149], v[162:165]\n"
        "ds_read_b128 v[146:149], v4 offset:13312\n"
        "v_lshl_add_u32 v48, v49, 6, v48\n"
        "v_mfma_f32_16x16x32_bf16 v[158:161], a[240:243], v[150:153], v[158:161]\n"
        "ds_read_b64_tr_b16 v[98:99], v38 offset:264\n"
        "buffer_load_dwordx4 v[70:73], v19, s[20:23], s46 offen sc1\n"
        "v_mfma_f32_16x16x32_bf16 v[162:165], a[244:247], v[150:153], v[162:165]\n"
        "s_mov_b32 s46, 0x13000\n"
        "ds_read_b128 v[150:153], v4 offset:14336\n"
        "v_lshl_add_u32 v32, v3, 9, v48\n"
        "v_mfma_f32_16x16x32_bf16 v[158:161], a[248:251], v[154:157], v[158:161]\n"
        "ds_read_b64_tr_b16 v[100:101], v38 offset:256\n"
        "v_and_b32 v33, 16, v1\n"
        "v_mfma_f32_16x16x32_bf16 v[162:165], a[252:255], v[154:157], v[162:165]\n"
        "s_waitcnt lgkmcnt(5)\n"
        "ds_read_b128 v[154:157], v4 offset:15360\n"
        "v_lshl_add_u32 v32, v33, 1, v32\n"
        "v_mfma_f32_16x16x32_bf16 v[158:161], v[192:195], v[126:129], v[158:161]\n"
        "ds_read_b64_tr_b16 v[102:103], v38 offset:520\n"
        "buffer_load_dwordx4 v[74:77], v19, s[20:23], s46 offen sc1\n"
        "v_mfma_f32_16x16x32_bf16 v[162:165], v[196:199], v[126:129], v[162:165]\n"
        "s_mov_b32 s46, 0x14000\n"
        "ds_read_b64_tr_b16 v[104:105], v38 offset:512\n"
        "v_xor_b32 v33, 32, v32\n"
        "v_mfma_f32_16x16x32_bf16 v[158:161], v[200:203], v[130:133], v[158:161]\n"
        "ds_read_b64_tr_b16 v[106:107], v38 offset:776\n"
        "v_lshlrev_b32 v5, 7, v3\n"
        "v_mfma_f32_16x16x32_bf16 v[162:165], v[204:207], v[130:133], v[162:165]\n"
        "ds_read_b64_tr_b16 v[108:109], v38 offset:768\n"
        "v_lshl_add_u32 v5, v49, 4, v5\n"
        "v_mfma_f32_16x16x32_bf16 v[158:161], v[208:211], v[134:137], v[158:161]\n"
        "ds_read_b64_tr_b16 v[110:111], v38 offset:72\n"
        "buffer_load_dwordx4 v[78:81], v19, s[20:23], s46 offen sc1\n"
        "v_mfma_f32_16x16x32_bf16 v[162:165], v[212:215], v[134:137], v[162:165]\n"
        "s_mov_b32 s46, 0x15000\n"
        "ds_read_b64_tr_b16 v[112:113], v38 offset:64\n"
        "v_and_b32_e64 v49, 1, v1\n"
        "v_mfma_f32_16x16x32_bf16 v[158:161], v[216:219], v[138:141], v[158:161]\n"
        "ds_read_b64_tr_b16 v[114:115], v38 offset:328\n"
        "v_lshl_add_u32 v5, v49, 3, v5\n"
        "v_mfma_f32_16x16x32_bf16 v[162:165], v[220:223], v[138:141], v[162:165]\n"
        "ds_read_b64_tr_b16 v[116:117], v38 offset:320\n"
        "v_add_u32 v5, 0x8000, v5\n"
        "v_mfma_f32_16x16x32_bf16 v[158:161], v[224:227], v[142:145], v[158:161]\n"
        "ds_read_b64_tr_b16 v[118:119], v38 offset:584\n"
        "buffer_load_dwordx4 v[82:85], v19, s[20:23], s46 offen sc1\n"
        "v_mfma_f32_16x16x32_bf16 v[162:165], v[228:231], v[142:145], v[162:165]\n"
        "s_waitcnt lgkmcnt(5)\n"
        "ds_read_b64_tr_b16 v[120:121], v38 offset:576\n"
        "v_lshl_add_u32 v35, v3, 8, v48\n"
        "v_mfma_f32_16x16x32_bf16 v[158:161], v[232:235], v[146:149], v[158:161]\n"
        "s_mov_b32 s46, 0x16000\n"
        "ds_read_b64_tr_b16 v[122:123], v38 offset:840\n"
        "v_and_b32 v49, 32, v1\n"
        "v_mfma_f32_16x16x32_bf16 v[162:165], v[236:239], v[146:149], v[162:165]\n"
        "s_lshl_b32 s44, s57, 5\n"
        "ds_read_b64_tr_b16 v[124:125], v38 offset:832\n"
        "v_xor_b32 v49, s44, v49\n"
        "v_mfma_f32_16x16x32_bf16 v[158:161], v[240:243], v[150:153], v[158:161]\n"
        "s_lshl_b32 s44, s75, 11\n"
        "v_add3_u32 v35, v35, v49, s44\n"
        "s_lshl_b32 s44, s56, 9\n"
        "v_mfma_f32_16x16x32_bf16 v[162:165], v[244:247], v[150:153], v[162:165]\n"
        "s_add_u32 s44, s44, s79\n"
        "buffer_load_dwordx4 v[86:89], v19, s[20:23], s46 offen sc1\n"
        "v_add_u32 v43, s44, v4\n"
        "v_mfma_f32_16x16x32_bf16 v[158:161], v[248:251], v[154:157], v[158:161]\n"
        "s_mov_b32 s46, 0x17000\n"
        "v_add_u32 v44, 0x400, v43\n"
        "v_and_b32 v43, 0x1fff, v43\n"
        "v_mfma_f32_16x16x32_bf16 v[162:165], v[252:255], v[154:157], v[162:165]\n"
        "v_and_b32 v44, 0x1fff, v44\n"
        "v_and_b32 v48, 8, v1\n"
        "v_lshlrev_b32 v48, 2, v48\n"
        "v_xor_b32_e64 v48, 48, v48\n"
        "v_lshl_add_u32 v43, v43, 2, v48\n"
        "v_lshl_add_u32 v44, v44, 2, v48\n"
        "buffer_load_dwordx4 v[90:93], v19, s[20:23], s46 offen sc1\n"
        "s_lshl_b32 s46, s50, 12\n"
        "s_add_u32 s46, s46, 0x240000\n"
        "buffer_load_dwordx4 v[58:61], v19, s[20:23], s46 offen sc1\n"
        "v_pk_add_f32 v[130:131], v[158:159], v[160:161]\n"
        "v_pk_mul_f32 v[132:133], v[158:159], v[158:159]\n"
        "v_pk_add_f32 v[130:131], v[130:131], v[162:163]\n"
        "v_pk_fma_f32 v[132:133], v[160:161], v[160:161], v[132:133]\n"
        "v_pk_add_f32 v[130:131], v[130:131], v[164:165]\n"
        "v_pk_fma_f32 v[132:133], v[162:163], v[162:163], v[132:133]\n"
        "v_add_f32 v130, v130, v131\n"
        "v_pk_fma_f32 v[132:133], v[164:165], v[164:165], v[132:133]\n"
        "v_add_f32 v131, v132, v133\n"
        "v_cvt_pk_bf16_f32 v126, v158, v159\n"
        "v_cvt_pk_bf16_f32 v127, v160, v161\n"
        "v_permlane32_swap_b32 v130, v131\n"
        "v_add_f32 v130, v130, v131\n"
        "v_cvt_pk_bf16_f32 v128, v162, v163\n"
        "v_cvt_pk_bf16_f32 v129, v164, v165\n"
        "s_waitcnt vmcnt(9)\n"
        "ds_write_b32 v167, v130\n"
        "buffer_store_dword v126, v36, s[20:23], s92 offen sc1\n"
        "buffer_store_dword v127, v36, s[20:23], s93 offen sc1\n"
        "buffer_store_dword v128, v36, s[20:23], s94 offen sc1\n"
        "buffer_store_dword v129, v36, s[20:23], s95 offen sc1\n"
        "s_waitcnt lgkmcnt(0)\n"
        "s_barrier\n"
        "ds_read2st64_b32 v[134:135], v166 offset1:1\n"
        "ds_read2st64_b32 v[136:137], v166 offset0:2 offset1:3\n"
        "s_waitcnt lgkmcnt(0)\n"
        "v_pk_add_f32 v[134:135], v[134:135], v[136:137]\n"
        "v_add_f32 v136, v134, v135\n"
        "v_add_f32 v134, v134, v135\n"
        "s_nop 1\n"
        "v_permlane16_swap_b32 v134, v136\n"
        "v_add_f32 v134, v134, v136\n"
        "buffer_store_dword v134, v37, s[20:23], s40 offen sc1\n"
        "L_re_p300_%=:\n"
        "s_waitcnt vmcnt(11)\n"
        "v_pk_add_f32 v[62:63], v[62:63], v[70:71]\n"
        "v_pk_add_f32 v[64:65], v[64:65], v[72:73]\n"
        "s_waitcnt vmcnt(10)\n"
        "v_pk_add_f32 v[66:67], v[66:67], v[74:75]\n"
        "v_pk_add_f32 v[68:69], v[68:69], v[76:77]\n"
        "s_waitcnt vmcnt(7)\n"
        "v_pk_add_f32 v[78:79], v[78:79], v[86:87]\n"
        "v_pk_add_f32 v[80:81], v[80:81], v[88:89]\n"
        "s_waitcnt vmcnt(6)\n"
        "v_pk_add_f32 v[82:83], v[82:83], v[90:91]\n"
        "v_pk_add_f32 v[84:85], v[84:85], v[92:93]\n"
        "v_pk_add_f32 v[62:63], v[62:63], v[78:79]\n"
        "v_pk_add_f32 v[64:65], v[64:65], v[80:81]\n"
        "v_pk_add_f32 v[66:67], v[66:67], v[82:83]\n"
        "v_pk_add_f32 v[68:69], v[68:69], v[84:85]\n"
        "v_pk_mul_f32 v[62:63], v[62:63], s[60:61] op_sel_hi:[1,0]\n"
        "v_pk_fma_f32 v[66:67], v[66:67], s[60:61], s[60:61] op_sel:[0,0,1] op_sel_hi:[1,0,1]\n"
        "v_pk_mul_f32 v[64:65], v[64:65], s[60:61] op_sel_hi:[1,0]\n"
        "v_pk_fma_f32 v[68:69], v[68:69], s[60:61], s[60:61] op_sel:[0,0,1] op_sel_hi:[1,0,1]\n"
        "v_pk_fma_f32 v[66:67], v[62:63], v[62:63], v[66:67] neg_lo:[1,0,0] neg_hi:[1,0,0]\n"
        "v_pk_fma_f32 v[68:69], v[64:65], v[64:65], v[68:69] neg_lo:[1,0,0] neg_hi:[1,0,0]\n"
        "v_maximum3_f32 v66, s61, v66, v66\n"
        "v_maximum3_f32 v67, s61, v67, v67\n"
        "v_maximum3_f32 v68, s61, v68, v68\n"
        "v_maximum3_f32 v69, s61, v69, v69\n"
        "v_rsq_f32 v66, v66\n"
        "v_rsq_f32 v67, v67\n"
        "v_rsq_f32 v68, v68\n"
        "v_rsq_f32 v69, v69\n"
        "s_mov_b32 s40, 0xffff0000\n"
        "s_waitcnt vmcnt(5)\n"
        "v_lshlrev_b32 v46, 16, v58\n"
        "v_and_b32 v50, s40, v58\n"
        "v_lshlrev_b32 v47, 16, v59\n"
        "v_and_b32 v51, s40, v59\n"
        "v_lshlrev_b32 v48, 16, v60\n"
        "v_and_b32 v52, s40, v60\n"
        "v_lshlrev_b32 v49, 16, v61\n"
        "v_and_b32_e64 v53, s40, v61\n"
        "v_pk_mul_f32 v[62:63], v[62:63], v[66:67] neg_lo:[1,0] neg_hi:[1,0]\n"
        "v_pk_mul_f32 v[64:65], v[64:65], v[68:69] neg_lo:[1,0] neg_hi:[1,0]\n"
        "v_pk_fma_f32 v[78:79], v[46:47], v[66:67], v[62:63]\n"
        "v_pk_fma_f32 v[80:81], v[48:49], v[68:69], v[64:65]\n"
        "v_pk_add_f32 v[70:71], v[78:79], v[80:81]\n"
        "v_cmp_u_f32_e64 vcc, v70, v71\n"
        "v_pk_fma_f32 v[82:83], v[50:51], v[66:67], v[62:63]\n"
        "v_pk_fma_f32 v[84:85], v[52:53], v[68:69], v[64:65]\n"
        "v_cvt_pk_bf16_f32 v86, v78, v79\n"
        "v_cvt_pk_bf16_f32 v87, v80, v81\n"
        "v_cvt_pk_bf16_f32 v88, v82, v83\n"
        "v_cvt_pk_bf16_f32 v89, v84, v85\n"
        "s_cbranch_vccnz L_slow_p300_%=\n"
        "L_ret_p300_%=:\n"
        "ds_write_b64 v20, v[86:87]\n"
        "ds_write_b64 v20, v[88:89] offset:4608\n"
        "v_pk_fma_f32 v[46:47], v[78:79], s[72:73], v[46:47] op_sel_hi:[1,0,1]\n"
        "v_pk_fma_f32 v[48:49], v[80:81], s[72:73], v[48:49] op_sel_hi:[1,0,1]\n"
        "v_pk_fma_f32 v[50:51], v[82:83], s[72:73], v[50:51] op_sel:[0,1,0]\n"
        "v_pk_fma_f32 v[52:53], v[84:85], s[72:73], v[52:53] op_sel:[0,1,0]\n"
        "s_waitcnt lgkmcnt(0)\n"
        "s_barrier\n"
        "ds_read_b128 v[86:89], v21 offset:192\n"
        "v_mov_b32 v178, s62\n"
        "v_mov_b32 v179, s63\n"
        "v_or_b32 v180, s50, v0\n"
        "v_lshlrev_b32_e64 v180, 24, v180\n"
        "ds_read_b128 v[90:93], v21 offset:448\n"
        "buffer_store_dwordx2 v[178:179], v180, s[20:23], 0 offen sc1\n"
        "ds_read_b128 v[126:129], v21 offset:704\n"
        "ds_read_b128 v[130:133], v21 offset:960\n"
        "ds_read_b128 v[134:137], v21 offset:128\n"
        "s_waitcnt lgkmcnt(4)\n"
        "v_mfma_f32_16x16x32_bf16 v[54:57], v[94:97], v[86:89], 0\n"
        "ds_read_b128 v[138:141], v21 offset:384\n"
        "v_mfma_f32_16x16x32_bf16 v[58:61], v[98:101], v[86:89], 0\n"
        "v_mfma_f32_16x16x32_bf16 v[62:65], v[102:105], v[86:89], 0\n"
        "v_mfma_f32_16x16x32_bf16 v[66:69], v[106:109], v[86:89], 0\n"
        "s_waitcnt lgkmcnt(3)\n"
        "ds_read_b128 v[86:89], v21 offset:640\n"
        "v_mfma_f32_16x16x32_bf16 v[58:61], v[94:97], v[90:93], v[58:61]\n"
        "v_mfma_f32_16x16x32_bf16 v[62:65], v[98:101], v[90:93], v[62:65]\n"
        "v_mfma_f32_16x16x32_bf16 v[66:69], v[102:105], v[90:93], v[66:69]\n"
        "ds_read_b128 v[90:93], v21 offset:896\n"
        "v_mfma_f32_16x16x32_bf16 v[62:65], v[94:97], v[126:129], v[62:65]\n"
        "ds_read_b64_tr_b16 v[106:107], v38 offset:136\n"
        "v_mfma_f32_16x16x32_bf16 v[66:69], v[98:101], v[126:129], v[66:69]\n"
        "s_waitcnt lgkmcnt(3)\n"
        "ds_read_b64_tr_b16 v[108:109], v38 offset:128\n"
        "v_mfma_f32_16x16x32_bf16 v[66:69], v[94:97], v[130:133], v[66:69]\n"
        "ds_read_b128 v[126:129], v21 offset:64\n"
        "v_mfma_f32_16x16x32_bf16 v[54:57], v[110:113], v[134:137], v[54:57]\n"
        "ds_read_b64_tr_b16 v[102:103], v38 offset:392\n"
        "v_mfma_f32_16x16x32_bf16 v[58:61], v[114:117], v[134:137], v[58:61]\n"
        "ds_read_b64_tr_b16 v[104:105], v38 offset:384\n"
        "v_mfma_f32_16x16x32_bf16 v[62:65], v[118:121], v[134:137], v[62:65]\n"
        "ds_read_b64_tr_b16 v[98:99], v38 offset:648\n"
        "v_mfma_f32_16x16x32_bf16 v[66:69], v[122:125], v[134:137], v[66:69]\n"
        "ds_read_b64_tr_b16 v[100:101], v38 offset:640\n"
        "v_mfma_f32_16x16x32_bf16 v[58:61], v[110:113], v[138:141], v[58:61]\n"
        "ds_read_b64_tr_b16 v[94:95], v38 offset:904\n"
        "v_mfma_f32_16x16x32_bf16 v[62:65], v[114:117], v[138:141], v[62:65]\n"
        "ds_read_b64_tr_b16 v[96:97], v38 offset:896\n"
        "v_mfma_f32_16x16x32_bf16 v[66:69], v[118:121], v[138:141], v[66:69]\n"
        "s_waitcnt lgkmcnt(5)\n"
        "ds_read_b128 v[130:133], v21 offset:320\n"
        "v_mfma_f32_16x16x32_bf16 v[62:65], v[110:113], v[86:89], v[62:65]\n"
        "ds_read_b128 v[134:137], v21 offset:576\n"
        "v_mfma_f32_16x16x32_bf16 v[66:69], v[114:117], v[86:89], v[66:69]\n"
        "ds_read_b128 v[138:141], v21 offset:832\n"
        "v_mfma_f32_16x16x32_bf16 v[66:69], v[110:113], v[90:93], v[66:69]\n"
        "ds_read_b64_tr_b16 v[122:123], v38 offset:200\n"
        "v_mfma_f32_16x16x32_bf16 v[54:57], v[106:109], v[126:129], v[54:57]\n"
        "s_waitcnt lgkmcnt(5)\n"
        "ds_read_b64_tr_b16 v[124:125], v38 offset:192\n"
        "v_mfma_f32_16x16x32_bf16 v[58:61], v[102:105], v[126:129], v[58:61]\n"
        "ds_read_b128 v[86:89], v21\n"
        "v_mfma_f32_16x16x32_bf16 v[62:65], v[98:101], v[126:129], v[62:65]\n"
        "s_waitcnt lgkmcnt(5)\n"
        "ds_read_b64_tr_b16 v[118:119], v38 offset:456\n"
        "v_mfma_f32_16x16x32_bf16 v[66:69], v[94:97], v[126:129], v[66:69]\n"
        "ds_read_b64_tr_b16 v[120:121], v38 offset:448\n"
        "v_mfma_f32_16x16x32_bf16 v[58:61], v[106:109], v[130:133], v[58:61]\n"
        "ds_read_b128 v[90:93], v21 offset:256\n"
        "v_mfma_f32_16x16x32_bf16 v[62:65], v[102:105], v[130:133], v[62:65]\n"
        "ds_read_b64_tr_b16 v[114:115], v38 offset:712\n"
        "v_mfma_f32_16x16x32_bf16 v[66:69], v[98:101], v[130:133], v[66:69]\n"
        "s_waitcnt lgkmcnt(5)\n"
        "ds_read_b64_tr_b16 v[116:117], v38 offset:704\n"
        "v_mfma_f32_16x16x32_bf16 v[62:65], v[106:109], v[134:137], v[62:65]\n"
        "ds_read_b128 v[126:129], v21 offset:512\n"
        "v_mfma_f32_16x16x32_bf16 v[66:69], v[102:105], v[134:137], v[66:69]\n"
        "ds_read_b64_tr_b16 v[110:111], v38 offset:968\n"
        "v_mfma_f32_16x16x32_bf16 v[66:69], v[106:109], v[138:141], v[66:69]\n"
        "s_waitcnt lgkmcnt(5)\n"
        "ds_read_b64_tr_b16 v[112:113], v38 offset:960\n"
        "v_mfma_f32_16x16x32_bf16 v[54:57], v[122:125], v[86:89], v[54:57]\n"
        "ds_read_b128 v[130:133], v21 offset:768\n"
        "v_mfma_f32_16x16x32_bf16 v[58:61], v[118:121], v[86:89], v[58:61]\n"
        "s_waitcnt lgkmcnt(5)\n"
        "ds_read_b64_tr_b16 v[94:95], v38 offset:264\n"
        "v_mfma_f32_16x16x32_bf16 v[58:61], v[122:125], v[90:93], v[58:61]\n"
        "s_waitcnt lgkmcnt(5)\n"
        "ds_read_b64_tr_b16 v[96:97], v38 offset:256\n"
        "s_nop 0\n"
        "ds_write_b128 v39, v[54:57]\n"
        "v_mfma_f32_16x16x32_bf16 v[62:65], v[114:117], v[86:89], v[62:65]\n"
        "ds_read_b128 v[134:137], v21 offset:960\n"
        "v_mfma_f32_16x16x32_bf16 v[62:65], v[118:121], v[90:93], v[62:65]\n"
        "s_waitcnt lgkmcnt(5)\n"
        "ds_read_b64_tr_b16 v[98:99], v38 offset:520\n"
        "ds_write_b128 v39, v[58:61] offset:1024\n"
        "v_mfma_f32_16x16x32_bf16 v[62:65], v[122:125], v[126:129], v[62:65]\n"
        "ds_read_b64_tr_b16 v[100:101], v38 offset:512\n"
        "v_mfma_f32_16x16x32_bf16 v[66:69], v[110:113], v[86:89], v[66:69]\n"
        "ds_read_b128 v[138:141], v21 offset:704\n"
        "v_mfma_f32_16x16x32_bf16 v[66:69], v[114:117], v[90:93], v[66:69]\n"
        "ds_read_b64_tr_b16 v[102:103], v38 offset:776\n"
        "v_mfma_f32_16x16x32_bf16 v[66:69], v[118:121], v[126:129], v[66:69]\n"
        "s_waitcnt lgkmcnt(5)\n"
        "ds_read_b64_tr_b16 v[104:105], v38 offset:768\n"
        "ds_write_b128 v39, v[62:65] offset:2048\n"
        "v_mfma_f32_16x16x32_bf16 v[66:69], v[122:125], v[130:133], v[66:69]\n"
        "s_mov_b32 s46, 0x18000\n"
        "ds_read_b128 v[86:89], v21 offset:448\n"
        "buffer_load_dwordx4 v[146:149], v19, s[20:23], s46 offen sc1\n"
        "v_mfma_f32_16x16x32_bf16 v[70:73], v[94:97], v[134:137], 0\n"
        "s_waitcnt lgkmcnt(5)\n"
        "ds_read_b64_tr_b16 v[106:107], v38 offset:1032\n"
        "s_mov_b32 s46, 0x19000\n"
        "v_mfma_f32_16x16x32_bf16 v[74:77], v[98:101], v[134:137], 0\n"
        "s_waitcnt lgkmcnt(5)\n"
        "ds_read_b64_tr_b16 v[108:109], v38 offset:1024\n"
        "ds_write_b128 v39, v[66:69] offset:3072\n"
        "v_mfma_f32_16x16x32_bf16 v[70:73], v[98:101], v[138:141], v[70:73]\n"
        "s_waitcnt lgkmcnt(4)\n"
        "ds_read_b128 v[90:93], v21 offset:192\n"
        "buffer_load_dwordx4 v[150:153], v19, s[20:23], s46 offen sc1\n"
        "v_mfma_f32_16x16x32_bf16 v[78:81], v[102:105], v[134:137], 0\n"
        "s_mov_b32 s46, 0x1a000\n"
        "ds_read_b64_tr_b16 v[110:111], v38 offset:1288\n"
        "v_mfma_f32_16x16x32_bf16 v[74:77], v[102:105], v[138:141], v[74:77]\n"
        "s_waitcnt lgkmcnt(5)\n"
        "ds_read_b64_tr_b16 v[112:113], v38 offset:1280\n"
        "v_mfma_f32_16x16x32_bf16 v[70:73], v[102:105], v[86:89], v[70:73]\n"
        "ds_read_b64_tr_b16 v[114:115], v38 offset:1544\n"
        "s_waitcnt lgkmcnt(5)\n"
        "s_waitcnt lgkmcnt(4)\n"
        "s_barrier\n"
        "ds_read_b128 v[54:57], v40\n"
        "v_mfma_f32_16x16x32_bf16 v[82:85], v[106:109], v[134:137], 0\n"
        "buffer_load_dwordx4 v[154:157], v19, s[20:23], s46 offen sc1\n"
        "ds_read_b64_tr_b16 v[116:117], v38 offset:1536\n"
        "v_mfma_f32_16x16x32_bf16 v[78:81], v[106:109], v[138:141], v[78:81]\n"
        "s_mov_b32 s46, 0x1b000\n"
        "ds_read_b64_tr_b16 v[118:119], v38 offset:1800\n"
        "ds_read_b128 v[58:61], v40 offset:4096\n"
        "v_mfma_f32_16x16x32_bf16 v[74:77], v[106:109], v[86:89], v[74:77]\n"
        "s_waitcnt lgkmcnt(6)\n"
        "ds_read_b64_tr_b16 v[120:121], v38 offset:1792\n"
        "v_mfma_f32_16x16x32_bf16 v[70:73], v[106:109], v[90:93], v[70:73]\n"
        "s_waitcnt lgkmcnt(6)\n"
        "ds_read_b64_tr_b16 v[122:123], v38 offset:328\n"
        "ds_read_b128 v[62:65], v40 offset:8192\n"
        "v_mfma_f32_16x16x32_bf16 v[82:85], v[110:113], v[138:141], v[82:85]\n"
        "buffer_load_dwordx4 v[158:161], v19, s[20:23], s46 offen sc1\n"
        "ds_read_b64_tr_b16 v[124:125], v38 offset:320\n"
        "v_mfma_f32_16x16x32_bf16 v[78:81], v[110:113], v[86:89], v[78:81]\n"
        "s_mov_b32 s46, 0x1c000\n"
        "ds_read_b128 v[126:129], v21 offset:896\n"
        "v_mfma_f32_16x16x32_bf16 v[74:77], v[110:113], v[90:93], v[74:77]\n"
        "s_waitcnt lgkmcnt(6)\n"
        "ds_read_b64_tr_b16 v[94:95], v38 offset:584\n"
        "ds_read_b128 v[66:69], v40 offset:12288\n"
        "s_mov_b32 s91, 0x840000\n"
        "v_add_f32 v46, v46, v54\n"
        "v_add_f32 v47, v47, v55\n"
        "v_add_f32 v48, v48, v56\n"
        "v_add_f32 v49, v49, v57\n"
        "buffer_load_dwordx4 v[162:165], v19, s[20:23], s46 offen sc1\n"
        "v_mfma_f32_16x16x32_bf16 v[82:85], v[114:117], v[86:89], v[82:85]\n"
        "s_waitcnt lgkmcnt(4)\n"
        "ds_read_b64_tr_b16 v[96:97], v38 offset:576\n"
        "v_add_f32 v46, v46, v58\n"
        "v_add_f32 v47, v47, v59\n"
        "v_add_f32 v48, v48, v60\n"
        "v_add_f32 v49, v49, v61\n"
        "v_add_f32 v50, v50, v62\n"
        "v_add_f32 v51, v51, v63\n"
        "v_add_f32 v52, v52, v64\n"
        "v_add_f32 v53, v53, v65\n"
        "s_mov_b32 s46, 0x1d000\n"
        "v_mfma_f32_16x16x32_bf16 v[78:81], v[114:117], v[90:93], v[78:81]\n"
        "s_waitcnt lgkmcnt(1)\n"
        "ds_read_b128 v[130:133], v21 offset:640\n"
        "buffer_load_dwordx4 v[166:169], v19, s[20:23], s46 offen sc1\n"
        "v_add_f32 v50, v50, v66\n"
        "v_add_f32 v51, v51, v67\n"
        "v_add_f32 v52, v52, v68\n"
        "v_add_f32_e64 v53, v53, v69\n"
        "v_cvt_pk_bf16_f32 v54, v46, v47\n"
        "v_cvt_pk_bf16_f32 v55, v48, v49\n"
        "v_cvt_pk_bf16_f32 v56, v50, v51\n"
        "v_cvt_pk_bf16_f32 v57, v52, v53\n"
        "v_mfma_f32_16x16x32_bf16 v[82:85], v[118:121], v[90:93], v[82:85]\n"
        "s_mov_b32 s46, 0x1e000\n"
        "ds_read_b64_tr_b16 v[98:99], v38 offset:840\n"
        "buffer_store_dwordx2 v[54:55], v41, s[20:23], s91 offen sc1\n"
        "buffer_store_dwordx2 v[56:57], v41, s[20:23], s91 offen offset:64 sc1\n"
        "v_mfma_f32_16x16x32_bf16 v[70:73], v[122:125], v[126:129], v[70:73]\n"
        "s_waitcnt lgkmcnt(2)\n"
        "ds_read_b64_tr_b16 v[100:101], v38 offset:832\n"
        "buffer_load_dwordx4 v[170:173], v19, s[20:23], s46 offen sc1\n"
        "v_mfma_f32_16x16x32_bf16 v[74:77], v[94:97], v[126:129], v[74:77]\n"
        "s_mov_b32 s46, 0x1f000\n"
        "ds_read_b128 v[134:137], v21 offset:384\n"
        "s_waitcnt lgkmcnt(3)\n"
        "v_mfma_f32_16x16x32_bf16 v[70:73], v[94:97], v[130:133], v[70:73]\n"
        "ds_read_b64_tr_b16 v[102:103], v38 offset:1096\n"
        "ds_read_b64_tr_b16 v[104:105], v38 offset:1088\n"
        "ds_read_b128 v[138:141], v21 offset:128\n"
        "ds_read_b64_tr_b16 v[106:107], v38 offset:1352\n"
        "buffer_load_dwordx4 v[174:177], v19, s[20:23], s46 offen sc1\n"
        "ds_read_b64_tr_b16 v[108:109], v38 offset:1344\n"
        "s_lshl_b32 s46, s50, 12\n"
        "ds_read_b64_tr_b16 v[110:111], v38 offset:1608\n"
        "s_waitcnt lgkmcnt(7)\n"
        "v_mfma_f32_16x16x32_bf16 v[78:81], v[98:101], v[126:129], v[78:81]\n"
        "s_add_u32 s46, s46, 0x340000\n"
        "ds_read_b64_tr_b16 v[112:113], v38 offset:1600\n"
        "v_mfma_f32_16x16x32_bf16 v[74:77], v[98:101], v[130:133], v[74:77]\n"
        "s_waitcnt lgkmcnt(7)\n"
        "ds_read_b64_tr_b16 v[114:115], v38 offset:1864\n"
        "v_mfma_f32_16x16x32_bf16 v[70:73], v[98:101], v[134:137], v[70:73]\n"
        "s_waitcnt lgkmcnt(6)\n"
        "ds_read_b64_tr_b16 v[116:117], v38 offset:1856\n"
        "buffer_load_dwordx4 v[142:145], v19, s[20:23], s46 offen sc1\n"
        "v_mfma_f32_16x16x32_bf16 v[82:85], v[102:105], v[126:129], v[82:85]\n"
        "ds_read_b64_tr_b16 v[118:119], v38 offset:392\n"
        "v_mov_b32 v62, -1\n"
        "v_mfma_f32_16x16x32_bf16 v[78:81], v[102:105], v[130:133], v[78:81]\n"
        "ds_read_b64_tr_b16 v[120:121], v38 offset:384\n"
        "v_mov_b32 v63, -1\n"
        "v_mfma_f32_16x16x32_bf16 v[74:77], v[102:105], v[134:137], v[74:77]\n"
        "s_waitcnt lgkmcnt(5)\n"
        "ds_read_b128 v[86:89], v21 offset:832\n"
        "v_mov_b32 v64, -1\n"
        "v_mfma_f32_16x16x32_bf16 v[70:73], v[102:105], v[138:141], v[70:73]\n"
        "ds_read_b64_tr_b16 v[122:123], v38 offset:648\n"
        "v_mov_b32 v65, -1\n"
        "v_mfma_f32_16x16x32_bf16 v[82:85], v[106:109], v[130:133], v[82:85]\n"
        "ds_read_b64_tr_b16 v[124:125], v38 offset:640\n"
        "v_lshrrev_b32 v66, 5, v1\n"
        "v_mfma_f32_16x16x32_bf16 v[78:81], v[106:109], v[134:137], v[78:81]\n"
        "ds_read_b128 v[90:93], v21 offset:576\n"
        "v_mul_u32_u24 v66, 0x3e00, v66\n"
        "v_mfma_f32_16x16x32_bf16 v[74:77], v[106:109], v[138:141], v[74:77]\n"
        "s_waitcnt lgkmcnt(5)\n"
        "ds_read_b64_tr_b16 v[94:95], v38 offset:904\n"
        "s_lshl_b32 s46, s51, 9\n"
        "v_mfma_f32_16x16x32_bf16 v[82:85], v[110:113], v[134:137], v[82:85]\n"
        "ds_read_b64_tr_b16 v[96:97], v38 offset:896\n"
        "v_add3_u32 v66, v66, v4, s46\n"
        "v_mfma_f32_16x16x32_bf16 v[78:81], v[110:113], v[138:141], v[78:81]\n"
        "s_add_u32 s46, s82, 0x440000\n"
        "ds_read_b128 v[126:129], v21 offset:320\n"
        "buffer_store_dwordx4 v[62:65], v66, s[20:23], s46 offen sc1\n"
        "v_mfma_f32_16x16x32_bf16 v[82:85], v[114:117], v[138:141], v[82:85]\n"
        "s_waitcnt lgkmcnt(5)\n"
        "ds_read_b64_tr_b16 v[98:99], v38 offset:1160\n"
        "s_add_u32 s46, s82, 0x540000\n"
        "v_mfma_f32_16x16x32_bf16 v[70:73], v[118:121], v[86:89], v[70:73]\n"
        "s_waitcnt lgkmcnt(5)\n"
        "ds_read_b64_tr_b16 v[100:101], v38 offset:1152\n"
        "v_mfma_f32_16x16x32_bf16 v[74:77], v[122:125], v[86:89], v[74:77]\n"
        "s_waitcnt lgkmcnt(5)\n"
        "ds_read_b128 v[130:133], v21 offset:64\n"
        "v_mfma_f32_16x16x32_bf16 v[70:73], v[122:125], v[90:93], v[70:73]\n"
        "ds_read_b64_tr_b16 v[102:103], v38 offset:1416\n"
        "s_waitcnt lgkmcnt(5)\n"
        "v_mfma_f32_16x16x32_bf16 v[78:81], v[94:97], v[86:89], v[78:81]\n"
        "ds_read_b64_tr_b16 v[104:105], v38 offset:1408\n"
        "buffer_store_dwordx4 v[62:65], v66, s[20:23], s46 offen sc1\n"
        "v_mfma_f32_16x16x32_bf16 v[74:77], v[94:97], v[90:93], v[74:77]\n"
        "s_waitcnt lgkmcnt(5)\n"
        "ds_read_b64_tr_b16 v[106:107], v38 offset:1672\n"
        "v_mfma_f32_16x16x32_bf16 v[70:73], v[94:97], v[126:129], v[70:73]\n"
        "ds_read_b64_tr_b16 v[108:109], v38 offset:1664\n"
        "s_waitcnt lgkmcnt(5)\n"
        "v_mfma_f32_16x16x32_bf16 v[82:85], v[98:101], v[86:89], v[82:85]\n"
        "ds_read_b64_tr_b16 v[110:111], v38 offset:1928\n"
        "v_mfma_f32_16x16x32_bf16 v[78:81], v[98:101], v[90:93], v[78:81]\n"
        "ds_read_b64_tr_b16 v[112:113], v38 offset:1920\n"
        "v_mfma_f32_16x16x32_bf16 v[74:77], v[98:101], v[126:129], v[74:77]\n"
        "s_waitcnt lgkmcnt(5)\n"
        "ds_read_b64_tr_b16 v[114:115], v38 offset:456\n"
        "v_mfma_f32_16x16x32_bf16 v[70:73], v[98:101], v[130:133], v[70:73]\n"
        "s_waitcnt lgkmcnt(5)\n"
        "ds_read_b64_tr_b16 v[116:117], v38 offset:448\n"
        "v_mfma_f32_16x16x32_bf16 v[82:85], v[102:105], v[90:93], v[82:85]\n"
        "ds_read_b128 v[134:137], v21 offset:768\n"
        "v_mfma_f32_16x16x32_bf16 v[78:81], v[102:105], v[126:129], v[78:81]\n"
        "ds_read_b64_tr_b16 v[118:119], v38 offset:712\n"
        "v_mfma_f32_16x16x32_bf16 v[74:77], v[102:105], v[130:133], v[74:77]\n"
        "s_waitcnt lgkmcnt(5)\n"
        "ds_read_b64_tr_b16 v[120:121], v38 offset:704\n"
        "v_mfma_f32_16x16x32_bf16 v[82:85], v[106:109], v[126:129], v[82:85]\n"
        "ds_read_b128 v[138:141], v21 offset:512\n"
        "v_mfma_f32_16x16x32_bf16 v[78:81], v[106:109], v[130:133], v[78:81]\n"
        "s_waitcnt lgkmcnt(5)\n"
        "ds_read_b64_tr_b16 v[122:123], v38 offset:968\n"
        "v_mfma_f32_16x16x32_bf16 v[82:85], v[110:113], v[130:133], v[82:85]\n"
        "ds_read_b64_tr_b16 v[124:125], v38 offset:960\n"
        "s_waitcnt lgkmcnt(5)\n"
        "v_mfma_f32_16x16x32_bf16 v[70:73], v[114:117], v[134:137], v[70:73]\n"
        "ds_read_b128 v[86:89], v21 offset:256\n"
        "ds_read_b64_tr_b16 v[94:95], v38 offset:1224\n"
        "s_waitcnt lgkmcnt(5)\n"
        "v_mfma_f32_16x16x32_bf16 v[74:77], v[118:121], v[134:137], v[74:77]\n"
        "ds_read_b64_tr_b16 v[96:97], v38 offset:1216\n"
        "s_waitcnt lgkmcnt(5)\n"
        "v_mfma_f32_16x16x32_bf16 v[70:73], v[118:121], v[138:141], v[70:73]\n"
        "ds_read_b128 v[90:93], v21\n"
        "ds_read_b64_tr_b16 v[98:99], v38 offset:1480\n"
        "s_waitcnt lgkmcnt(5)\n"
        "v_mfma_f32_16x16x32_bf16 v[78:81], v[122:125], v[134:137], v[78:81]\n"
        "ds_read_b64_tr_b16 v[100:101], v38 offset:1472\n"
        "v_mfma_f32_16x16x32_bf16 v[74:77], v[122:125], v[138:141], v[74:77]\n"
        "s_waitcnt lgkmcnt(5)\n"
        "ds_read_b64_tr_b16 v[102:103], v38 offset:1736\n"
        "v_mfma_f32_16x16x32_bf16 v[70:73], v[122:125], v[86:89], v[70:73]\n"
        "ds_read_b64_tr_b16 v[104:105], v38 offset:1728\n"
        "s_waitcnt lgkmcnt(5)\n"
        "v_mfma_f32_16x16x32_bf16 v[82:85], v[94:97], v[134:137], v[82:85]\n"
        "ds_read_b64_tr_b16 v[106:107], v38 offset:1992\n"
        "v_mfma_f32_16x16x32_bf16 v[78:81], v[94:97], v[138:141], v[78:81]\n"
        "ds_read_b64_tr_b16 v[108:109], v38 offset:1984\n"
        "v_mfma_f32_16x16x32_bf16 v[74:77], v[94:97], v[86:89], v[74:77]\n"
        "s_waitcnt lgkmcnt(5)\n"
        "ds_read_b64_tr_b16 v[110:111], v38 offset:8\n"
        "v_mfma_f32_16x16x32_bf16 v[70:73], v[94:97], v[90:93], v[70:73]\n"
        "s_waitcnt lgkmcnt(5)\n"
        "ds_read_b64_tr_b16 v[112:113], v38\n"
        "v_mfma_f32_16x16x32_bf16 v[82:85], v[98:101], v[138:141], v[82:85]\n"
        "ds_read_b64_tr_b16 v[114:115], v38 offset:264\n"
        "v_mfma_f32_16x16x32_bf16 v[78:81], v[98:101], v[86:89], v[78:81]\n"
        "ds_read_b64_tr_b16 v[116:117], v38 offset:256\n"
        "v_mfma_f32_16x16x32_bf16 v[74:77], v[98:101], v[90:93], v[74:77]\n"
        "s_waitcnt lgkmcnt(5)\n"
        "ds_read_b64_tr_b16 v[118:119], v38 offset:520\n"
        "v_mfma_f32_16x16x32_bf16 v[82:85], v[102:105], v[86:89], v[82:85]\n"
        "ds_read_b64_tr_b16 v[120:121], v38 offset:512\n"
        "v_mfma_f32_16x16x32_bf16 v[78:81], v[102:105], v[90:93], v[78:81]\n"
        "s_waitcnt lgkmcnt(5)\n"
        "ds_read_b64_tr_b16 v[122:123], v38 offset:776\n"
        "v_mfma_f32_16x16x32_bf16 v[82:85], v[106:109], v[90:93], v[82:85]\n"
        "ds_read_b64_tr_b16 v[124:125], v38 offset:768\n"
        "ds_read_b64_tr_b16 v[94:95], v38 offset:72\n"
        "ds_read_b64_tr_b16 v[96:97], v38 offset:64\n"
        "ds_read_b64_tr_b16 v[98:99], v38 offset:328\n"
        "ds_read_b64_tr_b16 v[100:101], v38 offset:320\n"
        "ds_read_b64_tr_b16 v[102:103], v38 offset:584\n"
        "ds_read_b64_tr_b16 v[104:105], v38 offset:576\n"
        "ds_read_b64_tr_b16 v[106:107], v38 offset:840\n"
        "ds_read_b64_tr_b16 v[108:109], v38 offset:832\n"
        "L_re_p301_%=:\n"
        "s_waitcnt vmcnt(10)\n"
        "v_pk_add_f32 v[146:147], v[146:147], v[154:155]\n"
        "v_pk_add_f32 v[148:149], v[148:149], v[156:157]\n"
        "s_waitcnt vmcnt(9)\n"
        "v_pk_add_f32 v[150:151], v[150:151], v[158:159]\n"
        "v_pk_add_f32 v[152:153], v[152:153], v[160:161]\n"
        "s_waitcnt vmcnt(4)\n"
        "v_pk_add_f32 v[162:163], v[162:163], v[170:171]\n"
        "v_pk_add_f32 v[164:165], v[164:165], v[172:173]\n"
        "s_waitcnt vmcnt(3)\n"
        "v_pk_add_f32 v[166:167], v[166:167], v[174:175]\n"
        "v_pk_add_f32 v[168:169], v[168:169], v[176:177]\n"
        "v_pk_add_f32 v[146:147], v[146:147], v[162:163]\n"
        "v_pk_add_f32 v[148:149], v[148:149], v[164:165]\n"
        "v_pk_add_f32 v[150:151], v[150:151], v[166:167]\n"
        "v_pk_add_f32 v[152:153], v[152:153], v[168:169]\n"
        "v_pk_mul_f32 v[146:147], v[146:147], s[60:61] op_sel_hi:[1,0]\n"
        "v_pk_fma_f32 v[150:151], v[150:151], s[60:61], s[60:61] op_sel:[0,0,1] op_sel_hi:[1,0,1]\n"
        "v_pk_mul_f32 v[148:149], v[148:149], s[60:61] op_sel_hi:[1,0]\n"
        "v_pk_fma_f32 v[152:153], v[152:153], s[60:61], s[60:61] op_sel:[0,0,1] op_sel_hi:[1,0,1]\n"
        "v_pk_fma_f32 v[150:151], v[146:147], v[146:147], v[150:151] neg_lo:[1,0,0] neg_hi:[1,0,0]\n"
        "v_pk_fma_f32 v[152:153], v[148:149], v[148:149], v[152:153] neg_lo:[1,0,0] neg_hi:[1,0,0]\n"
        "v_maximum3_f32 v150, s61, v150, v150\n"
        "v_maximum3_f32 v151, s61, v151, v151\n"
        "L_pg5_%=:\n"
        "s_cbranch_execz L_pg9_%=\n"
        "v_maximum3_f32 v152, s61, v152, v152\n"
        "v_maximum3_f32 v153, s61, v153, v153\n"
        "v_rsq_f32 v150, v150\n"
        "v_rsq_f32 v151, v151\n"
        "v_rsq_f32 v152, v152\n"
        "v_rsq_f32 v153, v153\n"
        "s_mov_b32 s40, 0xffff0000\n"
        "s_waitcnt vmcnt(2)\n"
        "v_lshlrev_b32 v46, 16, v142\n"
        "v_and_b32 v50, s40, v142\n"
        "v_lshlrev_b32 v47, 16, v143\n"
        "v_and_b32 v51, s40, v143\n"
        "v_lshlrev_b32 v48, 16, v144\n"
        "v_and_b32 v52, s40, v144\n"
        "v_lshlrev_b32 v49, 16, v145\n"
        "v_and_b32 v53, s40, v145\n"
        "v_pk_mul_f32 v[146:147], v[146:147], v[150:151] neg_lo:[1,0] neg_hi:[1,0]\n"
        "v_pk_mul_f32 v[148:149], v[148:149], v[152:153] neg_lo:[1,0] neg_hi:[1,0]\n"
        "v_pk_fma_f32 v[162:163], v[46:47], v[150:151], v[146:147]\n"
        "v_pk_fma_f32 v[164:165], v[48:49], v[152:153], v[148:149]\n"
        "v_pk_add_f32 v[154:155], v[162:163], v[164:165]\n"
        "v_cmp_u_f32_e64 vcc, v154, v155\n"
        "v_pk_fma_f32 v[166:167], v[50:51], v[150:151], v[146:147]\n"
        "v_pk_fma_f32 v[168:169], v[52:53], v[152:153], v[148:149]\n"
        "v_cvt_pk_bf16_f32 v170, v162, v163\n"
        "v_cvt_pk_bf16_f32 v171, v164, v165\n"
        "v_cvt_pk_bf16_f32 v172, v166, v167\n"
        "v_cvt_pk_bf16_f32 v173, v168, v169\n"
        "s_cbranch_vccnz L_slow_p301_%=\n"
        "L_ret_p301_%=:\n"
        "ds_write_b64 v20, v[170:171] offset:1024\n"
        "ds_write_b64 v20, v[172:173] offset:5632\n"
        "v_pk_fma_f32 v[46:47], v[162:163], s[72:73], v[46:47] op_sel_hi:[1,0,1]\n"
        "v_pk_fma_f32 v[48:49], v[164:165], s[72:73], v[48:49] op_sel_hi:[1,0,1]\n"
        "v_pk_fma_f32 v[50:51], v[166:167], s[72:73], v[50:51] op_sel:[0,1,0]\n"
        "v_pk_fma_f32 v[52:53], v[168:169], s[72:73], v[52:53] op_sel:[0,1,0]\n"
        "s_waitcnt lgkmcnt(0)\n"
        "s_barrier\n"
        "ds_read_b128 v[86:89], v21 offset:1216\n"
        "ds_read_b128 v[90:93], v21 offset:1472\n"
        "ds_read_b128 v[126:129], v21 offset:1728\n"
        "ds_read_b128 v[130:133], v21 offset:1984\n"
        "ds_read_b128 v[134:137], v21 offset:1152\n"
        "ds_read_b128 v[138:141], v21 offset:1408\n"
        "s_waitcnt lgkmcnt(5)\n"
        "v_mfma_f32_16x16x32_bf16 v[70:73], v[110:113], v[86:89], v[70:73]\n"
        "ds_read_b128 v[58:61], v21 offset:1664\n"
        "v_mfma_f32_16x16x32_bf16 v[74:77], v[114:117], v[86:89], v[74:77]\n"
        "ds_read_b128 v[62:65], v21 offset:1920\n"
        "v_mfma_f32_16x16x32_bf16 v[78:81], v[118:121], v[86:89], v[78:81]\n"
        "v_mfma_f32_16x16x32_bf16 v[82:85], v[122:125], v[86:89], v[82:85]\n"
        "s_waitcnt lgkmcnt(4)\n"
        "ds_read_b64_tr_b16 v[122:123], v38 offset:136\n"
        "v_mfma_f32_16x16x32_bf16 v[74:77], v[110:113], v[90:93], v[74:77]\n"
        "ds_read_b64_tr_b16 v[124:125], v38 offset:128\n"
        "v_mfma_f32_16x16x32_bf16 v[78:81], v[114:117], v[90:93], v[78:81]\n"
        "ds_read_b128 v[66:69], v21 offset:1088\n"
        "v_mfma_f32_16x16x32_bf16 v[82:85], v[118:121], v[90:93], v[82:85]\n"
        "ds_read_b64_tr_b16 v[118:119], v38 offset:392\n"
        "v_mfma_f32_16x16x32_bf16 v[78:81], v[110:113], v[126:129], v[78:81]\n"
        "ds_read_b64_tr_b16 v[120:121], v38 offset:384\n"
        "v_mfma_f32_16x16x32_bf16 v[82:85], v[114:117], v[126:129], v[82:85]\n"
        "ds_read_b64_tr_b16 v[114:115], v38 offset:648\n"
        "v_mfma_f32_16x16x32_bf16 v[82:85], v[110:113], v[130:133], v[82:85]\n"
        "s_waitcnt lgkmcnt(5)\n"
        "ds_read_b64_tr_b16 v[116:117], v38 offset:640\n"
        "v_mfma_f32_16x16x32_bf16 v[70:73], v[94:97], v[134:137], v[70:73]\n"
        "ds_read_b64_tr_b16 v[110:111], v38 offset:904\n"
        "v_mfma_f32_16x16x32_bf16 v[74:77], v[98:101], v[134:137], v[74:77]\n"
        "s_lshl1_add_u32 m0, s81, 0\n"
        "ds_read_b64_tr_b16 v[112:113], v38 offset:896\n"
        "s_add_u32 s46, s28, 0x840000\n"
        "v_mfma_f32_16x16x32_bf16 v[78:81], v[102:105], v[134:137], v[78:81]\n"
        "ds_read_b128 v[86:89], v21 offset:1344\n"
        "buffer_load_dwordx4 v4, s[20:23], s46 offen sc1 lds\n"
        "v_mfma_f32_16x16x32_bf16 v[82:85], v[106:109], v[134:137], v[82:85]\n"
        "ds_read_b128 v[90:93], v21 offset:1600\n"
        "v_mfma_f32_16x16x32_bf16 v[74:77], v[94:97], v[138:141], v[74:77]\n"
        "ds_read_b128 v[126:129], v21 offset:1856\n"
        "v_mfma_f32_16x16x32_bf16 v[78:81], v[98:101], v[138:141], v[78:81]\n"
        "ds_read_b64_tr_b16 v[106:107], v38 offset:200\n"
        "v_mfma_f32_16x16x32_bf16 v[82:85], v[102:105], v[138:141], v[82:85]\n"
        "ds_read_b64_tr_b16 v[108:109], v38 offset:192\n"
        "buffer_load_dwordx4 v4, s[20:23], s46 offen offset:1024 sc1 lds\n"
        "v_mfma_f32_16x16x32_bf16 v[78:81], v[94:97], v[58:61], v[78:81]\n"
        "s_lshl1_add_u32 m0, s81, 0x800\n"
        "ds_read_b128 v[130:133], v21 offset:1024\n"
        "s_add_u32 s46, s29, 0x840000\n"
        "v_mfma_f32_16x16x32_bf16 v[82:85], v[98:101], v[58:61], v[82:85]\n"
        "ds_read_b64_tr_b16 v[102:103], v38 offset:456\n"
        "v_mfma_f32_16x16x32_bf16 v[82:85], v[94:97], v[62:65], v[82:85]\n"
        "s_waitcnt lgkmcnt(5)\n"
        "ds_read_b64_tr_b16 v[104:105], v38 offset:448\n"
        "v_mfma_f32_16x16x32_bf16 v[70:73], v[122:125], v[66:69], v[70:73]\n"
        "ds_read_b128 v[134:137], v21 offset:1280\n"
        "buffer_load_dwordx4 v4, s[20:23], s46 offen sc1 lds\n"
        "v_mfma_f32_16x16x32_bf16 v[74:77], v[118:121], v[66:69], v[74:77]\n"
        "ds_read_b64_tr_b16 v[98:99], v38 offset:712\n"
        "v_mfma_f32_16x16x32_bf16 v[78:81], v[114:117], v[66:69], v[78:81]\n"
        "ds_read_b64_tr_b16 v[100:101], v38 offset:704\n"
        "v_mfma_f32_16x16x32_bf16 v[82:85], v[110:113], v[66:69], v[82:85]\n"
        "ds_read_b128 v[138:141], v21 offset:1536\n"
        "v_mfma_f32_16x16x32_bf16 v[74:77], v[122:125], v[86:89], v[74:77]\n"
        "ds_read_b64_tr_b16 v[94:95], v38 offset:968\n"
        "buffer_load_dwordx4 v4, s[20:23], s46 offen offset:1024 sc1 lds\n"
        "v_mfma_f32_16x16x32_bf16 v[78:81], v[118:121], v[86:89], v[78:81]\n"
        "s_lshl1_add_u32 m0, s81, 0x1000\n"
        "ds_read_b64_tr_b16 v[96:97], v38 offset:960\n"
        "s_add_u32 s46, s30, 0x840000\n"
        "v_mfma_f32_16x16x32_bf16 v[82:85], v[114:117], v[86:89], v[82:85]\n"
        "ds_read_b128 v[58:61], v21 offset:1792\n"
        "v_mfma_f32_16x16x32_bf16 v[78:81], v[122:125], v[90:93], v[78:81]\n"
        "v_mfma_f32_16x16x32_bf16 v[82:85], v[118:121], v[90:93], v[82:85]\n"
        "s_waitcnt lgkmcnt(4)\n"
        "buffer_load_dwordx4 v4, s[20:23], s46 offen sc1 lds\n"
        "v_mfma_f32_16x16x32_bf16 v[82:85], v[122:125], v[126:129], v[82:85]\n"
        "v_mfma_f32_16x16x32_bf16 v[70:73], v[106:109], v[130:133], v[70:73]\n"
        "v_mfma_f32_16x16x32_bf16 v[74:77], v[102:105], v[130:133], v[74:77]\n"
        "v_mfma_f32_16x16x32_bf16 v[74:77], v[106:109], v[134:137], v[74:77]\n"
        "buffer_load_dwordx4 v4, s[20:23], s46 offen offset:1024 sc1 lds\n"
        "s_lshl1_add_u32 m0, s81, 0x1800\n"
        "v_mfma_f32_16x16x32_bf16 v[78:81], v[98:101], v[130:133], v[78:81]\n"
        "s_add_u32 s46, s31, 0x840000\n"
        "v_mfma_f32_16x16x32_bf16 v[78:81], v[102:105], v[134:137], v[78:81]\n"
        "s_waitcnt lgkmcnt(0)\n"
        "ds_write_b128 v39, v[70:73]\n"
        "v_mfma_f32_16x16x32_bf16 v[78:81], v[106:109], v[138:141], v[78:81]\n"
        "ds_write_b128 v39, v[74:77] offset:1024\n"
        "buffer_load_dwordx4 v4, s[20:23], s46 offen sc1 lds\n"
        "v_mfma_f32_16x16x32_bf16 v[82:85], v[94:97], v[130:133], v[82:85]\n"
        "v_mfma_f32_16x16x32_bf16 v[82:85], v[98:101], v[134:137], v[82:85]\n"
        "v_mfma_f32_16x16x32_bf16 v[82:85], v[102:105], v[138:141], v[82:85]\n"
        "v_mfma_f32_16x16x32_bf16 v[82:85], v[106:109], v[58:61], v[82:85]\n"
        "s_nop 1\n"
        "ds_write_b128 v39, v[78:81] offset:2048\n"
        "buffer_load_dwordx4 v4, s[20:23], s46 offen offset:1024 sc1 lds\n"
        "s_add_u32 m0, s79, 0x8000\n"
        "s_add_u32 s46, s33, 0x82c000\n"
        "s_nop 1\n"
        "ds_write_b128 v39, v[82:85] offset:3072\n"
        "s_waitcnt vmcnt(8)\n"
        "buffer_load_dwordx4 v43, s[20:23], s46 offen sc1 lds\n"
        "s_add_u32 m0, s79, 0x8400\n"
        "s_add_u32 s46, s33, 0x82c000\n"
        "v_mov_b32 v1, v1\n"
        "s_nop 3\n"
        "v_mov_b32_e64 v1, v1\n"
        "s_nop 1\n"
        "s_waitcnt lgkmcnt(0)\n"
        "buffer_load_dwordx4 v44, s[20:23], s46 offen sc1 lds\n"
        "s_barrier\n"
        "ds_read_b128 v[70:73], v40\n"
        "ds_read_b128 v[74:77], v40 offset:4096\n"
        "ds_read_b128 v[78:81], v40 offset:8192\n"
        "ds_read_b128 v[82:85], v40 offset:12288\n"
        "s_mov_b32 s91, 0x980000\n"
        "s_waitcnt lgkmcnt(3)\n"
        "v_pk_add_f32 v[46:47], v[46:47], v[70:71]\n"
        "v_pk_add_f32 v[48:49], v[48:49], v[72:73]\n"
        "s_waitcnt lgkmcnt(2)\n"
        "v_pk_add_f32 v[46:47], v[46:47], v[74:75]\n"
        "v_pk_add_f32 v[48:49], v[48:49], v[76:77]\n"
        "s_waitcnt lgkmcnt(1)\n"
        "v_pk_add_f32 v[50:51], v[50:51], v[78:79]\n"
        "v_pk_add_f32 v[52:53], v[52:53], v[80:81]\n"
        "s_waitcnt lgkmcnt(0)\n"
        "v_pk_add_f32 v[50:51], v[50:51], v[82:83]\n"
        "v_pk_add_f32 v[52:53], v[52:53], v[84:85]\n"
        "v_cvt_pk_bf16_f32 v70, v46, v47\n"
        "v_cvt_pk_bf16_f32 v71, v48, v49\n"
        "v_cvt_pk_bf16_f32 v72, v50, v51\n"
        "v_cvt_pk_bf16_f32 v73, v52, v53\n"
        "buffer_store_dwordx2 v[70:71], v41, s[20:23], s91 offen sc1\n"
        "buffer_store_dwordx2 v[72:73], v41, s[20:23], s91 offen offset:64 sc1\n"
        "s_lshl1_add_u32 s43, s81, 0\n"
        "v_add_u32 v87, s43, v4\n"
        "s_add_u32 s43, s79, 0x8000\n"
        "v_add_u32_e64 v88, s43, v4\n"
        "s_waitcnt vmcnt(10)\n"
        "ds_read_b128 v[46:49], v87\n"
        "ds_read_b128 v[50:53], v87 offset:1024\n"
        "s_waitcnt vmcnt(8)\n"
        "ds_read_b128 v[54:57], v87 offset:2048\n"
        "ds_read_b128 v[58:61], v87 offset:3072\n"
        "s_waitcnt vmcnt(6)\n"
        "ds_read_b128 v[62:65], v87 offset:4096\n"
        "ds_read_b128 v[66:69], v87 offset:5120\n"
        "s_waitcnt vmcnt(4)\n"
        "ds_read_b128 v[70:73], v87 offset:6144\n"
        "ds_read_b128 v[74:77], v87 offset:7168\n"
        "s_waitcnt vmcnt(2)\n"
        "ds_read_b128 v[78:81], v88\n"
        "ds_read_b128 v[82:85], v88 offset:1024\n"
        "s_waitcnt lgkmcnt(2)\n"
        "v_max3_u32 v86, v46, v46, v47\n"
        "v_max3_u32 v86, v86, v48, v49\n"
        "v_max3_u32 v86, v86, v50, v51\n"
        "v_max3_u32 v86, v86, v52, v53\n"
        "v_max3_u32 v86, v86, v54, v55\n"
        "v_max3_u32 v86, v86, v56, v57\n"
        "v_max3_u32 v86, v86, v58, v59\n"
        "v_max3_u32 v86, v86, v60, v61\n"
        "v_max3_u32 v86, v86, v62, v63\n"
        "v_max3_u32 v86, v86, v64, v65\n"
        "v_max3_u32 v86, v86, v66, v67\n"
        "v_max3_u32 v86, v86, v68, v69\n"
        "v_max3_u32 v86, v86, v70, v71\n"
        "v_max3_u32 v86, v86, v72, v73\n"
        "v_max3_u32 v86, v86, v74, v75\n"
        "v_max3_u32 v86, v86, v76, v77\n"
        "s_waitcnt lgkmcnt(1)\n"
        "v_max3_u32 v86, v86, v78, v79\n"
        "v_max3_u32 v86, v86, v80, v81\n"
        "s_waitcnt lgkmcnt(0)\n"
        "v_max3_u32 v86, v86, v82, v83\n"
        "v_max3_u32 v86, v86, v84, v85\n"
        "v_cmp_eq_u32 vcc, -1, v86\n"
        "s_cbranch_vccnz L_slow_x20_%=\n"
        "L_ret_x20_%=:\n"
        "s_mul_i32 s40, s51, 0x800\n"
        "v_add_u32 v176, s40, v32\n"
        "v_add_u32 v177, s40, v33\n"
        "s_lshl_b32 s40, s51, 9\n"
        "v_add_u32_e64 v45, s40, v5\n"
        "s_barrier\n"
        "ds_read_b64_tr_b16 v[88:89], v45\n"
        "ds_read_b64_tr_b16 v[90:91], v45 offset:64\n"
        "ds_read_b64_tr_b16 v[92:93], v176\n"
        "ds_read_b64_tr_b16 v[94:95], v176 offset:256\n"
        "ds_read_b64_tr_b16 v[96:97], v177\n"
        "ds_read_b64_tr_b16 v[98:99], v177 offset:256\n"
        "ds_read_b64_tr_b16 v[100:101], v45 offset:2048\n"
        "ds_read_b64_tr_b16 v[102:103], v45 offset:2112\n"
        "ds_read_b64_tr_b16 v[104:105], v176 offset:8192\n"
        "ds_read_b64_tr_b16 v[106:107], v176 offset:8448\n"
        "ds_read_b64_tr_b16 v[108:109], v177 offset:8192\n"
        "ds_read_b64_tr_b16 v[110:111], v177 offset:8448\n"
        "s_waitcnt lgkmcnt(10)\n"
        "v_mfma_f32_16x16x32_bf16 v[120:123], v[88:91], v[88:91], 0\n"
        "ds_read_b64_tr_b16 v[112:113], v45 offset:4096\n"
        "v_mfma_f32_16x16x32_bf16 v[132:135], v[6:9], v[88:91], 0\n"
        "s_waitcnt lgkmcnt(8)\n"
        "ds_read_b64_tr_b16 v[114:115], v45 offset:4160\n"
        "v_mfma_f32_16x16x32_bf16 v[124:127], v[92:95], v[92:95], 0\n"
        "ds_read_b64_tr_b16 v[116:117], v176 offset:16384\n"
        "v_mfma_f32_16x16x32_bf16 v[136:139], v[6:9], v[92:95], 0\n"
        "s_waitcnt lgkmcnt(6)\n"
        "ds_read_b64_tr_b16 v[118:119], v176 offset:16640\n"
        "v_mfma_f32_16x16x32_bf16 v[128:131], v[96:99], v[96:99], 0\n"
        "ds_read_b64_tr_b16 v[88:89], v177 offset:16384\n"
        "v_mfma_f32_16x16x32_bf16 v[140:143], v[6:9], v[96:99], 0\n"
        "ds_read_b64_tr_b16 v[90:91], v177 offset:16640\n"
        "v_mov_b32 v180, -1\n"
        "v_mfma_f32_16x16x32_bf16 v[120:123], v[100:103], v[100:103], v[120:123]\n"
        "ds_read_b64_tr_b16 v[92:93], v45 offset:6144\n"
        "v_mov_b32 v181, -1\n"
        "v_mfma_f32_16x16x32_bf16 v[132:135], v[6:9], v[100:103], v[132:135]\n"
        "s_waitcnt lgkmcnt(5)\n"
        "ds_read_b64_tr_b16 v[94:95], v45 offset:6208\n"
        "v_mov_b32 v182, -1\n"
        "v_mfma_f32_16x16x32_bf16 v[124:127], v[104:107], v[104:107], v[124:127]\n"
        "ds_read_b64_tr_b16 v[96:97], v176 offset:24576\n"
        "v_mov_b32 v183, -1\n"
        "v_mfma_f32_16x16x32_bf16 v[136:139], v[6:9], v[104:107], v[136:139]\n"
        "ds_read_b64_tr_b16 v[98:99], v176 offset:24832\n"
        "v_lshrrev_b32 v179, 2, v0\n"
        "v_mfma_f32_16x16x32_bf16 v[128:131], v[108:111], v[108:111], v[128:131]\n"
        "ds_read_b64_tr_b16 v[100:101], v177 offset:24576\n"
        "v_lshlrev_b32 v179, 12, v179\n"
        "v_mfma_f32_16x16x32_bf16 v[140:143], v[6:9], v[108:111], v[140:143]\n"
        "ds_read_b64_tr_b16 v[102:103], v177 offset:24832\n"
        "v_and_or_b32 v179, v4, 48, v179\n"
        "v_mfma_f32_16x16x32_bf16 v[120:123], v[112:115], v[112:115], v[120:123]\n"
        "s_add_u32 s46, s34, 0x640000\n"
        "ds_read_b64_tr_b16 v[104:105], v5\n"
        "buffer_store_dwordx4 v[180:183], v179, s[20:23], s46 offen sc1\n"
        "v_mfma_f32_16x16x32_bf16 v[132:135], v[6:9], v[112:115], v[132:135]\n"
        "s_waitcnt lgkmcnt(5)\n"
        "ds_read_b64_tr_b16 v[106:107], v5 offset:64\n"
        "s_add_u32 s46, s34, 0x740000\n"
        "v_mfma_f32_16x16x32_bf16 v[124:127], v[116:119], v[116:119], v[124:127]\n"
        "ds_read_b64_tr_b16 v[108:109], v32\n"
        "v_mfma_f32_16x16x32_bf16 v[136:139], v[6:9], v[116:119], v[136:139]\n"
        "ds_read_b64_tr_b16 v[110:111], v32 offset:256\n"
        "v_mfma_f32_16x16x32_bf16 v[128:131], v[88:91], v[88:91], v[128:131]\n"
        "ds_read_b64_tr_b16 v[112:113], v33\n"
        "buffer_store_dwordx4 v[180:183], v179, s[20:23], s46 offen sc1\n"
        "v_mfma_f32_16x16x32_bf16 v[140:143], v[6:9], v[88:91], v[140:143]\n"
        "s_add_u32 s46, s35, 0x20000\n"
        "ds_read_b64_tr_b16 v[114:115], v33 offset:256\n"
        "buffer_store_dword v180, v37, s[20:23], s46 offen sc1\n"
        "v_mfma_f32_16x16x32_bf16 v[120:123], v[92:95], v[92:95], v[120:123]\n"
        "s_add_u32 s46, s35, 0x28000\n"
        "ds_read_b64_tr_b16 v[116:117], v5 offset:512\n"
        "buffer_store_dword v180, v37, s[20:23], s46 offen sc1\n"
        "v_mfma_f32_16x16x32_bf16 v[132:135], v[6:9], v[92:95], v[132:135]\n"
        "s_waitcnt lgkmcnt(5)\n"
        "ds_read_b64_tr_b16 v[118:119], v5 offset:576\n"
        "v_mfma_f32_16x16x32_bf16 v[124:127], v[96:99], v[96:99], v[124:127]\n"
        "ds_read_b64_tr_b16 v[88:89], v32 offset:2048\n"
        "v_mfma_f32_16x16x32_bf16 v[136:139], v[6:9], v[96:99], v[136:139]\n"
        "ds_read_b64_tr_b16 v[90:91], v32 offset:2304\n"
        "v_mfma_f32_16x16x32_bf16 v[128:131], v[100:103], v[100:103], v[128:131]\n"
        "ds_read_b64_tr_b16 v[92:93], v33 offset:2048\n"
        "v_mfma_f32_16x16x32_bf16 v[140:143], v[6:9], v[100:103], v[140:143]\n"
        "ds_read_b64_tr_b16 v[94:95], v33 offset:2304\n"
        "v_cndmask_b32 v120, v120, v121, s[64:65]\n"
        "v_mfma_f32_16x16x32_bf16 v[56:59], a[4:7], v[104:107], 0\n"
        "ds_read_b64_tr_b16 v[96:97], v5 offset:1024\n"
        "v_cndmask_b32 v120, v120, v122, s[66:67]\n"
        "v_mfma_f32_16x16x32_bf16 v[60:63], v[104:107], a[8:11], 0\n"
        "s_waitcnt lgkmcnt(5)\n"
        "ds_read_b64_tr_b16 v[98:99], v5 offset:1088\n"
        "v_cndmask_b32 v133, v120, v123, s[68:69]\n"
        "v_mfma_f32_16x16x32_bf16 v[64:67], a[0:3], v[108:111], 0\n"
        "ds_read_b64_tr_b16 v[100:101], v32 offset:4096\n"
        "v_cndmask_b32 v124, v124, v125, s[64:65]\n"
        "v_mfma_f32_16x16x32_bf16 v[68:71], a[4:7], v[108:111], 0\n"
        "ds_read_b64_tr_b16 v[102:103], v32 offset:4352\n"
        "v_cndmask_b32 v124, v124, v126, s[66:67]\n"
        "v_mfma_f32_16x16x32_bf16 v[72:75], v[108:111], a[8:11], 0\n"
        "ds_read_b64_tr_b16 v[104:105], v33 offset:4096\n"
        "v_cndmask_b32 v137, v124, v127, s[68:69]\n"
        "v_mfma_f32_16x16x32_bf16 v[76:79], a[0:3], v[112:115], 0\n"
        "ds_read_b64_tr_b16 v[106:107], v33 offset:4352\n"
        "v_cndmask_b32 v128, v128, v129, s[64:65]\n"
        "v_mfma_f32_16x16x32_bf16 v[80:83], a[4:7], v[112:115], 0\n"
        "ds_read_b64_tr_b16 v[108:109], v5 offset:1536\n"
        "v_cndmask_b32 v128, v128, v130, s[66:67]\n"
        "v_mfma_f32_16x16x32_bf16 v[84:87], v[112:115], a[8:11], 0\n"
        "ds_read_b64_tr_b16 v[110:111], v5 offset:1600\n"
        "v_cndmask_b32 v141, v128, v131, s[68:69]\n"
        "v_mfma_f32_16x16x32_bf16 v[56:59], a[16:19], v[116:119], v[56:59]\n"
        "s_mul_i32 s48, s51, 0x180\n"
        "ds_read_b64_tr_b16 v[112:113], v32 offset:6144\n"
        "s_add_u32 s48, s48, 0x27000\n"
        "v_mfma_f32_16x16x32_bf16 v[60:63], v[116:119], a[20:23], v[60:63]\n"
        "s_waitcnt lgkmcnt(5)\n"
        "ds_read_b64_tr_b16 v[114:115], v32 offset:6400\n"
        "v_lshl_add_u32 v178, v2, 3, s48\n"
        "v_mfma_f32_16x16x32_bf16 v[64:67], a[12:15], v[88:91], v[64:67]\n"
        "s_mov_b64 exec, s[70:71]\n"
        "ds_write_b64 v178, v[132:133]\n"
        "ds_write_b64 v178, v[136:137] offset:128\n"
        "ds_write_b64 v178, v[140:141] offset:256\n"
        "s_mov_b64 exec, -1\n"
        "v_mfma_f32_16x16x32_bf16 v[68:71], a[16:19], v[88:91], v[68:71]\n"
        "s_mov_b32 s48, 0x27000\n"
        "ds_read_b64_tr_b16 v[116:117], v5 offset:2048\n"
        "v_lshl_add_u32 v178, v1, 3, s48\n"
        "v_mfma_f32_16x16x32_bf16 v[72:75], v[88:91], a[20:23], v[72:75]\n"
        "s_lshl_b32 s48, s51, 9\n"
        "ds_read_b64_tr_b16 v[118:119], v5 offset:2112\n"
        "s_add_u32 s48, s48, 0x27800\n"
        "v_mfma_f32_16x16x32_bf16 v[76:79], a[12:15], v[92:95], v[76:79]\n"
        "ds_read_b64_tr_b16 v[88:89], v32 offset:8192\n"
        "v_lshl_add_u32 v45, v1, 2, s48\n"
        "v_mfma_f32_16x16x32_bf16 v[80:83], a[16:19], v[92:95], v[80:83]\n"
        "ds_read_b64_tr_b16 v[90:91], v32 offset:8448\n"
        "v_lshl_add_u32 v176, v2, 2, s48\n"
        "v_mfma_f32_16x16x32_bf16 v[84:87], v[92:95], a[20:23], v[84:87]\n"
        "ds_read_b64_tr_b16 v[92:93], v5 offset:2560\n"
        "v_lshl_add_u32 v177, v3, 4, s48\n"
        "v_mfma_f32_16x16x32_bf16 v[56:59], a[28:31], v[96:99], v[56:59]\n"
        "s_waitcnt lgkmcnt(5)\n"
        "ds_read_b64_tr_b16 v[94:95], v5 offset:2624\n"
        "s_barrier\n"
        "v_mfma_f32_16x16x32_bf16 v[60:63], v[96:99], a[32:35], v[60:63]\n"
        "ds_read_b64 v[120:121], v178\n"
        "v_mfma_f32_16x16x32_bf16 v[64:67], a[24:27], v[100:103], v[64:67]\n"
        "ds_read_b64 v[122:123], v178 offset:384\n"
        "v_mfma_f32_16x16x32_bf16 v[68:71], a[28:31], v[100:103], v[68:71]\n"
        "ds_read_b64 v[124:125], v178 offset:768\n"
        "v_mfma_f32_16x16x32_bf16 v[72:75], v[100:103], a[32:35], v[72:75]\n"
        "ds_read_b64 v[126:127], v178 offset:1152\n"
        "v_mfma_f32_16x16x32_bf16 v[76:79], a[24:27], v[104:107], v[76:79]\n"
        "ds_read_b64_tr_b16 v[52:53], v35\n"
        "v_mfma_f32_16x16x32_bf16 v[80:83], a[28:31], v[104:107], v[80:83]\n"
        "ds_read_b64_tr_b16 v[54:55], v35 offset:1024\n"
        "v_mfma_f32_16x16x32_bf16 v[84:87], v[104:107], a[32:35], v[84:87]\n"
        "ds_read_b64_tr_b16 v[96:97], v32 offset:10240\n"
        "v_mfma_f32_16x16x32_bf16 v[56:59], a[40:43], v[108:111], v[56:59]\n"
        "s_waitcnt lgkmcnt(5)\n"
        "ds_read_b64_tr_b16 v[98:99], v32 offset:10496\n"
        "v_add_f32 v120, v120, v122\n"
        "v_mfma_f32_16x16x32_bf16 v[60:63], v[108:111], a[44:47], v[60:63]\n"
        "ds_read_b64_tr_b16 v[100:101], v5 offset:3072\n"
        "v_mfma_f32_16x16x32_bf16 v[64:67], a[36:39], v[112:115], v[64:67]\n"
        "s_waitcnt lgkmcnt(5)\n"
        "ds_read_b64_tr_b16 v[102:103], v5 offset:3136\n"
        "v_add_f32 v124, v124, v126\n"
        "v_mfma_f32_16x16x32_bf16 v[68:71], a[40:43], v[112:115], v[68:71]\n"
        "ds_read_b64_tr_b16 v[104:105], v32 offset:12288\n"
        "v_add_f32 v121, v121, v123\n"
        "v_mfma_f32_16x16x32_bf16 v[72:75], v[112:115], a[44:47], v[72:75]\n"
        "ds_read_b64_tr_b16 v[106:107], v32 offset:12544\n"
        "v_add_f32 v125, v125, v127\n"
        "v_mfma_f32_16x16x32_bf16 v[56:59], a[52:55], v[116:119], v[56:59]\n"
        "ds_read_b64_tr_b16 v[108:109], v5 offset:3584\n"
        "v_add_f32 v120, v120, v124\n"
        "v_mfma_f32_16x16x32_bf16 v[60:63], v[116:119], a[56:59], v[60:63]\n"
        "ds_read_b64_tr_b16 v[110:111], v5 offset:3648\n"
        "v_add_f32 v121, v121, v125\n"
        "v_mfma_f32_16x16x32_bf16 v[64:67], a[48:51], v[88:91], v[64:67]\n"
        "ds_read_b64_tr_b16 v[112:113], v32 offset:14336\n"
        "v_mul_f32 v120, s60, v120\n"
        "v_mfma_f32_16x16x32_bf16 v[68:71], a[52:55], v[88:91], v[68:71]\n"
        "ds_read_b64_tr_b16 v[114:115], v32 offset:14592\n"
        "v_mul_f32_e64 v121, s60, v121\n"
        "v_mfma_f32_16x16x32_bf16 v[72:75], v[88:91], a[56:59], v[72:75]\n"
        "ds_read_b64_tr_b16 v[116:117], v5 offset:4096\n"
        "v_fma_f32 v121, -v120, v120, v121\n"
        "v_mfma_f32_16x16x32_bf16 v[56:59], a[64:67], v[92:95], v[56:59]\n"
        "ds_read_b64_tr_b16 v[118:119], v5 offset:4160\n"
        "v_max_f32 v121, 0, v121\n"
        "v_mfma_f32_16x16x32_bf16 v[60:63], v[92:95], a[68:71], v[60:63]\n"
        "s_waitcnt lgkmcnt(5)\n"
        "ds_read_b64_tr_b16 v[88:89], v32 offset:16384\n"
        "v_add_f32 v121, s61, v121\n"
        "v_mfma_f32_16x16x32_bf16 v[64:67], a[60:63], v[96:99], v[64:67]\n"
        "v_rsq_f32 v121, v121\n"
        "v_mfma_f32_16x16x32_bf16 v[68:71], a[64:67], v[96:99], v[68:71]\n"
        "ds_read_b64_tr_b16 v[90:91], v32 offset:16640\n"
        "v_mul_f32 v120, -v120, v121\n"
        "v_mfma_f32_16x16x32_bf16 v[72:75], v[96:99], a[68:71], v[72:75]\n"
        "ds_write2_b32 v45, v121, v120 offset1:64\n"
        "v_mfma_f32_16x16x32_bf16 v[56:59], a[76:79], v[100:103], v[56:59]\n"
        "ds_read2_b32 v[128:129], v176 offset0:0 offset1:64\n"
        "v_mfma_f32_16x16x32_bf16 v[60:63], v[100:103], a[80:83], v[60:63]\n"
        "ds_read2_b32 v[130:131], v176 offset0:16 offset1:80\n"
        "v_mfma_f32_16x16x32_bf16 v[64:67], a[72:75], v[104:107], v[64:67]\n"
        "ds_read2_b32 v[124:125], v176 offset0:32 offset1:96\n"
        "v_mfma_f32_16x16x32_bf16 v[68:71], a[76:79], v[104:107], v[68:71]\n"
        "ds_read_b128 v[144:147], v177 offset:256\n"
        "v_mfma_f32_16x16x32_bf16 v[72:75], v[104:107], a[80:83], v[72:75]\n"
        "s_waitcnt lgkmcnt(5)\n"
        "ds_read_b128 v[132:135], v177\n"
        "v_mfma_f32_16x16x32_bf16 v[56:59], a[88:91], v[108:111], v[56:59]\n"
        "ds_read_b64_tr_b16 v[92:93], v5 offset:4608\n"
        "v_mfma_f32_16x16x32_bf16 v[60:63], v[108:111], a[92:95], v[60:63]\n"
        "ds_read_b64_tr_b16 v[94:95], v5 offset:4672\n"
        "v_mfma_f32_16x16x32_bf16 v[64:67], a[84:87], v[112:115], v[64:67]\n"
        "ds_read_b64_tr_b16 v[96:97], v32 offset:18432\n"
        "v_mfma_f32_16x16x32_bf16 v[68:71], a[88:91], v[112:115], v[68:71]\n"
        "ds_read_b64_tr_b16 v[98:99], v32 offset:18688\n"
        "v_mfma_f32_16x16x32_bf16 v[72:75], v[112:115], a[92:95], v[72:75]\n"
        "ds_read_b128 v[148:151], v177 offset:320\n"
        "v_mfma_f32_16x16x32_bf16 v[56:59], a[100:103], v[116:119], v[56:59]\n"
        "ds_read_b64_tr_b16 v[100:101], v5 offset:5120\n"
        "v_mfma_f32_16x16x32_bf16 v[60:63], v[116:119], a[104:107], v[60:63]\n"
        "ds_read_b64_tr_b16 v[102:103], v5 offset:5184\n"
        "v_mfma_f32_16x16x32_bf16 v[64:67], a[96:99], v[88:91], v[64:67]\n"
        "ds_read_b64_tr_b16 v[104:105], v32 offset:20480\n"
        "v_mfma_f32_16x16x32_bf16 v[68:71], a[100:103], v[88:91], v[68:71]\n"
        "ds_read_b64_tr_b16 v[106:107], v32 offset:20736\n"
        "v_mfma_f32_16x16x32_bf16 v[72:75], v[88:91], a[104:107], v[72:75]\n"
        "s_waitcnt lgkmcnt(5)\n"
        "ds_read_b128 v[136:139], v177 offset:64\n"
        "v_mfma_f32_16x16x32_bf16 v[56:59], a[112:115], v[92:95], v[56:59]\n"
        "ds_read_b64_tr_b16 v[108:109], v5 offset:5632\n"
        "v_mfma_f32_16x16x32_bf16 v[60:63], v[92:95], a[116:119], v[60:63]\n"
        "ds_read_b64_tr_b16 v[110:111], v5 offset:5696\n"
        "v_mfma_f32_16x16x32_bf16 v[64:67], a[108:111], v[96:99], v[64:67]\n"
        "ds_read_b64_tr_b16 v[112:113], v32 offset:22528\n"
        "v_mfma_f32_16x16x32_bf16 v[68:71], a[112:115], v[96:99], v[68:71]\n"
        "ds_read_b64_tr_b16 v[114:115], v32 offset:22784\n"
        "v_mfma_f32_16x16x32_bf16 v[72:75], v[96:99], a[116:119], v[72:75]\n"
        "s_waitcnt lgkmcnt(5)\n"
        "ds_read_b128 v[152:155], v177 offset:384\n"
        "s_lshl1_add_u32 m0, s81, 0xa500\n"
        "v_mfma_f32_16x16x32_bf16 v[56:59], a[124:127], v[100:103], v[56:59]\n"
        "s_add_u32 s46, s28, 0x980000\n"
        "ds_read_b64_tr_b16 v[116:117], v5 offset:6144\n"
        "buffer_load_dwordx4 v4, s[20:23], s46 offen sc1 lds\n"
        "v_mfma_f32_16x16x32_bf16 v[60:63], v[100:103], a[128:131], v[60:63]\n"
        "ds_read_b64_tr_b16 v[118:119], v5 offset:6208\n"
        "v_mfma_f32_16x16x32_bf16 v[64:67], a[120:123], v[104:107], v[64:67]\n"
        "ds_read_b64_tr_b16 v[88:89], v32 offset:24576\n"
        "v_mfma_f32_16x16x32_bf16 v[68:71], a[124:127], v[104:107], v[68:71]\n"
        "ds_read_b64_tr_b16 v[90:91], v32 offset:24832\n"
        "v_mfma_f32_16x16x32_bf16 v[72:75], v[104:107], a[128:131], v[72:75]\n"
        "s_waitcnt lgkmcnt(5)\n"
        "ds_read_b128 v[140:143], v177 offset:128\n"
        "buffer_load_dwordx4 v4, s[20:23], s46 offen offset:1024 sc1 lds\n"
        "v_mfma_f32_16x16x32_bf16 v[56:59], a[136:139], v[108:111], v[56:59]\n"
        "s_lshl1_add_u32 m0, s81, 0xad00\n"
        "ds_read_b64_tr_b16 v[92:93], v5 offset:6656\n"
        "s_add_u32 s46, s29, 0x980000\n"
        "v_mfma_f32_16x16x32_bf16 v[60:63], v[108:111], a[140:143], v[60:63]\n"
        "s_mov_b32 s48, 0x3eb8aa3b\n"
        "ds_read_b64_tr_b16 v[94:95], v5 offset:6720\n"
        "v_mul_f32 v120, s48, v130\n"
        "v_mfma_f32_16x16x32_bf16 v[64:67], a[132:135], v[112:115], v[64:67]\n"
        "ds_read_b64_tr_b16 v[96:97], v32 offset:26624\n"
        "v_mul_f32 v121, s48, v131\n"
        "v_mfma_f32_16x16x32_bf16 v[68:71], a[136:139], v[112:115], v[68:71]\n"
        "ds_read_b64_tr_b16 v[98:99], v32 offset:26880\n"
        "buffer_load_dwordx4 v4, s[20:23], s46 offen sc1 lds\n"
        "v_mfma_f32_16x16x32_bf16 v[72:75], v[112:115], a[140:143], v[72:75]\n"
        "s_waitcnt lgkmcnt(5)\n"
        "ds_read_b64_tr_b16 v[100:101], v5 offset:7168\n"
        "v_mul_f32 v122, s48, v124\n"
        "v_mfma_f32_16x16x32_bf16 v[56:59], a[148:151], v[116:119], v[56:59]\n"
        "ds_read_b64_tr_b16 v[102:103], v5 offset:7232\n"
        "v_mul_f32 v123, s48, v125\n"
        "v_mfma_f32_16x16x32_bf16 v[60:63], v[116:119], a[152:155], v[60:63]\n"
        "ds_read_b64_tr_b16 v[104:105], v32 offset:28672\n"
        "v_mul_f32 v164, v129, v14\n"
        "v_mfma_f32_16x16x32_bf16 v[64:67], a[144:147], v[88:91], v[64:67]\n"
        "ds_read_b64_tr_b16 v[106:107], v32 offset:28928\n"
        "buffer_load_dwordx4 v4, s[20:23], s46 offen offset:1024 sc1 lds\n"
        "v_mfma_f32_16x16x32_bf16 v[68:71], a[148:151], v[88:91], v[68:71]\n"
        "s_lshl1_add_u32 m0, s81, 0xb500\n"
        "ds_read_b64_tr_b16 v[108:109], v5 offset:7680\n"
        "v_mul_f32 v165, v129, v15\n"
        "v_mfma_f32_16x16x32_bf16 v[72:75], v[88:91], a[152:155], v[72:75]\n"
        "s_waitcnt lgkmcnt(5)\n"
        "ds_read_b64_tr_b16 v[110:111], v5 offset:7744\n"
        "v_mul_f32 v166, v129, v16\n"
        "v_mfma_f32_16x16x32_bf16 v[56:59], a[160:163], v[92:95], v[56:59]\n"
        "s_add_u32 s46, s30, 0x980000\n"
        "ds_read_b64_tr_b16 v[112:113], v32 offset:30720\n"
        "v_mul_f32 v167, v129, v17\n"
        "v_mfma_f32_16x16x32_bf16 v[60:63], v[92:95], a[164:167], v[60:63]\n"
        "ds_read_b64_tr_b16 v[114:115], v32 offset:30976\n"
        "buffer_load_dwordx4 v4, s[20:23], s46 offen sc1 lds\n"
        "v_mfma_f32_16x16x32_bf16 v[64:67], a[156:159], v[96:99], v[64:67]\n"
        "ds_read_b64_tr_b16 v[116:117], v33 offset:6144\n"
        "v_mul_f32 v144, v144, v18\n"
        "v_mfma_f32_16x16x32_bf16 v[68:71], a[160:163], v[96:99], v[68:71]\n"
        "ds_read_b64_tr_b16 v[118:119], v33 offset:6400\n"
        "v_mul_f32 v145, v145, v18\n"
        "v_mfma_f32_16x16x32_bf16 v[72:75], v[96:99], a[164:167], v[72:75]\n"
        "s_waitcnt lgkmcnt(5)\n"
        "ds_read_b64_tr_b16 v[88:89], v33 offset:8192\n"
        "v_mul_f32 v146, v146, v18\n"
        "v_mfma_f32_16x16x32_bf16 v[56:59], a[172:175], v[100:103], v[56:59]\n"
        "ds_read_b64_tr_b16 v[90:91], v33 offset:8448\n"
        "buffer_load_dwordx4 v4, s[20:23], s46 offen offset:1024 sc1 lds\n"
        "v_mfma_f32_16x16x32_bf16 v[60:63], v[100:103], a[176:179], v[60:63]\n"
        "s_lshl1_add_u32 m0, s81, 0xbd00\n"
        "ds_read_b64_tr_b16 v[92:93], v33 offset:10240\n"
        "v_mul_f32 v147, v147, v18\n"
        "v_mfma_f32_16x16x32_bf16 v[64:67], a[168:171], v[104:107], v[64:67]\n"
        "s_add_u32 s46, s31, 0x980000\n"
        "ds_read_b64_tr_b16 v[94:95], v33 offset:10496\n"
        "v_mul_f32 v168, v131, v14\n"
        "v_mfma_f32_16x16x32_bf16 v[68:71], a[172:175], v[104:107], v[68:71]\n"
        "ds_read_b64_tr_b16 v[96:97], v33 offset:12288\n"
        "v_mul_f32 v169, v131, v15\n"
        "v_mfma_f32_16x16x32_bf16 v[72:75], v[104:107], a[176:179], v[72:75]\n"
        "s_waitcnt lgkmcnt(5)\n"
        "ds_read_b64_tr_b16 v[98:99], v33 offset:12544\n"
        "buffer_load_dwordx4 v4, s[20:23], s46 offen sc1 lds\n"
        "v_mfma_f32_16x16x32_bf16 v[56:59], a[184:187], v[108:111], v[56:59]\n"
        "ds_read_b64_tr_b16 v[100:101], v33 offset:14336\n"
        "v_mul_f32 v170, v131, v16\n"
        "v_mfma_f32_16x16x32_bf16 v[60:63], v[108:111], a[188:191], v[60:63]\n"
        "ds_read_b64_tr_b16 v[102:103], v33 offset:14592\n"
        "v_mul_f32 v171, v131, v17\n"
        "v_mfma_f32_16x16x32_bf16 v[64:67], a[180:183], v[112:115], v[64:67]\n"
        "ds_read_b64_tr_b16 v[104:105], v33 offset:16384\n"
        "v_mul_f32_e64 v156, v121, v10\n"
        "v_mfma_f32_16x16x32_bf16 v[68:71], a[184:187], v[112:115], v[68:71]\n"
        "ds_read_b64_tr_b16 v[106:107], v33 offset:16640\n"
        "buffer_load_dwordx4 v4, s[20:23], s46 offen offset:1024 sc1 lds\n"
        "v_mfma_f32_16x16x32_bf16 v[72:75], v[112:115], a[188:191], v[72:75]\n"
        "s_add_u32 m0, s79, 0x12500\n"
        "L_pg6_%=:\n"
        "s_cbranch_execz L_pg10_%=\n"
        "ds_read_b64_tr_b16 v[108:109], v33 offset:18432\n"
        "v_mul_f32 v157, v121, v11\n"
        "v_mfma_f32_16x16x32_bf16 v[76:79], a[36:39], v[116:119], v[76:79]\n"
        "s_add_u32 s46, s32, 0x96c000\n"
        "ds_read_b64_tr_b16 v[110:111], v33 offset:18688\n"
        "v_mul_f32 v158, v121, v12\n"
        "v_mfma_f32_16x16x32_bf16 v[80:83], a[40:43], v[116:119], v[80:83]\n"
        "ds_read_b64_tr_b16 v[112:113], v33 offset:20480\n"
        "v_mul_f32_e64 v159, v121, v13\n"
        "v_mfma_f32_16x16x32_bf16 v[84:87], v[116:119], a[44:47], v[84:87]\n"
        "s_waitcnt lgkmcnt(5)\n"
        "ds_read_b64_tr_b16 v[114:115], v33 offset:20736\n"
        "buffer_load_dwordx4 v43, s[20:23], s46 offen sc1 lds\n"
        "v_mfma_f32_16x16x32_bf16 v[76:79], a[48:51], v[88:91], v[76:79]\n"
        "s_add_u32 m0, s79, 0x12900\n"
        "ds_read_b64_tr_b16 v[116:117], v33 offset:22528\n"
        "v_mul_f32 v148, v148, v18\n"
        "v_mfma_f32_16x16x32_bf16 v[80:83], a[52:55], v[88:91], v[80:83]\n"
        "s_add_u32 s46, s32, 0x96c000\n"
        "ds_read_b64_tr_b16 v[118:119], v33 offset:22784\n"
        "v_mul_f32 v149, v149, v18\n"
        "v_mfma_f32_16x16x32_bf16 v[84:87], v[88:91], a[56:59], v[84:87]\n"
        "ds_read_b64_tr_b16 v[88:89], v33 offset:24576\n"
        "v_mul_f32_e64 v150, v150, v18\n"
        "v_mfma_f32_16x16x32_bf16 v[76:79], a[60:63], v[92:95], v[76:79]\n"
        "ds_read_b64_tr_b16 v[90:91], v33 offset:24832\n"
        "buffer_load_dwordx4 v44, s[20:23], s46 offen sc1 lds\n"
        "v_mfma_f32_16x16x32_bf16 v[80:83], a[64:67], v[92:95], v[80:83]\n"
        "s_lshl1_add_u32 s44, s81, 0xa500\n"
        "v_mul_f32 v151, v151, v18\n"
        "v_mul_f32 v172, v125, v14\n"
        "v_mfma_f32_16x16x32_bf16 v[84:87], v[92:95], a[68:71], v[84:87]\n"
        "ds_read_b64_tr_b16 v[92:93], v33 offset:26624\n"
        "v_mul_f32 v173, v125, v15\n"
        "v_mfma_f32_16x16x32_bf16 v[76:79], a[72:75], v[96:99], v[76:79]\n"
        "ds_read_b64_tr_b16 v[94:95], v33 offset:26880\n"
        "v_mul_f32 v174, v125, v16\n"
        "v_mfma_f32_16x16x32_bf16 v[80:83], a[76:79], v[96:99], v[80:83]\n"
        "v_mul_f32 v175, v125, v17\n"
        "v_mul_f32 v160, v123, v10\n"
        "v_mfma_f32_16x16x32_bf16 v[84:87], v[96:99], a[80:83], v[84:87]\n"
        "ds_read_b64_tr_b16 v[96:97], v33 offset:28672\n"
        "v_mul_f32 v161, v123, v11\n"
        "v_mfma_f32_16x16x32_bf16 v[76:79], a[84:87], v[100:103], v[76:79]\n"
        "ds_read_b64_tr_b16 v[98:99], v33 offset:28928\n"
        "v_mul_f32 v162, v123, v12\n"
        "v_mfma_f32_16x16x32_bf16 v[80:83], a[88:91], v[100:103], v[80:83]\n"
        "v_mul_f32 v163, v123, v13\n"
        "v_mul_f32 v152, v152, v18\n"
        "v_mfma_f32_16x16x32_bf16 v[84:87], v[100:103], a[92:95], v[84:87]\n"
        "s_waitcnt lgkmcnt(3)\n"
        "ds_read_b64_tr_b16 v[100:101], v33 offset:30720\n"
        "v_mul_f32 v153, v153, v18\n"
        "v_mfma_f32_16x16x32_bf16 v[76:79], a[96:99], v[104:107], v[76:79]\n"
        "ds_read_b64_tr_b16 v[102:103], v33 offset:30976\n"
        "v_mul_f32 v154, v154, v18\n"
        "v_mfma_f32_16x16x32_bf16 v[80:83], a[100:103], v[104:107], v[80:83]\n"
        "v_mul_f32 v155, v155, v18\n"
        "v_add_u32 v188, s44, v4\n"
        "v_mfma_f32_16x16x32_bf16 v[84:87], v[104:107], a[104:107], v[84:87]\n"
        "s_add_u32 s44, s79, 0x12500\n"
        "v_add_u32 v189, s44, v4\n"
        "v_fmac_f32 v168, v68, v130\n"
        "v_mfma_f32_16x16x32_bf16 v[76:79], a[108:111], v[108:111], v[76:79]\n"
        "s_waitcnt vmcnt(8)\n"
        "ds_read_b128 v[184:187], v188\n"
        "v_fmac_f32 v169, v69, v130\n"
        "v_mfma_f32_16x16x32_bf16 v[80:83], a[112:115], v[108:111], v[80:83]\n"
        "ds_read_b128 v[180:183], v188 offset:1024\n"
        "v_fmac_f32 v170, v70, v130\n"
        "v_mfma_f32_16x16x32_bf16 v[84:87], v[108:111], a[116:119], v[84:87]\n"
        "v_fmac_f32 v171, v71, v130\n"
        "v_cvt_pk_bf16_f32 v168, v168, v169\n"
        "v_mfma_f32_16x16x32_bf16 v[76:79], a[120:123], v[112:115], v[76:79]\n"
        "v_cvt_pk_bf16_f32 v169, v170, v171\n"
        "v_fmac_f32 v156, v64, v120\n"
        "v_mfma_f32_16x16x32_bf16 v[80:83], a[124:127], v[112:115], v[80:83]\n"
        "v_fmac_f32 v157, v65, v120\n"
        "v_fmac_f32 v158, v66, v120\n"
        "v_mfma_f32_16x16x32_bf16 v[84:87], v[112:115], a[128:131], v[84:87]\n"
        "v_fmac_f32 v159, v67, v120\n"
        "v_cvt_pk_bf16_f32 v156, v156, v157\n"
        "v_mfma_f32_16x16x32_bf16 v[76:79], a[132:135], v[116:119], v[76:79]\n"
        "s_waitcnt lgkmcnt(1)\n"
        "v_max3_u32 v179, v184, v184, v185\n"
        "v_max3_u32 v179, v179, v186, v187\n"
        "v_mfma_f32_16x16x32_bf16 v[80:83], a[136:139], v[116:119], v[80:83]\n"
        "s_waitcnt vmcnt(6)\n"
        "ds_read_b128 v[184:187], v188 offset:2048\n"
        "v_cvt_pk_bf16_f32 v157, v158, v159\n"
        "v_mfma_f32_16x16x32_bf16 v[84:87], v[116:119], a[140:143], v[84:87]\n"
        "s_waitcnt lgkmcnt(1)\n"
        "v_max3_u32 v179, v179, v180, v181\n"
        "v_max3_u32 v179, v179, v182, v183\n"
        "v_mfma_f32_16x16x32_bf16 v[76:79], a[144:147], v[88:91], v[76:79]\n"
        "ds_read_b128 v[180:183], v188 offset:3072\n"
        "v_fmac_f32 v164, v56, v128\n"
        "v_mfma_f32_16x16x32_bf16 v[80:83], a[148:151], v[88:91], v[80:83]\n"
        "v_fmac_f32 v165, v57, v128\n"
        "v_fmac_f32 v166, v58, v128\n"
        "v_mfma_f32_16x16x32_bf16 v[84:87], v[88:91], a[152:155], v[84:87]\n"
        "v_fmac_f32 v167, v59, v128\n"
        "v_cvt_pk_bf16_f32 v164, v164, v165\n"
        "v_mfma_f32_16x16x32_bf16 v[76:79], a[156:159], v[92:95], v[76:79]\n"
        "v_cvt_pk_bf16_f32 v165, v166, v167\n"
        "v_mfma_f32_16x16x16_bf16 v[68:71], v[168:169], v[156:157], v[26:29]\n"
        "s_waitcnt lgkmcnt(1)\n"
        "v_max3_u32 v179, v179, v184, v185\n"
        "v_max3_u32 v179, v179, v186, v187\n"
        "v_mfma_f32_16x16x16_bf16 v[56:59], v[164:165], v[156:157], v[22:25]\n"
        "s_waitcnt vmcnt(4)\n"
        "ds_read_b128 v[184:187], v188 offset:4096\n"
        "v_fmac_f32 v148, v72, v136\n"
        "v_mfma_f32_16x16x32_bf16 v[80:83], a[160:163], v[92:95], v[80:83]\n"
        "s_waitcnt lgkmcnt(1)\n"
        "v_max3_u32 v179, v179, v180, v181\n"
        "v_max3_u32 v179, v179, v182, v183\n"
        "v_mfma_f32_16x16x32_bf16 v[84:87], v[92:95], a[164:167], v[84:87]\n"
        "ds_read_b128 v[180:183], v188 offset:5120\n"
        "v_fmac_f32 v149, v73, v137\n"
        "v_mfma_f32_16x16x32_bf16 v[76:79], a[168:171], v[96:99], v[76:79]\n"
        "v_fmac_f32 v150, v74, v138\n"
        "v_fmac_f32 v151, v75, v139\n"
        "v_mfma_f32_16x16x32_bf16 v[80:83], a[172:175], v[96:99], v[80:83]\n"
        "v_cvt_pk_bf16_f32 v48, v148, v149\n"
        "v_cvt_pk_bf16_f32 v49, v150, v151\n"
        "v_mfma_f32_16x16x32_bf16 v[84:87], v[96:99], a[176:179], v[84:87]\n"
        "v_fmac_f32 v144, v60, v132\n"
        "v_fmac_f32 v145, v61, v133\n"
        "v_mfma_f32_16x16x32_bf16 v[76:79], a[180:183], v[100:103], v[76:79]\n"
        "s_waitcnt lgkmcnt(1)\n"
        "v_max3_u32 v179, v179, v184, v185\n"
        "v_max3_u32 v179, v179, v186, v187\n"
        "v_mfma_f32_16x16x32_bf16 v[80:83], a[184:187], v[100:103], v[80:83]\n"
        "s_waitcnt vmcnt(2)\n"
        "ds_read_b128 v[184:187], v188 offset:6144\n"
        "v_fmac_f32 v146, v62, v134\n"
        "v_mfma_f32_16x16x32_bf16 v[84:87], v[100:103], a[188:191], v[84:87]\n"
        "s_waitcnt lgkmcnt(1)\n"
        "v_max3_u32 v179, v179, v180, v181\n"
        "v_max3_u32 v179, v179, v182, v183\n"
        "v_fmac_f32 v147, v63, v135\n"
        "v_cvt_pk_bf16_f32 v46, v144, v145\n"
        "v_cvt_pk_bf16_f32 v47, v146, v147\n"
        "v_pk_fma_f32 v[160:161], v[76:77], v[122:123], v[160:161] op_sel_hi:[1,0,1]\n"
        "v_pk_fma_f32 v[162:163], v[78:79], v[122:123], v[162:163] op_sel_hi:[1,0,1]\n"
        "v_pk_fma_f32 v[172:173], v[80:81], v[124:125], v[172:173] op_sel_hi:[1,0,1]\n"
        "v_pk_fma_f32 v[174:175], v[82:83], v[124:125], v[174:175] op_sel_hi:[1,0,1]\n"
        "v_pk_fma_f32 v[152:153], v[84:85], v[140:141], v[152:153]\n"
        "v_pk_fma_f32 v[154:155], v[86:87], v[142:143], v[154:155]\n"
        "v_cvt_pk_bf16_f32 v160, v160, v161\n"
        "v_cvt_pk_bf16_f32 v161, v162, v163\n"
        "v_cvt_pk_bf16_f32 v172, v172, v173\n"
        "v_cvt_pk_bf16_f32 v173, v174, v175\n"
        "s_add_u32 s90, s82, 0x440000\n"
        "v_add_f32 v56, s74, v56\n"
        "v_mfma_f32_16x16x16_bf16 v[80:83], v[172:173], v[160:161], v[26:29]\n"
        "ds_read_b128 v[180:183], v188 offset:7168\n"
        "v_add_f32_e64 v57, s74, v57\n"
        "v_mfma_f32_16x16x16_bf16 v[76:79], v[168:169], v[160:161], v[22:25]\n"
        "s_waitcnt lgkmcnt(1)\n"
        "v_cvt_pk_bf16_f32 v50, v152, v153\n"
        "v_cvt_pk_bf16_f32 v51, v154, v155\n"
        "s_add_u32 s87, s82, 0x444000\n"
        "v_max3_u32 v179, v179, v184, v185\n"
        "v_max3_u32 v179, v179, v186, v187\n"
        "v_add_f32 v58, s74, v58\n"
        "v_add_f32 v59, s74, v59\n"
        "v_max_f32 v76, v76, v80\n"
        "v_max_f32 v77, v77, v81\n"
        "v_max_f32 v78, v78, v82\n"
        "v_max_f32 v79, v79, v83\n"
        "v_max3_f32 v174, v76, v77, v78\n"
        "v_max_f32 v175, v174, v79\n"
        "v_max_f32 v174, v174, v79\n"
        "v_max_f32 v56, v56, v68\n"
        "v_max_f32 v57, v57, v69\n"
        "v_permlane32_swap_b32 v174, v175\n"
        "v_max_f32 v80, v174, v175\n"
        "v_max_f32 v174, v174, v175\n"
        "v_max_f32 v58, v58, v70\n"
        "v_max_f32 v59, v59, v71\n"
        "v_permlane16_swap_b32 v174, v80\n"
        "v_max_f32 v174, v174, v80\n"
        "v_sub_f32 v76, v76, v174\n"
        "v_sub_f32 v77, v77, v174\n"
        "v_sub_f32 v78, v78, v174\n"
        "v_sub_f32 v79, v79, v174\n"
        "v_exp_f32 v76, v76\n"
        "v_exp_f32 v77, v77\n"
        "v_exp_f32 v78, v78\n"
        "v_exp_f32 v79, v79\n"
        "v_cvt_pk_bf16_f32 v154, v76, v77\n"
        "v_cvt_pk_bf16_f32 v155, v78, v79\n"
        "v_and_b32 v152, v154, v30\n"
        "v_and_b32 v153, v155, v31\n"
        "v_xor_b32 v154, v154, v152\n"
        "v_xor_b32 v155, v155, v153\n"
        "v_max3_f32 v166, v56, v57, v58\n"
        "v_max_f32 v167, v166, v59\n"
        "v_mfma_f32_16x16x32_bf16 v[160:163], v[6:9], v[152:155], 0\n"
        "s_waitcnt vmcnt(0)\n"
        "ds_read_b128 v[184:187], v189\n"
        "v_max_f32 v166, v166, v59\n"
        "v_mfma_f32_16x16x32_bf16 v[84:87], v[48:51], v[152:155], 0\n"
        "s_waitcnt lgkmcnt(1)\n"
        "v_permlane32_swap_b32 v166, v167\n"
        "v_max3_u32 v179, v179, v180, v181\n"
        "v_max3_u32 v179, v179, v182, v183\n"
        "v_rcp_f32 v160, v160\n"
        "v_max_f32 v68, v166, v167\n"
        "v_max_f32 v166, v166, v167\n"
        "s_nop 0\n"
        "v_pk_mul_f32 v[84:85], v[84:85], v[160:161] op_sel_hi:[1,0]\n"
        "v_pk_mul_f32 v[86:87], v[86:87], v[160:161] op_sel_hi:[1,0]\n"
        "v_cvt_pk_bf16_f32 v84, v84, v85\n"
        "v_cvt_pk_bf16_f32 v85, v86, v87\n"
        "buffer_store_dwordx2 v[84:85], v34, s[20:23], s87 offen sc1\n"
        "ds_read_b128 v[180:183], v189 offset:1024\n"
        "s_waitcnt lgkmcnt(1)\n"
        "v_max3_u32 v179, v179, v184, v185\n"
        "v_max3_u32 v179, v179, v186, v187\n"
        "v_permlane16_swap_b32 v166, v68\n"
        "v_max_f32 v166, v166, v68\n"
        "v_sub_f32 v56, v56, v166\n"
        "v_sub_f32 v57, v57, v166\n"
        "v_sub_f32 v58, v58, v166\n"
        "v_sub_f32 v59, v59, v166\n"
        "v_exp_f32 v56, v56\n"
        "v_exp_f32 v57, v57\n"
        "v_exp_f32 v58, v58\n"
        "v_exp_f32 v59, v59\n"
        "v_cvt_pk_bf16_f32 v146, v56, v57\n"
        "v_cvt_pk_bf16_f32 v147, v58, v59\n"
        "s_waitcnt lgkmcnt(0)\n"
        "v_max3_u32 v179, v179, v180, v181\n"
        "v_max3_u32 v179, v179, v182, v183\n"
        "v_cmp_eq_u32 vcc, -1, v179\n"
        "s_cbranch_vccnz L_slow_x21_%=\n"
        "L_ret_x21_%=:\n"
        "v_and_b32 v144, v146, v30\n"
        "v_and_b32 v145, v147, v31\n"
        "v_xor_b32 v146, v146, v144\n"
        "v_xor_b32 v147, v147, v145\n"
        "s_nop 1\n"
        "v_mfma_f32_16x16x32_bf16 v[60:63], v[6:9], v[144:147], 0\n"
        "v_mfma_f32_16x16x32_bf16 v[64:67], v[46:49], v[144:147], 0\n"
        "s_nop 6\n"
        "v_rcp_f32 v60, v60\n"
        "s_nop 0\n"
        "v_mul_f32 v64, v64, v60\n"
        "v_mul_f32 v65, v65, v60\n"
        "v_mul_f32 v66, v66, v60\n"
        "v_mul_f32 v67, v67, v60\n"
        "v_cvt_pk_bf16_f32 v64, v64, v65\n"
        "v_cvt_pk_bf16_f32 v65, v66, v67\n"
        "buffer_store_dwordx2 v[64:65], v34, s[20:23], s90 offen sc1\n"
        "v_add_u32 v32, 0xa500, v32\n"
        "v_add_u32 v33, 0xa500, v33\n"
        "v_add_u32 v5, 0xa500, v5\n"
        "v_add_u32 v35, 0xa500, v35\n"
        "s_mul_i32 s40, s51, 0x800\n"
        "v_add_u32 v176, s40, v32\n"
        "v_add_u32 v177, s40, v33\n"
        "s_lshl_b32 s40, s51, 9\n"
        "v_add_u32_e64 v45, s40, v5\n"
        "s_barrier\n"
        "ds_read_b64_tr_b16 v[88:89], v45\n"
        "ds_read_b64_tr_b16 v[90:91], v45 offset:64\n"
        "ds_read_b64_tr_b16 v[92:93], v176\n"
        "ds_read_b64_tr_b16 v[94:95], v176 offset:256\n"
        "ds_read_b64_tr_b16 v[96:97], v177\n"
        "ds_read_b64_tr_b16 v[98:99], v177 offset:256\n"
        "ds_read_b64_tr_b16 v[100:101], v45 offset:2048\n"
        "ds_read_b64_tr_b16 v[102:103], v45 offset:2112\n"
        "ds_read_b64_tr_b16 v[104:105], v176 offset:8192\n"
        "ds_read_b64_tr_b16 v[106:107], v176 offset:8448\n"
        "ds_read_b64_tr_b16 v[108:109], v177 offset:8192\n"
        "ds_read_b64_tr_b16 v[110:111], v177 offset:8448\n"
        "s_waitcnt lgkmcnt(10)\n"
        "v_mfma_f32_16x16x32_bf16 v[120:123], v[88:91], v[88:91], 0\n"
        "ds_read_b64_tr_b16 v[112:113], v45 offset:4096\n"
        "v_mfma_f32_16x16x32_bf16 v[132:135], v[6:9], v[88:91], 0\n"
        "s_waitcnt lgkmcnt(8)\n"
        "ds_read_b64_tr_b16 v[114:115], v45 offset:4160\n"
        "v_mfma_f32_16x16x32_bf16 v[124:127], v[92:95], v[92:95], 0\n"
        "ds_read_b64_tr_b16 v[116:117], v176 offset:16384\n"
        "v_mfma_f32_16x16x32_bf16 v[136:139], v[6:9], v[92:95], 0\n"
        "s_waitcnt lgkmcnt(6)\n"
        "ds_read_b64_tr_b16 v[118:119], v176 offset:16640\n"
        "v_mfma_f32_16x16x32_bf16 v[128:131], v[96:99], v[96:99], 0\n"
        "ds_read_b64_tr_b16 v[88:89], v177 offset:16384\n"
        "v_mfma_f32_16x16x32_bf16 v[140:143], v[6:9], v[96:99], 0\n"
        "ds_read_b64_tr_b16 v[90:91], v177 offset:16640\n"
        "v_mov_b32 v184, s62\n"
        "v_mfma_f32_16x16x32_bf16 v[120:123], v[100:103], v[100:103], v[120:123]\n"
        "ds_read_b64_tr_b16 v[92:93], v45 offset:6144\n"
        "v_mov_b32 v185, s63\n"
        "v_mfma_f32_16x16x32_bf16 v[132:135], v[6:9], v[100:103], v[132:135]\n"
        "s_waitcnt lgkmcnt(5)\n"
        "ds_read_b64_tr_b16 v[94:95], v45 offset:6208\n"
        "v_lshlrev_b32_e64 v186, 24, v0\n"
        "v_mfma_f32_16x16x32_bf16 v[124:127], v[104:107], v[104:107], v[124:127]\n"
        "s_lshl3_add_u32 s91, s52, s53\n"
        "ds_read_b64_tr_b16 v[96:97], v176 offset:24576\n"
        "s_lshl_b32 s91, s91, 6\n"
        "v_mfma_f32_16x16x32_bf16 v[136:139], v[6:9], v[104:107], v[136:139]\n"
        "s_add_u32 s91, s91, 0x5000\n"
        "ds_read_b64_tr_b16 v[98:99], v176 offset:24832\n"
        "buffer_store_dwordx2 v[184:185], v186, s[20:23], s91 offen sc1\n"
        "v_mfma_f32_16x16x32_bf16 v[128:131], v[108:111], v[108:111], v[128:131]\n"
        "ds_read_b64_tr_b16 v[100:101], v177 offset:24576\n"
        "v_mov_b32 v180, -1\n"
        "v_mfma_f32_16x16x32_bf16 v[140:143], v[6:9], v[108:111], v[140:143]\n"
        "ds_read_b64_tr_b16 v[102:103], v177 offset:24832\n"
        "v_mov_b32 v181, -1\n"
        "v_mfma_f32_16x16x32_bf16 v[120:123], v[112:115], v[112:115], v[120:123]\n"
        "ds_read_b64_tr_b16 v[104:105], v5\n"
        "v_mov_b32 v182, -1\n"
        "v_mfma_f32_16x16x32_bf16 v[132:135], v[6:9], v[112:115], v[132:135]\n"
        "s_waitcnt lgkmcnt(5)\n"
        "ds_read_b64_tr_b16 v[106:107], v5 offset:64\n"
        "v_mov_b32 v183, -1\n"
        "v_mfma_f32_16x16x32_bf16 v[124:127], v[116:119], v[116:119], v[124:127]\n"
        "ds_read_b64_tr_b16 v[108:109], v32\n"
        "v_lshrrev_b32 v179, 3, v0\n"
        "v_mfma_f32_16x16x32_bf16 v[136:139], v[6:9], v[116:119], v[136:139]\n"
        "ds_read_b64_tr_b16 v[110:111], v32 offset:256\n"
        "v_mul_u32_u24 v179, 0xa000, v179\n"
        "v_mfma_f32_16x16x32_bf16 v[128:131], v[88:91], v[88:91], v[128:131]\n"
        "s_mov_b32 s46, 0x70\n"
        "ds_read_b64_tr_b16 v[112:113], v5 offset:512\n"
        "v_and_or_b32 v179, v4, s46, v179\n"
        "v_mfma_f32_16x16x32_bf16 v[140:143], v[6:9], v[88:91], v[140:143]\n"
        "s_lshl_b32 s46, s50, 7\n"
        "ds_read_b64_tr_b16 v[114:115], v5 offset:576\n"
        "s_add_u32 s46, s46, 0xad4000\n"
        "v_mfma_f32_16x16x32_bf16 v[120:123], v[92:95], v[92:95], v[120:123]\n"
        "ds_read_b64_tr_b16 v[116:117], v32 offset:2048\n"
        "buffer_store_dwordx4 v[180:183], v179, s[20:23], s46 offen sc1\n"
        "v_mfma_f32_16x16x32_bf16 v[132:135], v[6:9], v[92:95], v[132:135]\n"
        "s_waitcnt lgkmcnt(5)\n"
        "ds_read_b64_tr_b16 v[118:119], v32 offset:2304\n"
        "s_lshl_b32 s46, s50, 7\n"
        "v_mfma_f32_16x16x32_bf16 v[124:127], v[96:99], v[96:99], v[124:127]\n"
        "s_add_u32 s46, s46, 0xc14000\n"
        "ds_read_b64_tr_b16 v[88:89], v5 offset:1024\n"
        "v_mfma_f32_16x16x32_bf16 v[136:139], v[6:9], v[96:99], v[136:139]\n"
        "ds_read_b64_tr_b16 v[90:91], v5 offset:1088\n"
        "v_mfma_f32_16x16x32_bf16 v[128:131], v[100:103], v[100:103], v[128:131]\n"
        "ds_read_b64_tr_b16 v[92:93], v32 offset:4096\n"
        "buffer_store_dwordx4 v[180:183], v179, s[20:23], s46 offen sc1\n"
        "v_mfma_f32_16x16x32_bf16 v[140:143], v[6:9], v[100:103], v[140:143]\n"
        "ds_read_b64_tr_b16 v[94:95], v32 offset:4352\n"
        "v_cndmask_b32 v120, v120, v121, s[64:65]\n"
        "v_mfma_f32_16x16x32_bf16 v[56:59], a[4:7], v[104:107], 0\n"
        "ds_read_b64_tr_b16 v[96:97], v5 offset:1536\n"
        "v_cndmask_b32 v120, v120, v122, s[66:67]\n"
        "v_mfma_f32_16x16x32_bf16 v[60:63], v[104:107], a[8:11], 0\n"
        "s_waitcnt lgkmcnt(5)\n"
        "ds_read_b64_tr_b16 v[98:99], v5 offset:1600\n"
        "v_cndmask_b32 v133, v120, v123, s[68:69]\n"
        "v_mfma_f32_16x16x32_bf16 v[64:67], a[0:3], v[108:111], 0\n"
        "ds_read_b64_tr_b16 v[100:101], v32 offset:6144\n"
        "v_cndmask_b32 v124, v124, v125, s[64:65]\n"
        "v_mfma_f32_16x16x32_bf16 v[68:71], a[4:7], v[108:111], 0\n"
        "ds_read_b64_tr_b16 v[102:103], v32 offset:6400\n"
        "v_cndmask_b32 v124, v124, v126, s[66:67]\n"
        "v_mfma_f32_16x16x32_bf16 v[72:75], v[108:111], a[8:11], 0\n"
        "ds_read_b64_tr_b16 v[104:105], v5 offset:2048\n"
        "v_cndmask_b32 v137, v124, v127, s[68:69]\n"
        "v_mfma_f32_16x16x32_bf16 v[56:59], a[16:19], v[112:115], v[56:59]\n"
        "ds_read_b64_tr_b16 v[106:107], v5 offset:2112\n"
        "v_cndmask_b32 v128, v128, v129, s[64:65]\n"
        "v_mfma_f32_16x16x32_bf16 v[60:63], v[112:115], a[20:23], v[60:63]\n"
        "ds_read_b64_tr_b16 v[108:109], v32 offset:8192\n"
        "v_cndmask_b32 v128, v128, v130, s[66:67]\n"
        "v_mfma_f32_16x16x32_bf16 v[64:67], a[12:15], v[116:119], v[64:67]\n"
        "ds_read_b64_tr_b16 v[110:111], v32 offset:8448\n"
        "v_cndmask_b32 v141, v128, v131, s[68:69]\n"
        "v_mfma_f32_16x16x32_bf16 v[68:71], a[16:19], v[116:119], v[68:71]\n"
        "s_mul_i32 s48, s51, 0x180\n"
        "ds_read_b64_tr_b16 v[112:113], v5 offset:2560\n"
        "s_add_u32 s48, s48, 0x27000\n"
        "v_mfma_f32_16x16x32_bf16 v[72:75], v[116:119], a[20:23], v[72:75]\n"
        "s_waitcnt lgkmcnt(5)\n"
        "ds_read_b64_tr_b16 v[114:115], v5 offset:2624\n"
        "v_lshl_add_u32 v178, v2, 3, s48\n"
        "v_mfma_f32_16x16x32_bf16 v[56:59], a[28:31], v[88:91], v[56:59]\n"
        "s_mov_b64 exec, s[70:71]\n"
        "ds_write_b64 v178, v[132:133]\n"
        "ds_write_b64 v178, v[136:137] offset:128\n"
        "ds_write_b64 v178, v[140:141] offset:256\n"
        "s_mov_b64 exec, -1\n"
        "v_mfma_f32_16x16x32_bf16 v[60:63], v[88:91], a[32:35], v[60:63]\n"
        "s_mov_b32 s48, 0x27000\n"
        "ds_read_b64_tr_b16 v[116:117], v32 offset:10240\n"
        "v_lshl_add_u32 v178, v1, 3, s48\n"
        "v_mfma_f32_16x16x32_bf16 v[64:67], a[24:27], v[92:95], v[64:67]\n"
        "s_lshl_b32 s48, s51, 9\n"
        "ds_read_b64_tr_b16 v[118:119], v32 offset:10496\n"
        "s_add_u32 s48, s48, 0x27800\n"
        "v_mfma_f32_16x16x32_bf16 v[68:71], a[28:31], v[92:95], v[68:71]\n"
        "ds_read_b64_tr_b16 v[88:89], v5 offset:3072\n"
        "v_lshl_add_u32 v45, v1, 2, s48\n"
        "v_mfma_f32_16x16x32_bf16 v[72:75], v[92:95], a[32:35], v[72:75]\n"
        "ds_read_b64_tr_b16 v[90:91], v5 offset:3136\n"
        "v_lshl_add_u32 v176, v2, 2, s48\n"
        "v_mfma_f32_16x16x32_bf16 v[56:59], a[40:43], v[96:99], v[56:59]\n"
        "ds_read_b64_tr_b16 v[92:93], v32 offset:12288\n"
        "v_lshl_add_u32 v177, v3, 4, s48\n"
        "v_mfma_f32_16x16x32_bf16 v[60:63], v[96:99], a[44:47], v[60:63]\n"
        "s_waitcnt lgkmcnt(5)\n"
        "ds_read_b64_tr_b16 v[94:95], v32 offset:12544\n"
        "s_barrier\n"
        "v_mfma_f32_16x16x32_bf16 v[64:67], a[36:39], v[100:103], v[64:67]\n"
        "ds_read_b64 v[120:121], v178\n"
        "v_mfma_f32_16x16x32_bf16 v[68:71], a[40:43], v[100:103], v[68:71]\n"
        "ds_read_b64 v[122:123], v178 offset:384\n"
        "v_mfma_f32_16x16x32_bf16 v[72:75], v[100:103], a[44:47], v[72:75]\n"
        "ds_read_b64 v[124:125], v178 offset:768\n"
        "v_mfma_f32_16x16x32_bf16 v[56:59], a[52:55], v[104:107], v[56:59]\n"
        "ds_read_b64 v[126:127], v178 offset:1152\n"
        "v_mfma_f32_16x16x32_bf16 v[60:63], v[104:107], a[56:59], v[60:63]\n"
        "ds_read_b64_tr_b16 v[188:189], v35\n"
        "v_mfma_f32_16x16x32_bf16 v[64:67], a[48:51], v[108:111], v[64:67]\n"
        "ds_read_b64_tr_b16 v[190:191], v35 offset:1024\n"
        "v_mfma_f32_16x16x32_bf16 v[68:71], a[52:55], v[108:111], v[68:71]\n"
        "ds_read_b64_tr_b16 v[96:97], v5 offset:3584\n"
        "v_mfma_f32_16x16x32_bf16 v[72:75], v[108:111], a[56:59], v[72:75]\n"
        "s_waitcnt lgkmcnt(5)\n"
        "ds_read_b64_tr_b16 v[98:99], v5 offset:3648\n"
        "v_add_f32 v120, v120, v122\n"
        "v_mfma_f32_16x16x32_bf16 v[56:59], a[64:67], v[112:115], v[56:59]\n"
        "ds_read_b64_tr_b16 v[100:101], v32 offset:14336\n"
        "v_mfma_f32_16x16x32_bf16 v[60:63], v[112:115], a[68:71], v[60:63]\n"
        "s_waitcnt lgkmcnt(5)\n"
        "ds_read_b64_tr_b16 v[102:103], v32 offset:14592\n"
        "v_add_f32 v124, v124, v126\n"
        "v_mfma_f32_16x16x32_bf16 v[64:67], a[60:63], v[116:119], v[64:67]\n"
        "ds_read_b64_tr_b16 v[104:105], v5 offset:4096\n"
        "v_add_f32 v121, v121, v123\n"
        "v_mfma_f32_16x16x32_bf16 v[68:71], a[64:67], v[116:119], v[68:71]\n"
        "ds_read_b64_tr_b16 v[106:107], v5 offset:4160\n"
        "v_add_f32 v125, v125, v127\n"
        "v_mfma_f32_16x16x32_bf16 v[72:75], v[116:119], a[68:71], v[72:75]\n"
        "ds_read_b64_tr_b16 v[108:109], v32 offset:16384\n"
        "v_add_f32 v120, v120, v124\n"
        "v_mfma_f32_16x16x32_bf16 v[56:59], a[76:79], v[88:91], v[56:59]\n"
        "ds_read_b64_tr_b16 v[110:111], v32 offset:16640\n"
        "v_add_f32 v121, v121, v125\n"
        "v_mfma_f32_16x16x32_bf16 v[60:63], v[88:91], a[80:83], v[60:63]\n"
        "ds_read_b64_tr_b16 v[112:113], v5 offset:4608\n"
        "v_mul_f32 v120, s60, v120\n"
        "v_mfma_f32_16x16x32_bf16 v[64:67], a[72:75], v[92:95], v[64:67]\n"
        "ds_read_b64_tr_b16 v[114:115], v5 offset:4672\n"
        "v_mul_f32 v121, s60, v121\n"
        "v_mfma_f32_16x16x32_bf16 v[68:71], a[76:79], v[92:95], v[68:71]\n"
        "ds_read_b64_tr_b16 v[116:117], v32 offset:18432\n"
        "v_fma_f32 v121, -v120, v120, v121\n"
        "v_mfma_f32_16x16x32_bf16 v[72:75], v[92:95], a[80:83], v[72:75]\n"
        "s_waitcnt lgkmcnt(5)\n"
        "ds_read_b64_tr_b16 v[118:119], v32 offset:18688\n"
        "v_max_f32 v121, 0, v121\n"
        "v_mfma_f32_16x16x32_bf16 v[56:59], a[88:91], v[96:99], v[56:59]\n"
        "ds_read_b64_tr_b16 v[88:89], v5 offset:5120\n"
        "v_add_f32 v121, s61, v121\n"
        "v_mfma_f32_16x16x32_bf16 v[60:63], v[96:99], a[92:95], v[60:63]\n"
        "v_rsq_f32 v121, v121\n"
        "v_mfma_f32_16x16x32_bf16 v[64:67], a[84:87], v[100:103], v[64:67]\n"
        "ds_read_b64_tr_b16 v[90:91], v5 offset:5184\n"
        "v_mul_f32 v120, -v120, v121\n"
        "v_mfma_f32_16x16x32_bf16 v[68:71], a[88:91], v[100:103], v[68:71]\n"
        "ds_write2_b32 v45, v121, v120 offset1:64\n"
        "v_mfma_f32_16x16x32_bf16 v[72:75], v[100:103], a[92:95], v[72:75]\n"
        "ds_read2_b32 v[128:129], v176 offset0:0 offset1:64\n"
        "v_mfma_f32_16x16x32_bf16 v[56:59], a[100:103], v[104:107], v[56:59]\n"
        "ds_read2_b32 v[130:131], v176 offset0:16 offset1:80\n"
        "v_mfma_f32_16x16x32_bf16 v[60:63], v[104:107], a[104:107], v[60:63]\n"
        "s_waitcnt lgkmcnt(4)\n"
        "ds_read2_b32 v[124:125], v176 offset0:32 offset1:96\n"
        "v_mfma_f32_16x16x32_bf16 v[64:67], a[96:99], v[108:111], v[64:67]\n"
        "ds_read_b64_tr_b16 v[92:93], v32 offset:20480\n"
        "v_mfma_f32_16x16x32_bf16 v[68:71], a[100:103], v[108:111], v[68:71]\n"
        "ds_read_b64_tr_b16 v[94:95], v32 offset:20736\n"
        "v_mfma_f32_16x16x32_bf16 v[72:75], v[108:111], a[104:107], v[72:75]\n"
        "ds_read_b128 v[144:147], v177 offset:256\n"
        "v_mfma_f32_16x16x32_bf16 v[56:59], a[112:115], v[112:115], v[56:59]\n"
        "ds_read_b64_tr_b16 v[96:97], v5 offset:5632\n"
        "v_mfma_f32_16x16x32_bf16 v[60:63], v[112:115], a[116:119], v[60:63]\n"
        "ds_read_b64_tr_b16 v[98:99], v5 offset:5696\n"
        "v_mfma_f32_16x16x32_bf16 v[64:67], a[108:111], v[116:119], v[64:67]\n"
        "ds_read_b64_tr_b16 v[100:101], v32 offset:22528\n"
        "v_mfma_f32_16x16x32_bf16 v[68:71], a[112:115], v[116:119], v[68:71]\n"
        "ds_read_b64_tr_b16 v[102:103], v32 offset:22784\n"
        "v_mfma_f32_16x16x32_bf16 v[72:75], v[116:119], a[116:119], v[72:75]\n"
        "s_waitcnt lgkmcnt(5)\n"
        "ds_read_b128 v[132:135], v177\n"
        "v_mfma_f32_16x16x32_bf16 v[56:59], a[124:127], v[88:91], v[56:59]\n"
        "ds_read_b64_tr_b16 v[104:105], v5 offset:6144\n"
        "v_mfma_f32_16x16x32_bf16 v[60:63], v[88:91], a[128:131], v[60:63]\n"
        "ds_read_b64_tr_b16 v[106:107], v5 offset:6208\n"
        "v_mfma_f32_16x16x32_bf16 v[64:67], a[120:123], v[92:95], v[64:67]\n"
        "ds_read_b64_tr_b16 v[108:109], v32 offset:24576\n"
        "v_mfma_f32_16x16x32_bf16 v[68:71], a[124:127], v[92:95], v[68:71]\n"
        "ds_read_b64_tr_b16 v[110:111], v32 offset:24832\n"
        "v_mfma_f32_16x16x32_bf16 v[72:75], v[92:95], a[128:131], v[72:75]\n"
        "s_waitcnt lgkmcnt(5)\n"
        "ds_read_b128 v[148:151], v177 offset:320\n"
        "v_mfma_f32_16x16x32_bf16 v[56:59], a[136:139], v[96:99], v[56:59]\n"
        "ds_read_b64_tr_b16 v[112:113], v5 offset:6656\n"
        "v_mfma_f32_16x16x32_bf16 v[60:63], v[96:99], a[140:143], v[60:63]\n"
        "ds_read_b64_tr_b16 v[114:115], v5 offset:6720\n"
        "v_mfma_f32_16x16x32_bf16 v[64:67], a[132:135], v[100:103], v[64:67]\n"
        "ds_read_b64_tr_b16 v[116:117], v32 offset:26624\n"
        "v_mfma_f32_16x16x32_bf16 v[68:71], a[136:139], v[100:103], v[68:71]\n"
        "ds_read_b64_tr_b16 v[118:119], v32 offset:26880\n"
        "v_mfma_f32_16x16x32_bf16 v[72:75], v[100:103], a[140:143], v[72:75]\n"
        "s_waitcnt lgkmcnt(5)\n"
        "ds_read_b128 v[136:139], v177 offset:64\n"
        "v_mfma_f32_16x16x32_bf16 v[56:59], a[148:151], v[104:107], v[56:59]\n"
        "ds_read_b64_tr_b16 v[88:89], v5 offset:7168\n"
        "v_mfma_f32_16x16x32_bf16 v[60:63], v[104:107], a[152:155], v[60:63]\n"
        "ds_read_b64_tr_b16 v[90:91], v5 offset:7232\n"
        "v_mfma_f32_16x16x32_bf16 v[64:67], a[144:147], v[108:111], v[64:67]\n"
        "ds_read_b64_tr_b16 v[92:93], v32 offset:28672\n"
        "v_mfma_f32_16x16x32_bf16 v[68:71], a[148:151], v[108:111], v[68:71]\n"
        "ds_read_b64_tr_b16 v[94:95], v32 offset:28928\n"
        "v_mfma_f32_16x16x32_bf16 v[72:75], v[108:111], a[152:155], v[72:75]\n"
        "s_waitcnt lgkmcnt(5)\n"
        "ds_read_b128 v[152:155], v177 offset:384\n"
        "v_mfma_f32_16x16x32_bf16 v[56:59], a[160:163], v[112:115], v[56:59]\n"
        "ds_read_b64_tr_b16 v[96:97], v5 offset:7680\n"
        "v_mfma_f32_16x16x32_bf16 v[60:63], v[112:115], a[164:167], v[60:63]\n"
        "ds_read_b64_tr_b16 v[98:99], v5 offset:7744\n"
        "v_mfma_f32_16x16x32_bf16 v[64:67], a[156:159], v[116:119], v[64:67]\n"
        "ds_read_b64_tr_b16 v[100:101], v32 offset:30720\n"
        "v_mfma_f32_16x16x32_bf16 v[68:71], a[160:163], v[116:119], v[68:71]\n"
        "ds_read_b64_tr_b16 v[102:103], v32 offset:30976\n"
        "v_mfma_f32_16x16x32_bf16 v[72:75], v[116:119], a[164:167], v[72:75]\n"
        "s_waitcnt lgkmcnt(5)\n"
        "ds_read_b128 v[140:143], v177 offset:128\n"
        "s_mov_b32 s48, 0x3eb8aa3b\n"
        "v_mfma_f32_16x16x32_bf16 v[56:59], a[172:175], v[88:91], v[56:59]\n"
        "ds_read_b64_tr_b16 v[104:105], v33\n"
        "v_mul_f32 v120, s48, v130\n"
        "v_mfma_f32_16x16x32_bf16 v[60:63], v[88:91], a[176:179], v[60:63]\n"
        "ds_read_b64_tr_b16 v[106:107], v33 offset:256\n"
        "v_mul_f32 v121, s48, v131\n"
        "v_mfma_f32_16x16x32_bf16 v[64:67], a[168:171], v[92:95], v[64:67]\n"
        "ds_read_b64_tr_b16 v[108:109], v33 offset:2048\n"
        "v_mul_f32 v122, s48, v124\n"
        "v_mfma_f32_16x16x32_bf16 v[68:71], a[172:175], v[92:95], v[68:71]\n"
        "ds_read_b64_tr_b16 v[110:111], v33 offset:2304\n"
        "v_mul_f32 v123, s48, v125\n"
        "v_mfma_f32_16x16x32_bf16 v[72:75], v[92:95], a[176:179], v[72:75]\n"
        "s_waitcnt lgkmcnt(5)\n"
        "ds_read_b64_tr_b16 v[112:113], v33 offset:4096\n"
        "v_mul_f32 v164, v129, v14\n"
        "v_mfma_f32_16x16x32_bf16 v[56:59], a[184:187], v[96:99], v[56:59]\n"
        "s_add_u32 m0, s81, 0x6000\n"
        "ds_read_b64_tr_b16 v[114:115], v33 offset:4352\n"
        "v_mul_f32_e64 v165, v129, v15\n"
        "v_mfma_f32_16x16x32_bf16 v[60:63], v[96:99], a[188:191], v[60:63]\n"
        "s_add_u32 s46, s83, 0x440000\n"
        "ds_read_b64_tr_b16 v[116:117], v33 offset:6144\n"
        "buffer_load_dwordx4 v4, s[20:23], s46 offen sc1 lds\n"
        "v_mfma_f32_16x16x32_bf16 v[64:67], a[180:183], v[100:103], v[64:67]\n"
        "s_add_u32 m0, s81, 0x6400\n"
        "ds_read_b64_tr_b16 v[118:119], v33 offset:6400\n"
        "v_mul_f32 v166, v129, v16\n"
        "v_mfma_f32_16x16x32_bf16 v[68:71], a[184:187], v[100:103], v[68:71]\n"
        "s_add_u32 s46, s84, 0x440000\n"
        "ds_read_b64_tr_b16 v[88:89], v33 offset:8192\n"
        "v_mul_f32 v167, v129, v17\n"
        "v_mfma_f32_16x16x32_bf16 v[72:75], v[100:103], a[188:191], v[72:75]\n"
        "s_waitcnt lgkmcnt(5)\n"
        "ds_read_b64_tr_b16 v[90:91], v33 offset:8448\n"
        "v_mul_f32 v144, v144, v18\n"
        "v_mfma_f32_16x16x32_bf16 v[76:79], a[0:3], v[104:107], 0\n"
        "ds_read_b64_tr_b16 v[92:93], v33 offset:10240\n"
        "buffer_load_dwordx4 v4, s[20:23], s46 offen sc1 lds\n"
        "v_mfma_f32_16x16x32_bf16 v[80:83], a[4:7], v[104:107], 0\n"
        "s_add_u32 m0, s81, 0x6800\n"
        "ds_read_b64_tr_b16 v[94:95], v33 offset:10496\n"
        "v_mul_f32 v145, v145, v18\n"
        "v_mfma_f32_16x16x32_bf16 v[84:87], v[104:107], a[8:11], 0\n"
        "s_add_u32 s46, s85, 0x440000\n"
        "ds_read_b64_tr_b16 v[96:97], v33 offset:12288\n"
        "v_mul_f32 v146, v146, v18\n"
        "v_mfma_f32_16x16x32_bf16 v[76:79], a[12:15], v[108:111], v[76:79]\n"
        "ds_read_b64_tr_b16 v[98:99], v33 offset:12544\n"
        "v_mul_f32 v147, v147, v18\n"
        "v_mfma_f32_16x16x32_bf16 v[80:83], a[16:19], v[108:111], v[80:83]\n"
        "ds_read_b64_tr_b16 v[100:101], v33 offset:14336\n"
        "buffer_load_dwordx4 v4, s[20:23], s46 offen sc1 lds\n"
        "v_mfma_f32_16x16x32_bf16 v[84:87], v[108:111], a[20:23], v[84:87]\n"
        "s_waitcnt lgkmcnt(5)\n"
        "ds_read_b64_tr_b16 v[102:103], v33 offset:14592\n"
        "v_mul_f32 v168, v131, v14\n"
        "v_mfma_f32_16x16x32_bf16 v[76:79], a[24:27], v[112:115], v[76:79]\n"
        "s_add_u32 m0, s81, 0x6c00\n"
        "L_pg7_%=:\n"
        "s_cbranch_execz L_pg11_%=\n"
        "ds_read_b64_tr_b16 v[104:105], v33 offset:16384\n"
        "v_mul_f32 v169, v131, v15\n"
        "v_mfma_f32_16x16x32_bf16 v[80:83], a[28:31], v[112:115], v[80:83]\n"
        "s_add_u32 s46, s86, 0x440000\n"
        "ds_read_b64_tr_b16 v[106:107], v33 offset:16640\n"
        "v_mul_f32 v170, v131, v16\n"
        "v_mfma_f32_16x16x32_bf16 v[84:87], v[112:115], a[32:35], v[84:87]\n"
        "ds_read_b64_tr_b16 v[108:109], v33 offset:18432\n"
        "buffer_load_dwordx4 v4, s[20:23], s46 offen sc1 lds\n"
        "v_mfma_f32_16x16x32_bf16 v[76:79], a[36:39], v[116:119], v[76:79]\n"
        "ds_read_b64_tr_b16 v[110:111], v33 offset:18688\n"
        "v_mul_f32 v171, v131, v17\n"
        "v_mfma_f32_16x16x32_bf16 v[80:83], a[40:43], v[116:119], v[80:83]\n"
        "ds_read_b64_tr_b16 v[112:113], v33 offset:20480\n"
        "v_mul_f32 v156, v121, v10\n"
        "v_mfma_f32_16x16x32_bf16 v[84:87], v[116:119], a[44:47], v[84:87]\n"
        "ds_read_b64_tr_b16 v[114:115], v33 offset:20736\n"
        "v_mul_f32 v157, v121, v11\n"
        "v_mfma_f32_16x16x32_bf16 v[76:79], a[48:51], v[88:91], v[76:79]\n"
        "ds_read_b64_tr_b16 v[116:117], v33 offset:22528\n"
        "v_mul_f32 v158, v121, v12\n"
        "v_mfma_f32_16x16x32_bf16 v[80:83], a[52:55], v[88:91], v[80:83]\n"
        "ds_read_b64_tr_b16 v[118:119], v33 offset:22784\n"
        "v_mul_f32 v159, v121, v13\n"
        "v_mfma_f32_16x16x32_bf16 v[84:87], v[88:91], a[56:59], v[84:87]\n"
        "s_waitcnt lgkmcnt(5)\n"
        "ds_read_b64_tr_b16 v[88:89], v33 offset:24576\n"
        "v_mul_f32 v148, v148, v18\n"
        "v_mfma_f32_16x16x32_bf16 v[76:79], a[60:63], v[92:95], v[76:79]\n"
        "ds_read_b64_tr_b16 v[90:91], v33 offset:24832\n"
        "v_mul_f32 v149, v149, v18\n"
        "v_mfma_f32_16x16x32_bf16 v[80:83], a[64:67], v[92:95], v[80:83]\n"
        "v_mul_f32 v150, v150, v18\n"
        "v_mul_f32 v151, v151, v18\n"
        "v_mfma_f32_16x16x32_bf16 v[84:87], v[92:95], a[68:71], v[84:87]\n"
        "ds_read_b64_tr_b16 v[92:93], v33 offset:26624\n"
        "v_mul_f32 v172, v125, v14\n"
        "v_mfma_f32_16x16x32_bf16 v[76:79], a[72:75], v[96:99], v[76:79]\n"
        "ds_read_b64_tr_b16 v[94:95], v33 offset:26880\n"
        "v_mul_f32 v173, v125, v15\n"
        "v_mfma_f32_16x16x32_bf16 v[80:83], a[76:79], v[96:99], v[80:83]\n"
        "v_mul_f32 v174, v125, v16\n"
        "v_mul_f32 v175, v125, v17\n"
        "v_mfma_f32_16x16x32_bf16 v[84:87], v[96:99], a[80:83], v[84:87]\n"
        "ds_read_b64_tr_b16 v[96:97], v33 offset:28672\n"
        "v_mul_f32 v160, v123, v10\n"
        "v_mfma_f32_16x16x32_bf16 v[76:79], a[84:87], v[100:103], v[76:79]\n"
        "ds_read_b64_tr_b16 v[98:99], v33 offset:28928\n"
        "v_mul_f32 v161, v123, v11\n"
        "v_mfma_f32_16x16x32_bf16 v[80:83], a[88:91], v[100:103], v[80:83]\n"
        "v_mul_f32 v162, v123, v12\n"
        "v_mul_f32 v163, v123, v13\n"
        "v_mfma_f32_16x16x32_bf16 v[84:87], v[100:103], a[92:95], v[84:87]\n"
        "ds_read_b64_tr_b16 v[100:101], v33 offset:30720\n"
        "v_mul_f32 v152, v152, v18\n"
        "v_mfma_f32_16x16x32_bf16 v[76:79], a[96:99], v[104:107], v[76:79]\n"
        "ds_read_b64_tr_b16 v[102:103], v33 offset:30976\n"
        "v_mul_f32 v153, v153, v18\n"
        "v_mfma_f32_16x16x32_bf16 v[80:83], a[100:103], v[104:107], v[80:83]\n"
        "v_mul_f32 v154, v154, v18\n"
        "v_mul_f32 v155, v155, v18\n"
        "v_mfma_f32_16x16x32_bf16 v[84:87], v[104:107], a[104:107], v[84:87]\n"
        "s_waitcnt lgkmcnt(3)\n"
        "v_fmac_f32 v168, v68, v130\n"
        "v_fmac_f32 v169, v69, v130\n"
        "v_mfma_f32_16x16x32_bf16 v[76:79], a[108:111], v[108:111], v[76:79]\n"
        "v_fmac_f32 v170, v70, v130\n"
        "v_fmac_f32 v171, v71, v130\n"
        "v_mfma_f32_16x16x32_bf16 v[80:83], a[112:115], v[108:111], v[80:83]\n"
        "v_cvt_pk_bf16_f32 v168, v168, v169\n"
        "v_cvt_pk_bf16_f32 v169, v170, v171\n"
        "v_mfma_f32_16x16x32_bf16 v[84:87], v[108:111], a[116:119], v[84:87]\n"
        "v_fmac_f32 v156, v64, v120\n"
        "v_fmac_f32 v157, v65, v120\n"
        "v_mfma_f32_16x16x32_bf16 v[76:79], a[120:123], v[112:115], v[76:79]\n"
        "v_fmac_f32 v158, v66, v120\n"
        "v_fmac_f32 v159, v67, v120\n"
        "v_mfma_f32_16x16x32_bf16 v[80:83], a[124:127], v[112:115], v[80:83]\n"
        "v_cvt_pk_bf16_f32 v156, v156, v157\n"
        "v_cvt_pk_bf16_f32 v157, v158, v159\n"
        "v_mfma_f32_16x16x32_bf16 v[84:87], v[112:115], a[128:131], v[84:87]\n"
        "v_fmac_f32 v164, v56, v128\n"
        "v_fmac_f32 v165, v57, v128\n"
        "v_mfma_f32_16x16x32_bf16 v[76:79], a[132:135], v[116:119], v[76:79]\n"
        "v_fmac_f32 v166, v58, v128\n"
        "v_fmac_f32 v167, v59, v128\n"
        "v_mfma_f32_16x16x32_bf16 v[80:83], a[136:139], v[116:119], v[80:83]\n"
        "v_cvt_pk_bf16_f32 v164, v164, v165\n"
        "v_cvt_pk_bf16_f32 v165, v166, v167\n"
        "v_mfma_f32_16x16x16_bf16 v[68:71], v[168:169], v[156:157], v[26:29]\n"
        "s_nop 0\n"
        "v_mfma_f32_16x16x16_bf16 v[56:59], v[164:165], v[156:157], v[22:25]\n"
        "v_fmac_f32 v148, v72, v136\n"
        "v_fmac_f32 v149, v73, v137\n"
        "v_mfma_f32_16x16x32_bf16 v[84:87], v[116:119], a[140:143], v[84:87]\n"
        "v_fmac_f32 v150, v74, v138\n"
        "v_fmac_f32 v151, v75, v139\n"
        "v_mfma_f32_16x16x32_bf16 v[76:79], a[144:147], v[88:91], v[76:79]\n"
        "v_cvt_pk_bf16_f32 v48, v148, v149\n"
        "v_cvt_pk_bf16_f32 v49, v150, v151\n"
        "v_mfma_f32_16x16x32_bf16 v[80:83], a[148:151], v[88:91], v[80:83]\n"
        "v_fmac_f32 v144, v60, v132\n"
        "v_fmac_f32 v145, v61, v133\n"
        "v_mfma_f32_16x16x32_bf16 v[84:87], v[88:91], a[152:155], v[84:87]\n"
        "v_fmac_f32 v146, v62, v134\n"
        "v_fmac_f32 v147, v63, v135\n"
        "v_mfma_f32_16x16x32_bf16 v[76:79], a[156:159], v[92:95], v[76:79]\n"
        "v_cvt_pk_bf16_f32 v46, v144, v145\n"
        "v_cvt_pk_bf16_f32 v47, v146, v147\n"
        "v_mfma_f32_16x16x32_bf16 v[80:83], a[160:163], v[92:95], v[80:83]\n"
        "s_add_u32 s90, s82, 0x540000\n"
        "v_max_f32 v56, v56, v68\n"
        "v_max_f32 v57, v57, v69\n"
        "v_mfma_f32_16x16x32_bf16 v[84:87], v[92:95], a[164:167], v[84:87]\n"
        "s_waitcnt lgkmcnt(0)\n"
        "v_max_f32 v58, v58, v70\n"
        "v_max_f32 v59, v59, v71\n"
        "v_mfma_f32_16x16x32_bf16 v[76:79], a[168:171], v[96:99], v[76:79]\n"
        "v_max3_f32 v166, v56, v57, v58\n"
        "v_max_f32 v167, v166, v59\n"
        "v_mfma_f32_16x16x32_bf16 v[80:83], a[172:175], v[96:99], v[80:83]\n"
        "v_max_f32 v166, v166, v59\n"
        "v_mfma_f32_16x16x32_bf16 v[84:87], v[96:99], a[176:179], v[84:87]\n"
        "s_nop 0\n"
        "v_permlane32_swap_b32 v166, v167\n"
        "v_mfma_f32_16x16x32_bf16 v[76:79], a[180:183], v[100:103], v[76:79]\n"
        "v_max_f32 v68, v166, v167\n"
        "v_max_f32 v166, v166, v167\n"
        "v_mfma_f32_16x16x32_bf16 v[80:83], a[184:187], v[100:103], v[80:83]\n"
        "s_nop 0\n"
        "v_permlane16_swap_b32 v166, v68\n"
        "v_mfma_f32_16x16x32_bf16 v[84:87], v[100:103], a[188:191], v[84:87]\n"
        "v_max_f32 v166, v166, v68\n"
        "v_sub_f32 v56, v56, v166\n"
        "v_sub_f32 v57, v57, v166\n"
        "v_sub_f32 v58, v58, v166\n"
        "v_sub_f32 v59, v59, v166\n"
        "v_pk_fma_f32 v[160:161], v[76:77], v[122:123], v[160:161] op_sel_hi:[1,0,1]\n"
        "v_pk_fma_f32 v[162:163], v[78:79], v[122:123], v[162:163] op_sel_hi:[1,0,1]\n"
        "v_pk_fma_f32 v[172:173], v[80:81], v[124:125], v[172:173] op_sel_hi:[1,0,1]\n"
        "v_pk_fma_f32 v[174:175], v[82:83], v[124:125], v[174:175] op_sel_hi:[1,0,1]\n"
        "v_pk_fma_f32 v[152:153], v[84:85], v[140:141], v[152:153]\n"
        "v_pk_fma_f32 v[154:155], v[86:87], v[142:143], v[154:155]\n"
        "v_cvt_pk_bf16_f32 v160, v160, v161\n"
        "v_cvt_pk_bf16_f32 v161, v162, v163\n"
        "v_cvt_pk_bf16_f32 v172, v172, v173\n"
        "v_cvt_pk_bf16_f32 v173, v174, v175\n"
        "v_exp_f32 v56, v56\n"
        "s_nop 0\n"
        "v_mfma_f32_16x16x16_bf16 v[80:83], v[172:173], v[160:161], v[26:29]\n"
        "v_exp_f32_e64 v57, v57\n"
        "v_mfma_f32_16x16x16_bf16 v[76:79], v[168:169], v[160:161], v[22:25]\n"
        "v_cvt_pk_bf16_f32 v50, v152, v153\n"
        "v_cvt_pk_bf16_f32 v51, v154, v155\n"
        "s_add_u32 s87, s82, 0x544000\n"
        "v_exp_f32 v58, v58\n"
        "v_exp_f32 v59, v59\n"
        "v_cvt_pk_bf16_f32 v146, v56, v57\n"
        "v_cvt_pk_bf16_f32 v147, v58, v59\n"
        "v_and_b32 v144, v146, v30\n"
        "v_max_f32 v76, v76, v80\n"
        "v_max_f32 v77, v77, v81\n"
        "v_max_f32 v78, v78, v82\n"
        "v_max_f32 v79, v79, v83\n"
        "v_max3_f32 v174, v76, v77, v78\n"
        "v_max_f32 v175, v174, v79\n"
        "v_max_f32 v174, v174, v79\n"
        "v_and_b32 v145, v147, v31\n"
        "v_xor_b32 v146, v146, v144\n"
        "v_permlane32_swap_b32 v174, v175\n"
        "v_max_f32 v80, v174, v175\n"
        "v_max_f32 v174, v174, v175\n"
        "v_xor_b32 v147, v147, v145\n"
        "s_nop 0\n"
        "v_permlane16_swap_b32 v174, v80\n"
        "v_mfma_f32_16x16x32_bf16 v[60:63], v[6:9], v[144:147], 0\n"
        "v_max_f32 v174, v174, v80\n"
        "v_sub_f32 v76, v76, v174\n"
        "v_mfma_f32_16x16x32_bf16 v[64:67], v[46:49], v[144:147], 0\n"
        "v_sub_f32 v77, v77, v174\n"
        "v_sub_f32 v78, v78, v174\n"
        "v_sub_f32 v79, v79, v174\n"
        "v_exp_f32 v76, v76\n"
        "v_exp_f32 v77, v77\n"
        "v_exp_f32 v78, v78\n"
        "v_exp_f32 v79, v79\n"
        "v_cvt_pk_bf16_f32 v154, v76, v77\n"
        "v_cvt_pk_bf16_f32 v155, v78, v79\n"
        "v_and_b32 v152, v154, v30\n"
        "v_and_b32 v153, v155, v31\n"
        "v_xor_b32 v154, v154, v152\n"
        "v_xor_b32 v155, v155, v153\n"
        "v_rcp_f32 v60, v60\n"
        "s_nop 0\n"
        "v_mfma_f32_16x16x32_bf16 v[160:163], v[6:9], v[152:155], 0\n"
        "v_mul_f32 v64, v64, v60\n"
        "v_mul_f32 v65, v65, v60\n"
        "v_mfma_f32_16x16x32_bf16 v[84:87], v[48:51], v[152:155], 0\n"
        "v_mul_f32 v66, v66, v60\n"
        "v_mul_f32 v67, v67, v60\n"
        "v_cvt_pk_bf16_f32 v64, v64, v65\n"
        "v_cvt_pk_bf16_f32 v65, v66, v67\n"
        "s_nop 0\n"
        "v_rcp_f32_e64 v160, v160\n"
        "s_nop 1\n"
        "v_pk_mul_f32 v[84:85], v[84:85], v[160:161] op_sel_hi:[1,0]\n"
        "v_pk_mul_f32 v[86:87], v[86:87], v[160:161] op_sel_hi:[1,0]\n"
        "v_cvt_pk_bf16_f32 v84, v84, v85\n"
        "v_cvt_pk_bf16_f32 v85, v86, v87\n"
        "buffer_store_dwordx2 v[84:85], v34, s[20:23], s87 offen sc1\n"
        "buffer_store_dwordx2 v[64:65], v34, s[20:23], s90 offen sc1\n"
        "s_mov_b32 s43, 0xffff0000\n"
        "v_lshlrev_b32 v104, 16, v52\n"
        "v_and_b32 v105, s43, v52\n"
        "v_lshlrev_b32 v106, 16, v53\n"
        "v_and_b32 v107, s43, v53\n"
        "v_lshlrev_b32 v108, 16, v54\n"
        "v_and_b32 v109, s43, v54\n"
        "v_lshlrev_b32 v110, 16, v55\n"
        "v_and_b32 v111, s43, v55\n"
        "v_lshlrev_b32_e64 v186, 6, v0\n"
        "s_mov_b32 s91, 0x5000\n"
        "buffer_load_dwordx2 v[184:185], v186, s[20:23], s91 offen sc1\n"
        "s_waitcnt vmcnt(3)\n"
        "s_barrier\n"
        "ds_read_b128 v[60:63], v4 offset:24576\n"
        "s_add_u32 s92, s34, 0x640000\n"
        "s_add_u32 s93, s34, 0x641000\n"
        "s_add_u32 s94, s34, 0x648000\n"
        "ds_read_b128 v[64:67], v4 offset:25600\n"
        "s_add_u32 s95, s34, 0x649000\n"
        "s_lshl_b32 s48, s51, 8\n"
        "s_add_u32 s48, s48, 0x27000\n"
        "ds_read_b128 v[68:71], v4 offset:26624\n"
        "v_lshl_add_u32 v112, v1, 2, s48\n"
        "s_mov_b32 s48, 0x27000\n"
        "v_lshl_add_u32 v59, v1, 2, s48\n"
        "s_add_u32 s40, s35, 0x20000\n"
        "ds_read_b128 v[72:75], v4 offset:27648\n"
        "ds_read_b128 v[88:91], v4 offset:28672\n"
        "s_waitcnt lgkmcnt(4)\n"
        "v_mfma_f32_16x16x32_bf16 v[104:107], a[192:195], v[60:63], v[104:107]\n"
        "ds_read_b128 v[92:95], v4 offset:29696\n"
        "v_mfma_f32_16x16x32_bf16 v[108:111], a[196:199], v[60:63], v[108:111]\n"
        "s_waitcnt lgkmcnt(4)\n"
        "ds_read_b128 v[96:99], v4 offset:30720\n"
        "v_mfma_f32_16x16x32_bf16 v[104:107], a[200:203], v[64:67], v[104:107]\n"
        "ds_read_b128 v[100:103], v4 offset:31744\n"
        "v_mfma_f32_16x16x32_bf16 v[108:111], a[204:207], v[64:67], v[108:111]\n"
        "s_waitcnt lgkmcnt(4)\n"
        "ds_read_b128 v[60:63], v4 offset:32768\n"
        "s_lshr_b32 s45, s50, 5\n"
        "v_mfma_f32_16x16x32_bf16 v[104:107], a[208:211], v[68:71], v[104:107]\n"
        "s_lshl2_add_u32 s45, s45, s51\n"
        "ds_read_b128 v[64:67], v4 offset:33792\n"
        "s_and_b32 s44, s50, 31\n"
        "v_mfma_f32_16x16x32_bf16 v[108:111], a[212:215], v[68:71], v[108:111]\n"
        "s_mul_i32 s9, s44, 0xa000\n"
        "ds_read_b128 v[68:71], v4 offset:34816\n"
        "s_lshl_b32 s44, s45, 10\n"
        "v_mfma_f32_16x16x32_bf16 v[104:107], a[216:219], v[72:75], v[104:107]\n"
        "s_add_u32 s9, s9, s44\n"
        "s_lshr_b32 s44, s79, 1\n"
        "s_add_u32 s44, s44, 0xa500\n"
        "v_mfma_f32_16x16x32_bf16 v[108:111], a[220:223], v[72:75], v[108:111]\n"
        "s_waitcnt lgkmcnt(4)\n"
        "ds_read_b128 v[72:75], v4 offset:35840\n"
        "v_add_u32 v32, s44, v4\n"
        "v_mfma_f32_16x16x32_bf16 v[104:107], a[224:227], v[88:91], v[104:107]\n"
        "v_and_b32_e64 v45, 3, v1\n"
        "v_lshl_add_u32 v33, v45, 3, s44\n"
        "v_mfma_f32_16x16x32_bf16 v[108:111], a[228:231], v[88:91], v[108:111]\n"
        "ds_read_b128 v[88:91], v4 offset:36864\n"
        "v_bfe_u32 v45, v1, 2, 2\n"
        "v_mfma_f32_16x16x32_bf16 v[104:107], a[232:235], v[92:95], v[104:107]\n"
        "v_lshl_add_u32 v33, v45, 6, v33\n"
        "v_and_b32 v45, 1, v3\n"
        "v_mfma_f32_16x16x32_bf16 v[108:111], a[236:239], v[92:95], v[108:111]\n"
        "ds_read_b128 v[92:95], v4 offset:37888\n"
        "v_lshl_add_u32 v33, v45, 9, v33\n"
        "v_mfma_f32_16x16x32_bf16 v[104:107], a[240:243], v[96:99], v[104:107]\n"
        "v_lshrrev_b32 v56, 1, v3\n"
        "v_lshlrev_b32 v5, 4, v45\n"
        "v_mfma_f32_16x16x32_bf16 v[108:111], a[244:247], v[96:99], v[108:111]\n"
        "s_waitcnt lgkmcnt(2)\n"
        "ds_read_b128 v[96:99], v4 offset:38912\n"
        "v_xor_b32_e64 v45, v45, v56\n"
        "v_mfma_f32_16x16x32_bf16 v[104:107], a[248:251], v[100:103], v[104:107]\n"
        "v_lshl_add_u32 v33, v45, 5, v33\n"
        "v_lshl_add_u32 v5, v56, 15, v5\n"
        "v_mfma_f32_16x16x32_bf16 v[108:111], a[252:255], v[100:103], v[108:111]\n"
        "ds_read_b128 v[100:103], v4 offset:39936\n"
        "v_lshl_add_u32 v5, v2, 11, v5\n"
        "v_mfma_f32_16x16x32_bf16 v[104:107], v[192:195], v[60:63], v[104:107]\n"
        "s_lshl_b32 s45, s45, 5\n"
        "s_and_b32 s44, s50, 30\n"
        "s_lshl_b32 s44, s44, 15\n"
        "v_mfma_f32_16x16x32_bf16 v[108:111], v[196:199], v[60:63], v[108:111]\n"
        "s_add_u32 s45, s45, s44\n"
        "s_and_b32 s44, s50, 1\n"
        "s_lshl_b32 s44, s44, 10\n"
        "v_mfma_f32_16x16x32_bf16 v[104:107], v[200:203], v[64:67], v[104:107]\n"
        "s_add_u32 s44, s44, s45\n"
        "v_add_u32 v5, s44, v5\n"
        "v_mfma_f32_16x16x32_bf16 v[108:111], v[204:207], v[64:67], v[108:111]\n"
        "v_mfma_f32_16x16x32_bf16 v[104:107], v[208:211], v[68:71], v[104:107]\n"
        "v_mfma_f32_16x16x32_bf16 v[108:111], v[212:215], v[68:71], v[108:111]\n"
        "v_mfma_f32_16x16x32_bf16 v[104:107], v[216:219], v[72:75], v[104:107]\n"
        "v_mfma_f32_16x16x32_bf16 v[108:111], v[220:223], v[72:75], v[108:111]\n"
        "s_waitcnt lgkmcnt(0)\n"
        "v_mfma_f32_16x16x32_bf16 v[104:107], v[224:227], v[88:91], v[104:107]\n"
        "v_mfma_f32_16x16x32_bf16 v[108:111], v[228:231], v[88:91], v[108:111]\n"
        "v_mfma_f32_16x16x32_bf16 v[104:107], v[232:235], v[92:95], v[104:107]\n"
        "v_mfma_f32_16x16x32_bf16 v[108:111], v[236:239], v[92:95], v[108:111]\n"
        "v_mfma_f32_16x16x32_bf16 v[104:107], v[240:243], v[96:99], v[104:107]\n"
        "v_mfma_f32_16x16x32_bf16 v[108:111], v[244:247], v[96:99], v[108:111]\n"
        "v_mfma_f32_16x16x32_bf16 v[104:107], v[248:251], v[100:103], v[104:107]\n"
        "v_mfma_f32_16x16x32_bf16 v[108:111], v[252:255], v[100:103], v[108:111]\n"
        "L_re_o10_%=:\n"
        "v_mov_b32 v1, v1\n"
        "s_nop 3\n"
        "s_nop 1\n"
        "v_pk_add_f32 v[64:65], v[104:105], v[106:107]\n"
        "v_pk_mul_f32 v[66:67], v[104:105], v[104:105]\n"
        "v_pk_add_f32 v[64:65], v[64:65], v[108:109]\n"
        "v_pk_fma_f32 v[66:67], v[106:107], v[106:107], v[66:67]\n"
        "v_pk_add_f32 v[64:65], v[64:65], v[110:111]\n"
        "v_pk_fma_f32 v[66:67], v[108:109], v[108:109], v[66:67]\n"
        "v_cmp_u_f32 vcc, v64, v65\n"
        "v_add_f32 v64, v64, v65\n"
        "v_pk_fma_f32 v[66:67], v[110:111], v[110:111], v[66:67]\n"
        "v_add_f32 v65, v66, v67\n"
        "v_cvt_pk_bf16_f32 v60, v104, v105\n"
        "v_cvt_pk_bf16_f32 v61, v106, v107\n"
        "v_permlane32_swap_b32 v64, v65\n"
        "v_add_f32 v64, v64, v65\n"
        "v_cvt_pk_bf16_f32 v62, v108, v109\n"
        "v_cvt_pk_bf16_f32 v63, v110, v111\n"
        "s_cbranch_vccnz L_slow_o10_%=\n"
        "L_ret_o10_%=:\n"
        "ds_write_b32 v112, v64\n"
        "buffer_store_dword v60, v36, s[20:23], s92 offen sc1\n"
        "buffer_store_dword v61, v36, s[20:23], s93 offen sc1\n"
        "buffer_store_dword v62, v36, s[20:23], s94 offen sc1\n"
        "buffer_store_dword v63, v36, s[20:23], s95 offen sc1\n"
        "s_waitcnt lgkmcnt(0)\n"
        "s_barrier\n"
        "ds_read2st64_b32 v[68:69], v59 offset1:1\n"
        "ds_read2st64_b32 v[70:71], v59 offset0:2 offset1:3\n"
        "s_waitcnt lgkmcnt(0)\n"
        "v_pk_add_f32 v[68:69], v[68:69], v[70:71]\n"
        "v_add_f32 v70, v68, v69\n"
        "v_add_f32 v68, v68, v69\n"
        "s_nop 1\n"
        "v_permlane16_swap_b32 v68, v70\n"
        "v_add_f32_e64 v68, v68, v70\n"
        "buffer_store_dword v68, v37, s[20:23], s40 offen sc1\n"
        "s_add_u32 m0, s81, 0\n"
        "s_add_u32 s46, s83, 0x540000\n"
        "buffer_load_dwordx4 v4, s[20:23], s46 offen sc1 lds\n"
        "s_add_u32 m0, s81, 0x400\n"
        "s_add_u32 s46, s84, 0x540000\n"
        "buffer_load_dwordx4 v4, s[20:23], s46 offen sc1 lds\n"
        "s_add_u32 m0, s81, 0x800\n"
        "s_add_u32 s46, s85, 0x540000\n"
        "buffer_load_dwordx4 v4, s[20:23], s46 offen sc1 lds\n"
        "s_add_u32 m0, s81, 0xc00\n"
        "s_add_u32 s46, s86, 0x540000\n"
        "buffer_load_dwordx4 v4, s[20:23], s46 offen sc1 lds\n"
        "s_mov_b32 s43, 0xffff0000\n"
        "v_lshlrev_b32 v120, 16, v188\n"
        "v_and_b32 v121, s43, v188\n"
        "v_lshlrev_b32 v122, 16, v189\n"
        "v_and_b32 v123, s43, v189\n"
        "v_lshlrev_b32 v124, 16, v190\n"
        "v_and_b32 v125, s43, v190\n"
        "v_lshlrev_b32 v126, 16, v191\n"
        "v_and_b32 v127, s43, v191\n"
        "ds_read_b64_tr_b16 a[0:1], v38 offset:8\n"
        "ds_read_b64_tr_b16 a[2:3], v38\n"
        "ds_read_b64_tr_b16 a[4:5], v38 offset:72\n"
        "ds_read_b64_tr_b16 a[6:7], v38 offset:64\n"
        "ds_read_b64_tr_b16 a[8:9], v38 offset:136\n"
        "ds_read_b64_tr_b16 a[10:11], v38 offset:128\n"
        "ds_read_b64_tr_b16 a[12:13], v38 offset:200\n"
        "ds_read_b64_tr_b16 a[14:15], v38 offset:192\n"
        "ds_read_b64_tr_b16 a[16:17], v38 offset:264\n"
        "ds_read_b64_tr_b16 a[18:19], v38 offset:256\n"
        "ds_read_b64_tr_b16 a[20:21], v38 offset:328\n"
        "ds_read_b64_tr_b16 a[22:23], v38 offset:320\n"
        "ds_read_b64_tr_b16 a[24:25], v38 offset:392\n"
        "ds_read_b64_tr_b16 a[26:27], v38 offset:384\n"
        "ds_read_b64_tr_b16 a[28:29], v38 offset:456\n"
        "ds_read_b64_tr_b16 a[30:31], v38 offset:448\n"
        "ds_read_b64_tr_b16 a[32:33], v38 offset:520\n"
        "ds_read_b64_tr_b16 a[34:35], v38 offset:512\n"
        "ds_read_b64_tr_b16 a[36:37], v38 offset:584\n"
        "ds_read_b64_tr_b16 a[38:39], v38 offset:576\n"
        "ds_read_b64_tr_b16 a[40:41], v38 offset:648\n"
        "ds_read_b64_tr_b16 a[42:43], v38 offset:640\n"
        "ds_read_b64_tr_b16 a[44:45], v38 offset:712\n"
        "ds_read_b64_tr_b16 a[46:47], v38 offset:704\n"
        "ds_read_b64_tr_b16 a[48:49], v38 offset:776\n"
        "ds_read_b64_tr_b16 a[50:51], v38 offset:768\n"
        "ds_read_b64_tr_b16 a[52:53], v38 offset:840\n"
        "ds_read_b64_tr_b16 a[54:55], v38 offset:832\n"
        "ds_read_b64_tr_b16 a[56:57], v38 offset:904\n"
        "ds_read_b64_tr_b16 a[58:59], v38 offset:896\n"
        "ds_read_b64_tr_b16 a[60:61], v38 offset:968\n"
        "ds_read_b64_tr_b16 a[62:63], v38 offset:960\n"
        "ds_read_b64_tr_b16 a[64:65], v38 offset:1032\n"
        "ds_read_b64_tr_b16 a[66:67], v38 offset:1024\n"
        "ds_read_b64_tr_b16 a[68:69], v38 offset:1096\n"
        "ds_read_b64_tr_b16 a[70:71], v38 offset:1088\n"
        "ds_read_b64_tr_b16 a[72:73], v38 offset:1160\n"
        "ds_read_b64_tr_b16 a[74:75], v38 offset:1152\n"
        "ds_read_b64_tr_b16 a[76:77], v38 offset:1224\n"
        "ds_read_b64_tr_b16 a[78:79], v38 offset:1216\n"
        "ds_read_b64_tr_b16 a[80:81], v38 offset:1288\n"
        "ds_read_b64_tr_b16 a[82:83], v38 offset:1280\n"
        "ds_read_b64_tr_b16 a[84:85], v38 offset:1352\n"
        "ds_read_b64_tr_b16 a[86:87], v38 offset:1344\n"
        "ds_read_b64_tr_b16 a[88:89], v38 offset:1416\n"
        "ds_read_b64_tr_b16 a[90:91], v38 offset:1408\n"
        "ds_read_b64_tr_b16 a[92:93], v38 offset:1480\n"
        "ds_read_b64_tr_b16 a[94:95], v38 offset:1472\n"
        "ds_read_b64_tr_b16 a[96:97], v38 offset:1544\n"
        "ds_read_b64_tr_b16 a[98:99], v38 offset:1536\n"
        "ds_read_b64_tr_b16 a[100:101], v38 offset:1608\n"
        "ds_read_b64_tr_b16 a[102:103], v38 offset:1600\n"
        "ds_read_b64_tr_b16 a[104:105], v38 offset:1672\n"
        "ds_read_b64_tr_b16 a[106:107], v38 offset:1664\n"
        "ds_read_b64_tr_b16 a[108:109], v38 offset:1736\n"
        "ds_read_b64_tr_b16 a[110:111], v38 offset:1728\n"
        "ds_read_b64_tr_b16 a[112:113], v38 offset:1800\n"
        "ds_read_b64_tr_b16 a[114:115], v38 offset:1792\n"
        "ds_read_b64_tr_b16 a[116:117], v38 offset:1864\n"
        "ds_read_b64_tr_b16 a[118:119], v38 offset:1856\n"
        "ds_read_b64_tr_b16 a[120:121], v38 offset:1928\n"
        "ds_read_b64_tr_b16 a[122:123], v38 offset:1920\n"
        "ds_read_b64_tr_b16 a[124:125], v38 offset:1992\n"
        "ds_read_b64_tr_b16 a[126:127], v38 offset:1984\n"
        "s_waitcnt vmcnt(9)\n"
        "v_cmp_ne_u64 vcc, v[184:185], s[62:63]\n"
        "s_cbranch_vccnz L_slow_flag1_%=\n"
        "L_ret_flag1_%=:\n"
        "v_mov_b32 v1, v1\n"
        "s_nop 3\n"
        "v_mov_b32 v1, v1\n"
        "s_nop 3\n"
        "s_waitcnt lgkmcnt(0)\n"
        "s_waitcnt vmcnt(0)\n"
        "s_lshl_b32 s43, s51, 12\n"
        "s_add_u32 s43, s43, 0\n"
        "v_add_u32_e64 v101, s43, v4\n"
        "ds_read_b128 v[104:107], v101\n"
        "ds_read_b128 v[108:111], v101 offset:1024\n"
        "ds_read_b128 v[112:115], v101 offset:2048\n"
        "ds_read_b128 v[116:119], v101 offset:3072\n"
        "s_waitcnt lgkmcnt(3)\n"
        "v_max3_u32 v100, v104, v105, v106\n"
        "s_waitcnt lgkmcnt(2)\n"
        "v_max3_u32 v100, v100, v107, v108\n"
        "v_max3_u32 v100, v100, v109, v110\n"
        "s_waitcnt lgkmcnt(1)\n"
        "v_max3_u32 v100, v100, v111, v112\n"
        "v_max3_u32 v100, v100, v113, v114\n"
        "s_waitcnt lgkmcnt(0)\n"
        "v_max3_u32 v100, v100, v115, v116\n"
        "v_max3_u32 v100, v100, v117, v118\n"
        "v_max_u32 v100, v100, v119\n"
        "v_cmp_eq_u32 vcc, -1, v100\n"
        "s_cbranch_vccnz L_slow_o11_%=\n"
        "L_ret_o11_%=:\n"
        "s_barrier\n"
        "ds_read_b128 v[52:55], v4\n"
        "s_add_u32 s92, s34, 0x740000\n"
        "s_add_u32 s93, s34, 0x741000\n"
        "s_add_u32 s94, s34, 0x748000\n"
        "ds_read_b128 v[92:95], v4 offset:1024\n"
        "s_add_u32 s95, s34, 0x749000\n"
        "s_lshl_b32 s48, s51, 8\n"
        "s_add_u32 s48, s48, 0x27400\n"
        "ds_read_b128 v[96:99], v4 offset:2048\n"
        "v_lshl_add_u32 v59, v1, 2, s48\n"
        "s_mov_b32 s48, 0x27400\n"
        "v_lshl_add_u32 v51, v1, 2, s48\n"
        "s_add_u32 s40, s35, 0x28000\n"
        "ds_read_b128 v[100:103], v4 offset:3072\n"
        "ds_read_b128 v[104:107], v4 offset:4096\n"
        "s_waitcnt lgkmcnt(4)\n"
        "v_mfma_f32_16x16x32_bf16 v[120:123], a[192:195], v[52:55], v[120:123]\n"
        "ds_read_b128 v[108:111], v4 offset:5120\n"
        "v_mfma_f32_16x16x32_bf16 v[124:127], a[196:199], v[52:55], v[124:127]\n"
        "s_waitcnt lgkmcnt(4)\n"
        "ds_read_b128 v[112:115], v4 offset:6144\n"
        "v_mfma_f32_16x16x32_bf16 v[120:123], a[200:203], v[92:95], v[120:123]\n"
        "ds_read_b128 v[116:119], v4 offset:7168\n"
        "v_mfma_f32_16x16x32_bf16 v[124:127], a[204:207], v[92:95], v[124:127]\n"
        "s_waitcnt lgkmcnt(4)\n"
        "ds_read_b128 v[52:55], v4 offset:8192\n"
        "v_mfma_f32_16x16x32_bf16 v[120:123], a[208:211], v[96:99], v[120:123]\n"
        "ds_read_b128 v[92:95], v4 offset:9216\n"
        "v_mfma_f32_16x16x32_bf16 v[124:127], a[212:215], v[96:99], v[124:127]\n"
        "ds_read_b128 v[96:99], v4 offset:10240\n"
        "v_mfma_f32_16x16x32_bf16 v[120:123], a[216:219], v[100:103], v[120:123]\n"
        "v_mfma_f32_16x16x32_bf16 v[124:127], a[220:223], v[100:103], v[124:127]\n"
        "s_waitcnt lgkmcnt(4)\n"
        "ds_read_b128 v[100:103], v4 offset:11264\n"
        "v_mfma_f32_16x16x32_bf16 v[120:123], a[224:227], v[104:107], v[120:123]\n"
        "v_mfma_f32_16x16x32_bf16 v[124:127], a[228:231], v[104:107], v[124:127]\n"
        "s_mov_b32 s46, 0x20000\n"
        "ds_read_b128 v[104:107], v4 offset:12288\n"
        "buffer_load_dwordx4 v[60:63], v19, s[20:23], s46 offen sc1\n"
        "v_mfma_f32_16x16x32_bf16 v[120:123], a[232:235], v[108:111], v[120:123]\n"
        "s_mov_b32 s46, 0x21000\n"
        "v_mfma_f32_16x16x32_bf16 v[124:127], a[236:239], v[108:111], v[124:127]\n"
        "ds_read_b128 v[108:111], v4 offset:13312\n"
        "v_mfma_f32_16x16x32_bf16 v[120:123], a[240:243], v[112:115], v[120:123]\n"
        "v_mfma_f32_16x16x32_bf16 v[124:127], a[244:247], v[112:115], v[124:127]\n"
        "s_waitcnt lgkmcnt(2)\n"
        "ds_read_b128 v[112:115], v4 offset:14336\n"
        "buffer_load_dwordx4 v[64:67], v19, s[20:23], s46 offen sc1\n"
        "v_mfma_f32_16x16x32_bf16 v[120:123], a[248:251], v[116:119], v[120:123]\n"
        "s_mov_b32 s46, 0x22000\n"
        "v_mfma_f32_16x16x32_bf16 v[124:127], a[252:255], v[116:119], v[124:127]\n"
        "ds_read_b128 v[116:119], v4 offset:15360\n"
        "v_mfma_f32_16x16x32_bf16 v[120:123], v[192:195], v[52:55], v[120:123]\n"
        "v_mfma_f32_16x16x32_bf16 v[124:127], v[196:199], v[52:55], v[124:127]\n"
        "buffer_load_dwordx4 v[68:71], v19, s[20:23], s46 offen sc1\n"
        "s_mov_b32 s46, 0x23000\n"
        "v_mfma_f32_16x16x32_bf16 v[120:123], v[200:203], v[92:95], v[120:123]\n"
        "v_mfma_f32_16x16x32_bf16 v[124:127], v[204:207], v[92:95], v[124:127]\n"
        "v_mfma_f32_16x16x32_bf16 v[120:123], v[208:211], v[96:99], v[120:123]\n"
        "v_mfma_f32_16x16x32_bf16 v[124:127], v[212:215], v[96:99], v[124:127]\n"
        "buffer_load_dwordx4 v[72:75], v19, s[20:23], s46 offen sc1\n"
        "s_mov_b32 s46, 0x24000\n"
        "v_mfma_f32_16x16x32_bf16 v[120:123], v[216:219], v[100:103], v[120:123]\n"
        "v_mfma_f32_16x16x32_bf16 v[124:127], v[220:223], v[100:103], v[124:127]\n"
        "s_waitcnt lgkmcnt(0)\n"
        "v_mfma_f32_16x16x32_bf16 v[120:123], v[224:227], v[104:107], v[120:123]\n"
        "v_mfma_f32_16x16x32_bf16 v[124:127], v[228:231], v[104:107], v[124:127]\n"
        "buffer_load_dwordx4 v[76:79], v19, s[20:23], s46 offen sc1\n"
        "s_mov_b32 s46, 0x25000\n"
        "v_mfma_f32_16x16x32_bf16 v[120:123], v[232:235], v[108:111], v[120:123]\n"
        "v_mfma_f32_16x16x32_bf16 v[124:127], v[236:239], v[108:111], v[124:127]\n"
        "v_mfma_f32_16x16x32_bf16 v[120:123], v[240:243], v[112:115], v[120:123]\n"
        "v_mfma_f32_16x16x32_bf16 v[124:127], v[244:247], v[112:115], v[124:127]\n"
        "buffer_load_dwordx4 v[80:83], v19, s[20:23], s46 offen sc1\n"
        "s_mov_b32 s46, 0x26000\n"
        "v_mfma_f32_16x16x32_bf16 v[120:123], v[248:251], v[116:119], v[120:123]\n"
        "v_mfma_f32_16x16x32_bf16 v[124:127], v[252:255], v[116:119], v[124:127]\n"
        "buffer_load_dwordx4 v[84:87], v19, s[20:23], s46 offen sc1\n"
        "s_mov_b32 s46, 0x27000\n"
        "buffer_load_dwordx4 v[88:91], v19, s[20:23], s46 offen sc1\n"
        "s_lshl_b32 s46, s50, 12\n"
        "s_add_u32 s46, s46, 0x640000\n"
        "buffer_load_dwordx4 v[46:49], v19, s[20:23], s46 offen sc1\n"
        "s_nop 0\n"
        "v_pk_add_f32 v[92:93], v[120:121], v[122:123]\n"
        "v_pk_mul_f32 v[94:95], v[120:121], v[120:121]\n"
        "v_pk_add_f32 v[92:93], v[92:93], v[124:125]\n"
        "v_pk_fma_f32 v[94:95], v[122:123], v[122:123], v[94:95]\n"
        "v_pk_add_f32 v[92:93], v[92:93], v[126:127]\n"
        "v_pk_fma_f32 v[94:95], v[124:125], v[124:125], v[94:95]\n"
        "v_add_f32 v92, v92, v93\n"
        "v_pk_fma_f32 v[94:95], v[126:127], v[126:127], v[94:95]\n"
        "v_add_f32 v93, v94, v95\n"
        "v_cvt_pk_bf16_f32 v52, v120, v121\n"
        "v_cvt_pk_bf16_f32 v53, v122, v123\n"
        "v_permlane32_swap_b32 v92, v93\n"
        "v_add_f32_e64 v92, v92, v93\n"
        "v_cvt_pk_bf16_f32 v54, v124, v125\n"
        "v_cvt_pk_bf16_f32 v55, v126, v127\n"
        "ds_write_b32 v59, v92\n"
        "buffer_store_dword v52, v36, s[20:23], s92 offen sc1\n"
        "buffer_store_dword v53, v36, s[20:23], s93 offen sc1\n"
        "buffer_store_dword v54, v36, s[20:23], s94 offen sc1\n"
        "buffer_store_dword v55, v36, s[20:23], s95 offen sc1\n"
        "s_waitcnt lgkmcnt(0)\n"
        "s_barrier\n"
        "ds_read2st64_b32 v[96:97], v51 offset1:1\n"
        "ds_read2st64_b32 v[98:99], v51 offset0:2 offset1:3\n"
        "s_waitcnt lgkmcnt(0)\n"
        "v_pk_add_f32 v[96:97], v[96:97], v[98:99]\n"
        "v_add_f32 v98, v96, v97\n"
        "v_add_f32 v96, v96, v97\n"
        "s_nop 1\n"
        "v_permlane16_swap_b32 v96, v98\n"
        "v_add_f32 v96, v96, v98\n"
        "buffer_store_dword v96, v37, s[20:23], s40 offen sc1\n"
        "L_re_p310_%=:\n"
        "s_waitcnt vmcnt(11)\n"
        "v_pk_add_f32 v[60:61], v[60:61], v[68:69]\n"
        "v_pk_add_f32 v[62:63], v[62:63], v[70:71]\n"
        "s_waitcnt vmcnt(10)\n"
        "v_pk_add_f32 v[64:65], v[64:65], v[72:73]\n"
        "v_pk_add_f32 v[66:67], v[66:67], v[74:75]\n"
        "s_waitcnt vmcnt(7)\n"
        "v_pk_add_f32 v[76:77], v[76:77], v[84:85]\n"
        "v_pk_add_f32 v[78:79], v[78:79], v[86:87]\n"
        "s_waitcnt vmcnt(6)\n"
        "v_pk_add_f32 v[80:81], v[80:81], v[88:89]\n"
        "v_pk_add_f32 v[82:83], v[82:83], v[90:91]\n"
        "v_pk_add_f32 v[60:61], v[60:61], v[76:77]\n"
        "v_pk_add_f32 v[62:63], v[62:63], v[78:79]\n"
        "v_pk_add_f32 v[64:65], v[64:65], v[80:81]\n"
        "v_pk_add_f32 v[66:67], v[66:67], v[82:83]\n"
        "v_pk_mul_f32 v[60:61], v[60:61], s[60:61] op_sel_hi:[1,0]\n"
        "v_pk_fma_f32 v[64:65], v[64:65], s[60:61], s[60:61] op_sel:[0,0,1] op_sel_hi:[1,0,1]\n"
        "v_pk_mul_f32 v[62:63], v[62:63], s[60:61] op_sel_hi:[1,0]\n"
        "v_pk_fma_f32 v[66:67], v[66:67], s[60:61], s[60:61] op_sel:[0,0,1] op_sel_hi:[1,0,1]\n"
        "v_pk_fma_f32 v[64:65], v[60:61], v[60:61], v[64:65] neg_lo:[1,0,0] neg_hi:[1,0,0]\n"
        "v_pk_fma_f32 v[66:67], v[62:63], v[62:63], v[66:67] neg_lo:[1,0,0] neg_hi:[1,0,0]\n"
        "v_maximum3_f32 v64, s61, v64, v64\n"
        "v_maximum3_f32 v65, s61, v65, v65\n"
        "v_maximum3_f32 v66, s61, v66, v66\n"
        "v_maximum3_f32 v67, s61, v67, v67\n"
        "v_rsq_f32 v64, v64\n"
        "v_rsq_f32 v65, v65\n"
        "L_pg8_%=:\n"
        "s_cbranch_execz L_pg12_%=\n"
        "v_rsq_f32 v66, v66\n"
        "v_rsq_f32 v67, v67\n"
        "s_mov_b32 s40, 0xffff0000\n"
        "s_waitcnt vmcnt(5)\n"
        "v_lshlrev_b32 v52, 16, v46\n"
        "v_and_b32 v56, s40, v46\n"
        "v_lshlrev_b32 v53, 16, v47\n"
        "v_and_b32 v57, s40, v47\n"
        "v_lshlrev_b32 v54, 16, v48\n"
        "v_and_b32 v58, s40, v48\n"
        "v_lshlrev_b32 v55, 16, v49\n"
        "v_and_b32 v59, s40, v49\n"
        "v_pk_mul_f32 v[60:61], v[60:61], v[64:65] neg_lo:[1,0] neg_hi:[1,0]\n"
        "v_pk_mul_f32 v[62:63], v[62:63], v[66:67] neg_lo:[1,0] neg_hi:[1,0]\n"
        "v_pk_fma_f32 v[76:77], v[52:53], v[64:65], v[60:61]\n"
        "v_pk_fma_f32 v[78:79], v[54:55], v[66:67], v[62:63]\n"
        "v_pk_add_f32 v[68:69], v[76:77], v[78:79]\n"
        "v_cmp_u_f32_e64 vcc, v68, v69\n"
        "v_pk_fma_f32 v[80:81], v[56:57], v[64:65], v[60:61]\n"
        "v_pk_fma_f32 v[82:83], v[58:59], v[66:67], v[62:63]\n"
        "v_cvt_pk_bf16_f32 v84, v76, v77\n"
        "v_cvt_pk_bf16_f32 v85, v78, v79\n"
        "v_cvt_pk_bf16_f32 v86, v80, v81\n"
        "v_cvt_pk_bf16_f32 v87, v82, v83\n"
        "s_cbranch_vccnz L_slow_p310_%=\n"
        "L_ret_p310_%=:\n"
        "ds_write_b64 v20, v[84:85]\n"
        "ds_write_b64 v20, v[86:87] offset:4608\n"
        "v_pk_fma_f32 v[52:53], v[76:77], s[72:73], v[52:53] op_sel_hi:[1,0,1]\n"
        "v_pk_fma_f32 v[54:55], v[78:79], s[72:73], v[54:55] op_sel_hi:[1,0,1]\n"
        "v_pk_fma_f32 v[56:57], v[80:81], s[72:73], v[56:57] op_sel:[0,1,0]\n"
        "v_pk_fma_f32 v[58:59], v[82:83], s[72:73], v[58:59] op_sel:[0,1,0]\n"
        "s_waitcnt lgkmcnt(0)\n"
        "s_barrier\n"
        "ds_read_b128 a[128:131], v21 offset:192\n"
        "ds_read_b128 a[132:135], v21 offset:448\n"
        "ds_read_b128 a[136:139], v21 offset:704\n"
        "ds_read_b128 a[140:143], v21 offset:960\n"
        "ds_read_b128 a[144:147], v21 offset:128\n"
        "ds_read_b128 a[148:151], v21 offset:384\n"
        "s_waitcnt lgkmcnt(5)\n"
        "v_mfma_f32_16x16x32_bf16 v[46:49], a[0:3], a[128:131], 0\n"
        "ds_read_b128 a[152:155], v21 offset:640\n"
        "v_mfma_f32_16x16x32_bf16 v[60:63], a[16:19], a[128:131], 0\n"
        "ds_read_b128 a[156:159], v21 offset:896\n"
        "v_mfma_f32_16x16x32_bf16 v[64:67], a[32:35], a[128:131], 0\n"
        "ds_read_b128 a[160:163], v21 offset:64\n"
        "v_mfma_f32_16x16x32_bf16 v[68:71], a[48:51], a[128:131], 0\n"
        "s_waitcnt lgkmcnt(5)\n"
        "ds_read_b128 a[164:167], v21 offset:320\n"
        "v_mfma_f32_16x16x32_bf16 v[60:63], a[0:3], a[132:135], v[60:63]\n"
        "ds_read_b128 a[168:171], v21 offset:576\n"
        "v_mfma_f32_16x16x32_bf16 v[64:67], a[16:19], a[132:135], v[64:67]\n"
        "ds_read_b128 a[172:175], v21 offset:832\n"
        "v_mfma_f32_16x16x32_bf16 v[68:71], a[32:35], a[132:135], v[68:71]\n"
        "ds_read_b128 a[176:179], v21\n"
        "v_mfma_f32_16x16x32_bf16 v[64:67], a[0:3], a[136:139], v[64:67]\n"
        "ds_read_b128 a[180:183], v21 offset:256\n"
        "v_mfma_f32_16x16x32_bf16 v[68:71], a[16:19], a[136:139], v[68:71]\n"
        "ds_read_b128 a[184:187], v21 offset:512\n"
        "v_mfma_f32_16x16x32_bf16 v[68:71], a[0:3], a[140:143], v[68:71]\n"
        "s_waitcnt lgkmcnt(5)\n"
        "ds_read_b128 a[188:191], v21 offset:768\n"
        "v_mfma_f32_16x16x32_bf16 v[46:49], a[4:7], a[144:147], v[46:49]\n"
        "v_mfma_f32_16x16x32_bf16 v[60:63], a[20:23], a[144:147], v[60:63]\n"
        "v_mfma_f32_16x16x32_bf16 v[64:67], a[36:39], a[144:147], v[64:67]\n"
        "v_mfma_f32_16x16x32_bf16 v[68:71], a[52:55], a[144:147], v[68:71]\n"
        "v_mfma_f32_16x16x32_bf16 v[60:63], a[4:7], a[148:151], v[60:63]\n"
        "v_mfma_f32_16x16x32_bf16 v[64:67], a[20:23], a[148:151], v[64:67]\n"
        "v_mfma_f32_16x16x32_bf16 v[68:71], a[36:39], a[148:151], v[68:71]\n"
        "v_mfma_f32_16x16x32_bf16 v[64:67], a[4:7], a[152:155], v[64:67]\n"
        "v_mfma_f32_16x16x32_bf16 v[68:71], a[20:23], a[152:155], v[68:71]\n"
        "v_mfma_f32_16x16x32_bf16 v[68:71], a[4:7], a[156:159], v[68:71]\n"
        "v_mfma_f32_16x16x32_bf16 v[46:49], a[8:11], a[160:163], v[46:49]\n"
        "v_mfma_f32_16x16x32_bf16 v[60:63], a[24:27], a[160:163], v[60:63]\n"
        "v_mfma_f32_16x16x32_bf16 v[64:67], a[40:43], a[160:163], v[64:67]\n"
        "v_mfma_f32_16x16x32_bf16 v[68:71], a[56:59], a[160:163], v[68:71]\n"
        "v_mfma_f32_16x16x32_bf16 v[60:63], a[8:11], a[164:167], v[60:63]\n"
        "v_mfma_f32_16x16x32_bf16 v[64:67], a[24:27], a[164:167], v[64:67]\n"
        "v_mfma_f32_16x16x32_bf16 v[68:71], a[40:43], a[164:167], v[68:71]\n"
        "s_waitcnt lgkmcnt(0)\n"
        "v_mfma_f32_16x16x32_bf16 v[64:67], a[8:11], a[168:171], v[64:67]\n"
        "v_mfma_f32_16x16x32_bf16 v[68:71], a[24:27], a[168:171], v[68:71]\n"
        "v_mfma_f32_16x16x32_bf16 v[68:71], a[8:11], a[172:175], v[68:71]\n"
        "v_mfma_f32_16x16x32_bf16 v[46:49], a[12:15], a[176:179], v[46:49]\n"
        "v_mfma_f32_16x16x32_bf16 v[60:63], a[28:31], a[176:179], v[60:63]\n"
        "v_mfma_f32_16x16x32_bf16 v[60:63], a[12:15], a[180:183], v[60:63]\n"
        "v_mfma_f32_16x16x32_bf16 v[64:67], a[44:47], a[176:179], v[64:67]\n"
        "s_mov_b32 s46, 0x28000\n"
        "buffer_load_dwordx4 v[116:119], v19, s[20:23], s46 offen sc1\n"
        "s_mov_b32 s46, 0x29000\n"
        "v_mfma_f32_16x16x32_bf16 v[64:67], a[28:31], a[180:183], v[64:67]\n"
        "s_nop 0\n"
        "ds_write_b128 v39, v[46:49]\n"
        "v_mfma_f32_16x16x32_bf16 v[64:67], a[12:15], a[184:187], v[64:67]\n"
        "ds_write_b128 v39, v[60:63] offset:1024\n"
        "v_mfma_f32_16x16x32_bf16 v[68:71], a[60:63], a[176:179], v[68:71]\n"
        "buffer_load_dwordx4 v[120:123], v19, s[20:23], s46 offen sc1\n"
        "s_mov_b32 s46, 0x2a000\n"
        "v_mfma_f32_16x16x32_bf16 v[68:71], a[44:47], a[180:183], v[68:71]\n"
        "s_nop 2\n"
        "ds_write_b128 v39, v[64:67] offset:2048\n"
        "v_mfma_f32_16x16x32_bf16 v[68:71], a[28:31], a[184:187], v[68:71]\n"
        "v_mfma_f32_16x16x32_bf16 v[68:71], a[12:15], a[188:191], v[68:71]\n"
        "buffer_load_dwordx4 v[124:127], v19, s[20:23], s46 offen sc1\n"
        "s_mov_b32 s46, 0x2b000\n"
        "v_mfma_f32_16x16x32_bf16 v[72:75], a[16:19], a[140:143], 0\n"
        "v_mfma_f32_16x16x32_bf16 v[76:79], a[32:35], a[140:143], 0\n"
        "v_mfma_f32_16x16x32_bf16 v[72:75], a[32:35], a[136:139], v[72:75]\n"
        "s_nop 2\n"
        "ds_write_b128 v39, v[68:71] offset:3072\n"
        "v_mfma_f32_16x16x32_bf16 v[80:83], a[48:51], a[140:143], 0\n"
        "buffer_load_dwordx4 v[128:131], v19, s[20:23], s46 offen sc1\n"
        "s_mov_b32 s46, 0x2c000\n"
        "v_mfma_f32_16x16x32_bf16 v[76:79], a[48:51], a[136:139], v[76:79]\n"
        "v_mfma_f32_16x16x32_bf16 v[72:75], a[48:51], a[132:135], v[72:75]\n"
        "v_mfma_f32_16x16x32_bf16 v[84:87], a[64:67], a[140:143], 0\n"
        "s_waitcnt lgkmcnt(0)\n"
        "s_barrier\n"
        "ds_read_b128 v[46:49], v40\n"
        "v_mfma_f32_16x16x32_bf16 v[80:83], a[64:67], a[136:139], v[80:83]\n"
        "buffer_load_dwordx4 v[132:135], v19, s[20:23], s46 offen sc1\n"
        "ds_read_b128 v[60:63], v40 offset:4096\n"
        "v_mfma_f32_16x16x32_bf16 v[76:79], a[64:67], a[132:135], v[76:79]\n"
        "s_mov_b32 s46, 0x2d000\n"
        "ds_read_b128 v[64:67], v40 offset:8192\n"
        "v_mfma_f32_16x16x32_bf16 v[72:75], a[64:67], a[128:131], v[72:75]\n"
        "ds_read_b128 v[68:71], v40 offset:12288\n"
        "s_mov_b32 s91, 0xad4000\n"
        "v_mfma_f32_16x16x32_bf16 v[84:87], a[80:83], a[136:139], v[84:87]\n"
        "v_mfma_f32_16x16x32_bf16 v[80:83], a[80:83], a[132:135], v[80:83]\n"
        "buffer_load_dwordx4 v[136:139], v19, s[20:23], s46 offen sc1\n"
        "s_mov_b32 s46, 0x2e000\n"
        "v_mfma_f32_16x16x32_bf16 v[76:79], a[80:83], a[128:131], v[76:79]\n"
        "s_waitcnt lgkmcnt(3)\n"
        "v_add_f32 v52, v52, v46\n"
        "v_add_f32 v53, v53, v47\n"
        "v_add_f32 v54, v54, v48\n"
        "v_add_f32 v55, v55, v49\n"
        "v_mfma_f32_16x16x32_bf16 v[84:87], a[96:99], a[132:135], v[84:87]\n"
        "s_waitcnt lgkmcnt(1)\n"
        "v_add_f32 v52, v52, v60\n"
        "v_add_f32 v53, v53, v61\n"
        "v_add_f32 v54, v54, v62\n"
        "v_add_f32 v55, v55, v63\n"
        "v_add_f32 v56, v56, v64\n"
        "v_add_f32 v57, v57, v65\n"
        "v_add_f32 v58, v58, v66\n"
        "v_add_f32 v59, v59, v67\n"
        "buffer_load_dwordx4 v[140:143], v19, s[20:23], s46 offen sc1\n"
        "v_mfma_f32_16x16x32_bf16 v[80:83], a[96:99], a[128:131], v[80:83]\n"
        "s_waitcnt lgkmcnt(0)\n"
        "v_add_f32 v56, v56, v68\n"
        "v_add_f32 v57, v57, v69\n"
        "v_add_f32 v58, v58, v70\n"
        "v_add_f32_e64 v59, v59, v71\n"
        "v_cvt_pk_bf16_f32 v46, v52, v53\n"
        "v_cvt_pk_bf16_f32 v47, v54, v55\n"
        "v_cvt_pk_bf16_f32 v48, v56, v57\n"
        "v_cvt_pk_bf16_f32 v49, v58, v59\n"
        "v_mfma_f32_16x16x32_bf16 v[84:87], a[112:115], a[128:131], v[84:87]\n"
        "s_mov_b32 s46, 0x2f000\n"
        "buffer_store_dwordx2 v[46:47], v41, s[20:23], s91 offen sc1\n"
        "buffer_store_dwordx2 v[48:49], v41, s[20:23], s91 offen offset:64 sc1\n"
        "v_mfma_f32_16x16x32_bf16 v[72:75], a[20:23], a[156:159], v[72:75]\n"
        "buffer_load_dwordx4 v[144:147], v19, s[20:23], s46 offen sc1\n"
        "s_lshl_b32 s46, s50, 12\n"
        "v_mfma_f32_16x16x32_bf16 v[76:79], a[36:39], a[156:159], v[76:79]\n"
        "s_add_u32 s46, s46, 0x740000\n"
        "v_mfma_f32_16x16x32_bf16 v[72:75], a[36:39], a[152:155], v[72:75]\n"
        "v_mfma_f32_16x16x32_bf16 v[80:83], a[52:55], a[156:159], v[80:83]\n"
        "v_mfma_f32_16x16x32_bf16 v[76:79], a[52:55], a[152:155], v[76:79]\n"
        "buffer_load_dwordx4 v[112:115], v19, s[20:23], s46 offen sc1\n"
        "v_mfma_f32_16x16x32_bf16 v[72:75], a[52:55], a[148:151], v[72:75]\n"
        "v_mfma_f32_16x16x32_bf16 v[84:87], a[68:71], a[156:159], v[84:87]\n"
        "v_mfma_f32_16x16x32_bf16 v[80:83], a[68:71], a[152:155], v[80:83]\n"
        "v_mfma_f32_16x16x32_bf16 v[76:79], a[68:71], a[148:151], v[76:79]\n"
        "v_mfma_f32_16x16x32_bf16 v[72:75], a[68:71], a[144:147], v[72:75]\n"
        "v_mfma_f32_16x16x32_bf16 v[84:87], a[84:87], a[152:155], v[84:87]\n"
        "v_mfma_f32_16x16x32_bf16 v[80:83], a[84:87], a[148:151], v[80:83]\n"
        "v_mfma_f32_16x16x32_bf16 v[76:79], a[84:87], a[144:147], v[76:79]\n"
        "v_mfma_f32_16x16x32_bf16 v[84:87], a[100:103], a[148:151], v[84:87]\n"
        "v_mfma_f32_16x16x32_bf16 v[80:83], a[100:103], a[144:147], v[80:83]\n"
        "v_mfma_f32_16x16x32_bf16 v[84:87], a[116:119], a[144:147], v[84:87]\n"
        "v_mfma_f32_16x16x32_bf16 v[72:75], a[24:27], a[172:175], v[72:75]\n"
        "v_mfma_f32_16x16x32_bf16 v[76:79], a[40:43], a[172:175], v[76:79]\n"
        "v_mfma_f32_16x16x32_bf16 v[72:75], a[40:43], a[168:171], v[72:75]\n"
        "v_mfma_f32_16x16x32_bf16 v[80:83], a[56:59], a[172:175], v[80:83]\n"
        "v_mfma_f32_16x16x32_bf16 v[76:79], a[56:59], a[168:171], v[76:79]\n"
        "v_mfma_f32_16x16x32_bf16 v[72:75], a[56:59], a[164:167], v[72:75]\n"
        "v_mfma_f32_16x16x32_bf16 v[84:87], a[72:75], a[172:175], v[84:87]\n"
        "v_mfma_f32_16x16x32_bf16 v[80:83], a[72:75], a[168:171], v[80:83]\n"
        "v_mfma_f32_16x16x32_bf16 v[76:79], a[72:75], a[164:167], v[76:79]\n"
        "v_mfma_f32_16x16x32_bf16 v[72:75], a[72:75], a[160:163], v[72:75]\n"
        "v_mfma_f32_16x16x32_bf16 v[84:87], a[88:91], a[168:171], v[84:87]\n"
        "v_mfma_f32_16x16x32_bf16 v[80:83], a[88:91], a[164:167], v[80:83]\n"
        "v_mfma_f32_16x16x32_bf16 v[76:79], a[88:91], a[160:163], v[76:79]\n"
        "v_mfma_f32_16x16x32_bf16 v[84:87], a[104:107], a[164:167], v[84:87]\n"
        "v_mfma_f32_16x16x32_bf16 v[80:83], a[104:107], a[160:163], v[80:83]\n"
        "v_mfma_f32_16x16x32_bf16 v[84:87], a[120:123], a[160:163], v[84:87]\n"
        "v_mfma_f32_16x16x32_bf16 v[72:75], a[28:31], a[188:191], v[72:75]\n"
        "v_mfma_f32_16x16x32_bf16 v[76:79], a[44:47], a[188:191], v[76:79]\n"
        "v_mfma_f32_16x16x32_bf16 v[72:75], a[44:47], a[184:187], v[72:75]\n"
        "v_mfma_f32_16x16x32_bf16 v[80:83], a[60:63], a[188:191], v[80:83]\n"
        "v_mfma_f32_16x16x32_bf16 v[76:79], a[60:63], a[184:187], v[76:79]\n"
        "v_mfma_f32_16x16x32_bf16 v[72:75], a[60:63], a[180:183], v[72:75]\n"
        "v_mfma_f32_16x16x32_bf16 v[84:87], a[76:79], a[188:191], v[84:87]\n"
        "v_mfma_f32_16x16x32_bf16 v[80:83], a[76:79], a[184:187], v[80:83]\n"
        "v_mfma_f32_16x16x32_bf16 v[76:79], a[76:79], a[180:183], v[76:79]\n"
        "v_mfma_f32_16x16x32_bf16 v[72:75], a[76:79], a[176:179], v[72:75]\n"
        "v_mfma_f32_16x16x32_bf16 v[84:87], a[92:95], a[184:187], v[84:87]\n"
        "v_mfma_f32_16x16x32_bf16 v[80:83], a[92:95], a[180:183], v[80:83]\n"
        "v_mfma_f32_16x16x32_bf16 v[76:79], a[92:95], a[176:179], v[76:79]\n"
        "v_mfma_f32_16x16x32_bf16 v[84:87], a[108:111], a[180:183], v[84:87]\n"
        "v_mfma_f32_16x16x32_bf16 v[80:83], a[108:111], a[176:179], v[80:83]\n"
        "v_mfma_f32_16x16x32_bf16 v[84:87], a[124:127], a[176:179], v[84:87]\n"
        "L_re_p311_%=:\n"
        "s_waitcnt vmcnt(8)\n"
        "v_pk_add_f32 v[116:117], v[116:117], v[124:125]\n"
        "v_pk_add_f32 v[118:119], v[118:119], v[126:127]\n"
        "s_waitcnt vmcnt(7)\n"
        "v_pk_add_f32 v[120:121], v[120:121], v[128:129]\n"
        "v_pk_add_f32 v[122:123], v[122:123], v[130:131]\n"
        "s_waitcnt vmcnt(4)\n"
        "v_pk_add_f32 v[132:133], v[132:133], v[140:141]\n"
        "v_pk_add_f32 v[134:135], v[134:135], v[142:143]\n"
        "s_waitcnt vmcnt(1)\n"
        "v_pk_add_f32 v[136:137], v[136:137], v[144:145]\n"
        "v_pk_add_f32 v[138:139], v[138:139], v[146:147]\n"
        "v_pk_add_f32 v[116:117], v[116:117], v[132:133]\n"
        "v_pk_add_f32 v[118:119], v[118:119], v[134:135]\n"
        "v_pk_add_f32 v[120:121], v[120:121], v[136:137]\n"
        "v_pk_add_f32 v[122:123], v[122:123], v[138:139]\n"
        "v_pk_mul_f32 v[116:117], v[116:117], s[60:61] op_sel_hi:[1,0]\n"
        "v_pk_fma_f32 v[120:121], v[120:121], s[60:61], s[60:61] op_sel:[0,0,1] op_sel_hi:[1,0,1]\n"
        "v_pk_mul_f32 v[118:119], v[118:119], s[60:61] op_sel_hi:[1,0]\n"
        "v_pk_fma_f32 v[122:123], v[122:123], s[60:61], s[60:61] op_sel:[0,0,1] op_sel_hi:[1,0,1]\n"
        "v_pk_fma_f32 v[120:121], v[116:117], v[116:117], v[120:121] neg_lo:[1,0,0] neg_hi:[1,0,0]\n"
        "v_pk_fma_f32 v[122:123], v[118:119], v[118:119], v[122:123] neg_lo:[1,0,0] neg_hi:[1,0,0]\n"
        "v_maximum3_f32 v120, s61, v120, v120\n"
        "v_maximum3_f32 v121, s61, v121, v121\n"
        "v_maximum3_f32 v122, s61, v122, v122\n"
        "v_maximum3_f32 v123, s61, v123, v123\n"
        "v_rsq_f32 v120, v120\n"
        "v_rsq_f32 v121, v121\n"
        "v_rsq_f32 v122, v122\n"
        "v_rsq_f32 v123, v123\n"
        "s_mov_b32 s40, 0xffff0000\n"
        "s_waitcnt vmcnt(0)\n"
        "v_lshlrev_b32 v52, 16, v112\n"
        "v_and_b32 v56, s40, v112\n"
        "v_lshlrev_b32 v53, 16, v113\n"
        "v_and_b32 v57, s40, v113\n"
        "v_lshlrev_b32 v54, 16, v114\n"
        "v_and_b32 v58, s40, v114\n"
        "v_lshlrev_b32 v55, 16, v115\n"
        "v_and_b32 v59, s40, v115\n"
        "v_pk_mul_f32 v[116:117], v[116:117], v[120:121] neg_lo:[1,0] neg_hi:[1,0]\n"
        "v_pk_mul_f32 v[118:119], v[118:119], v[122:123] neg_lo:[1,0] neg_hi:[1,0]\n"
        "v_pk_fma_f32 v[132:133], v[52:53], v[120:121], v[116:117]\n"
        "v_pk_fma_f32 v[134:135], v[54:55], v[122:123], v[118:119]\n"
        "v_pk_add_f32 v[124:125], v[132:133], v[134:135]\n"
        "v_cmp_u_f32_e64 vcc, v124, v125\n"
        "v_pk_fma_f32 v[136:137], v[56:57], v[120:121], v[116:117]\n"
        "v_pk_fma_f32 v[138:139], v[58:59], v[122:123], v[118:119]\n"
        "v_cvt_pk_bf16_f32 v140, v132, v133\n"
        "v_cvt_pk_bf16_f32 v141, v134, v135\n"
        "v_cvt_pk_bf16_f32 v142, v136, v137\n"
        "v_cvt_pk_bf16_f32 v143, v138, v139\n"
        "s_cbranch_vccnz L_slow_p311_%=\n"
        "L_ret_p311_%=:\n"
        "ds_write_b64 v20, v[140:141] offset:1024\n"
        "ds_write_b64 v20, v[142:143] offset:5632\n"
        "v_pk_fma_f32 v[52:53], v[132:133], s[72:73], v[52:53] op_sel_hi:[1,0,1]\n"
        "v_pk_fma_f32 v[54:55], v[134:135], s[72:73], v[54:55] op_sel_hi:[1,0,1]\n"
        "v_pk_fma_f32 v[56:57], v[136:137], s[72:73], v[56:57] op_sel:[0,1,0]\n"
        "v_pk_fma_f32 v[58:59], v[138:139], s[72:73], v[58:59] op_sel:[0,1,0]\n"
        "s_waitcnt lgkmcnt(0)\n"
        "s_barrier\n"
        "ds_read_b128 a[128:131], v21 offset:1216\n"
        "ds_read_b128 a[132:135], v21 offset:1472\n"
        "ds_read_b128 a[136:139], v21 offset:1728\n"
        "ds_read_b128 a[140:143], v21 offset:1984\n"
        "ds_read_b128 a[144:147], v21 offset:1152\n"
        "ds_read_b128 a[148:151], v21 offset:1408\n"
        "s_waitcnt lgkmcnt(5)\n"
        "v_mfma_f32_16x16x32_bf16 v[72:75], a[0:3], a[128:131], v[72:75]\n"
        "ds_read_b128 a[152:155], v21 offset:1664\n"
        "v_mfma_f32_16x16x32_bf16 v[76:79], a[16:19], a[128:131], v[76:79]\n"
        "ds_read_b128 a[156:159], v21 offset:1920\n"
        "v_mfma_f32_16x16x32_bf16 v[80:83], a[32:35], a[128:131], v[80:83]\n"
        "ds_read_b128 a[160:163], v21 offset:1088\n"
        "v_mfma_f32_16x16x32_bf16 v[84:87], a[48:51], a[128:131], v[84:87]\n"
        "s_waitcnt lgkmcnt(5)\n"
        "ds_read_b128 a[164:167], v21 offset:1344\n"
        "v_mfma_f32_16x16x32_bf16 v[76:79], a[0:3], a[132:135], v[76:79]\n"
        "ds_read_b128 a[168:171], v21 offset:1600\n"
        "v_mfma_f32_16x16x32_bf16 v[80:83], a[16:19], a[132:135], v[80:83]\n"
        "ds_read_b128 a[172:175], v21 offset:1856\n"
        "v_mfma_f32_16x16x32_bf16 v[84:87], a[32:35], a[132:135], v[84:87]\n"
        "ds_read_b128 a[176:179], v21 offset:1024\n"
        "v_mfma_f32_16x16x32_bf16 v[80:83], a[0:3], a[136:139], v[80:83]\n"
        "ds_read_b128 a[180:183], v21 offset:1280\n"
        "v_mfma_f32_16x16x32_bf16 v[84:87], a[16:19], a[136:139], v[84:87]\n"
        "ds_read_b128 a[184:187], v21 offset:1536\n"
        "v_mfma_f32_16x16x32_bf16 v[84:87], a[0:3], a[140:143], v[84:87]\n"
        "s_waitcnt lgkmcnt(5)\n"
        "ds_read_b128 a[188:191], v21 offset:1792\n"
        "v_mfma_f32_16x16x32_bf16 v[72:75], a[4:7], a[144:147], v[72:75]\n"
        "v_mfma_f32_16x16x32_bf16 v[76:79], a[20:23], a[144:147], v[76:79]\n"
        "s_lshr_b32 s46, s79, 1\n"
        "s_add_u32 m0, s46, 0xa500\n"
        "s_add_u32 s46, s9, 0xad4000\n"
        "v_mfma_f32_16x16x32_bf16 v[80:83], a[36:39], a[144:147], v[80:83]\n"
        "buffer_load_dwordx4 v4, s[20:23], s46 offen sc1 lds\n"
        "v_mfma_f32_16x16x32_bf16 v[84:87], a[52:55], a[144:147], v[84:87]\n"
        "v_mfma_f32_16x16x32_bf16 v[76:79], a[4:7], a[148:151], v[76:79]\n"
        "v_mfma_f32_16x16x32_bf16 v[80:83], a[20:23], a[148:151], v[80:83]\n"
        "v_mfma_f32_16x16x32_bf16 v[84:87], a[36:39], a[148:151], v[84:87]\n"
        "v_mfma_f32_16x16x32_bf16 v[80:83], a[4:7], a[152:155], v[80:83]\n"
        "v_mfma_f32_16x16x32_bf16 v[84:87], a[20:23], a[152:155], v[84:87]\n"
        "v_mfma_f32_16x16x32_bf16 v[84:87], a[4:7], a[156:159], v[84:87]\n"
        "v_mfma_f32_16x16x32_bf16 v[72:75], a[8:11], a[160:163], v[72:75]\n"
        "v_mfma_f32_16x16x32_bf16 v[76:79], a[24:27], a[160:163], v[76:79]\n"
        "v_mfma_f32_16x16x32_bf16 v[80:83], a[40:43], a[160:163], v[80:83]\n"
        "v_mfma_f32_16x16x32_bf16 v[84:87], a[56:59], a[160:163], v[84:87]\n"
        "v_mfma_f32_16x16x32_bf16 v[76:79], a[8:11], a[164:167], v[76:79]\n"
        "v_mfma_f32_16x16x32_bf16 v[80:83], a[24:27], a[164:167], v[80:83]\n"
        "v_mfma_f32_16x16x32_bf16 v[84:87], a[40:43], a[164:167], v[84:87]\n"
        "s_waitcnt lgkmcnt(0)\n"
        "v_mfma_f32_16x16x32_bf16 v[80:83], a[8:11], a[168:171], v[80:83]\n"
        "v_mfma_f32_16x16x32_bf16 v[84:87], a[24:27], a[168:171], v[84:87]\n"
        "v_mfma_f32_16x16x32_bf16 v[84:87], a[8:11], a[172:175], v[84:87]\n"
        "v_mfma_f32_16x16x32_bf16 v[72:75], a[12:15], a[176:179], v[72:75]\n"
        "v_mfma_f32_16x16x32_bf16 v[76:79], a[28:31], a[176:179], v[76:79]\n"
        "v_mfma_f32_16x16x32_bf16 v[76:79], a[12:15], a[180:183], v[76:79]\n"
        "v_mfma_f32_16x16x32_bf16 v[80:83], a[44:47], a[176:179], v[80:83]\n"
        "v_mfma_f32_16x16x32_bf16 v[80:83], a[28:31], a[180:183], v[80:83]\n"
        "v_mfma_f32_16x16x32_bf16 v[80:83], a[12:15], a[184:187], v[80:83]\n"
        "v_mfma_f32_16x16x32_bf16 v[84:87], a[60:63], a[176:179], v[84:87]\n"
        "v_mfma_f32_16x16x32_bf16 v[84:87], a[44:47], a[180:183], v[84:87]\n"
        "s_nop 0\n"
        "ds_write_b128 v39, v[72:75]\n"
        "v_mfma_f32_16x16x32_bf16 v[84:87], a[28:31], a[184:187], v[84:87]\n"
        "ds_write_b128 v39, v[76:79] offset:1024\n"
        "v_mfma_f32_16x16x32_bf16 v[84:87], a[12:15], a[188:191], v[84:87]\n"
        "s_nop 0\n"
        "ds_write_b128 v39, v[80:83] offset:2048\n"
        "v_mov_b32 v1, v1\n"
        "s_nop 3\n"
        "s_nop 0\n"
        "ds_write_b128 v39, v[84:87] offset:3072\n"
        "v_mov_b32 v1, v1\n"
        "s_nop 3\n"
        "v_mov_b32 v1, v1\n"
        "s_nop 3\n"
        "v_mov_b32_e64 v1, v1\n"
        "s_nop 0\n"
        "s_waitcnt lgkmcnt(0)\n"
        "s_barrier\n"
        "ds_read_b128 v[72:75], v40\n"
        "ds_read_b128 v[76:79], v40 offset:4096\n"
        "ds_read_b128 v[80:83], v40 offset:8192\n"
        "ds_read_b128 v[84:87], v40 offset:12288\n"
        "s_mov_b32 s91, 0xc14000\n"
        "s_waitcnt lgkmcnt(3)\n"
        "v_pk_add_f32 v[52:53], v[52:53], v[72:73]\n"
        "v_pk_add_f32 v[54:55], v[54:55], v[74:75]\n"
        "s_waitcnt lgkmcnt(2)\n"
        "v_pk_add_f32 v[52:53], v[52:53], v[76:77]\n"
        "v_pk_add_f32 v[54:55], v[54:55], v[78:79]\n"
        "s_waitcnt lgkmcnt(1)\n"
        "v_pk_add_f32 v[56:57], v[56:57], v[80:81]\n"
        "v_pk_add_f32 v[58:59], v[58:59], v[82:83]\n"
        "s_waitcnt lgkmcnt(0)\n"
        "v_pk_add_f32 v[56:57], v[56:57], v[84:85]\n"
        "v_pk_add_f32 v[58:59], v[58:59], v[86:87]\n"
        "v_cvt_pk_bf16_f32 v72, v52, v53\n"
        "v_cvt_pk_bf16_f32 v73, v54, v55\n"
        "v_cvt_pk_bf16_f32 v74, v56, v57\n"
        "v_cvt_pk_bf16_f32 v75, v58, v59\n"
        "buffer_store_dwordx2 v[72:73], v41, s[20:23], s91 offen sc1\n"
        "buffer_store_dwordx2 v[74:75], v41, s[20:23], s91 offen offset:64 sc1\n"
        "s_mov_b32 s48, 0\n"
        "s_waitcnt vmcnt(2)\n"
        "ds_read_b128 v[116:119], v32\n"
        "ds_read_b64_tr_b16 v[112:113], v33\n"
        "ds_read_b64_tr_b16 v[114:115], v33 offset:256\n"
        "s_waitcnt lgkmcnt(2)\n"
        "v_max3_u32 v116, v116, v117, v118\n"
        "v_max_u32 v116, v116, v119\n"
        "v_cmp_eq_u32 vcc, -1, v116\n"
        "s_cbranch_vccnz L_slow_y0_%=\n"
        "L_ret_y0_%=:\n"
        "s_waitcnt lgkmcnt(0)\n"
        "buffer_store_dwordx4 v[112:115], v5, s[24:27], s48 offen \n"
        "s_mov_b32 s48, 0x100000\n"
        "s_lshr_b32 s46, s79, 1\n"
        "s_add_u32 m0, s46, 0xb500\n"
        "s_add_u32 s46, s9, 0xc14000\n"
        "s_sleep 14\n"
        "L_yl_%=:\n"
        "buffer_load_dwordx4 v4, s[20:23], s46 offen sc1 lds\n"
        "s_sub_u32 s59, s59, 1\n"
        "s_max_i32 s59, s59, 0\n"
        "s_waitcnt vmcnt(0)\n"
        "ds_read_b128 v[92:95], v32 offset:4096\n"
        "ds_read_b64_tr_b16 v[88:89], v33 offset:4096\n"
        "ds_read_b64_tr_b16 v[90:91], v33 offset:4352\n"
        "s_waitcnt lgkmcnt(2)\n"
        "v_max3_u32 v92, v92, v93, v94\n"
        "v_max_u32 v92, v92, v95\n"
        "v_cmp_eq_u32 vcc, -1, v92\n"
        "s_cmp_eq_u32 s59, 0\n"
        "s_cselect_b64 vcc, 0, vcc\n"
        "s_waitcnt lgkmcnt(0)\n"
        "s_cbranch_vccnz L_yl_%=\n"
        "buffer_store_dwordx4 v[88:91], v5, s[24:27], s48 offen \n"
        "s_endpgm\n"
        "L_slow_flag0g_%=:\n"
        "s_nop 7\n"
        "s_mov_b32 s36, s46\n"
        "s_mov_b32 s37, m0\n"
        "L_loop_flag0g_%=:\n"
        "s_cmp_eq_u32 s59, 0\n"
        "s_cbranch_scc1 L_out_flag0g_%=\n"
        "s_sub_u32 s59, s59, 1\n"
        "v_and_b32 v50, 7, v1\n"
        "v_lshlrev_b32 v50, 6, v50\n"
        "s_lshl_b32 s96, s52, 9\n"
        "s_add_u32 s96, s96, 0x1000\n"
        "buffer_load_dwordx2 v[48:49], v50, s[20:23], s96 offen sc1\n"
        "s_waitcnt vmcnt(0) lgkmcnt(0)\n"
        "L_chk_flag0g_%=:\n"
        "v_cmp_ne_u64 vcc, v[48:49], s[62:63]\n"
        "s_waitcnt vmcnt(0) lgkmcnt(0)\n"
        "s_cbranch_vccnz L_loop_flag0g_%=\n"
        "L_out_flag0g_%=:\n"
        "s_mov_b32 s46, s36\n"
        "s_mov_b32 m0, s37\n"
        "s_nop 7\n"
        "s_branch L_ret_flag0g_%=\n"
        "L_slow_o00_%=:\n"
        "s_nop 7\n"
        "s_mov_b32 s36, s46\n"
        "s_mov_b32 s37, m0\n"
        "L_loop_o00_%=:\n"
        "s_cmp_eq_u32 s59, 0\n"
        "s_cbranch_scc1 L_out_o00_%=\n"
        "s_sub_u32 s59, s59, 1\n"
        "s_add_u32 m0, s81, 0x8400\n"
        "s_add_u32 s46, s83, 0x40000\n"
        "buffer_load_dwordx4 v4, s[20:23], s46 offen sc1 lds\n"
        "s_add_u32 m0, s81, 0x8800\n"
        "s_add_u32 s46, s84, 0x40000\n"
        "buffer_load_dwordx4 v4, s[20:23], s46 offen sc1 lds\n"
        "s_add_u32 m0, s81, 0x8c00\n"
        "s_add_u32 s46, s85, 0x40000\n"
        "buffer_load_dwordx4 v4, s[20:23], s46 offen sc1 lds\n"
        "s_add_u32 m0, s81, 0x9000\n"
        "s_add_u32 s46, s86, 0x40000\n"
        "buffer_load_dwordx4 v4, s[20:23], s46 offen sc1 lds\n"
        "s_waitcnt vmcnt(0) lgkmcnt(0)\n"
        "L_chk_o00_%=:\n"
        "s_lshl_b32 s43, s51, 12\n"
        "s_add_u32 s43, s43, 0x8400\n"
        "v_add_u32_e64 v75, s43, v4\n"
        "ds_read_b128 v[78:81], v75\n"
        "ds_read_b128 v[94:97], v75 offset:1024\n"
        "ds_read_b128 v[98:101], v75 offset:2048\n"
        "ds_read_b128 v[102:105], v75 offset:3072\n"
        "s_waitcnt lgkmcnt(3)\n"
        "v_max3_u32 v74, v78, v79, v80\n"
        "s_waitcnt lgkmcnt(2)\n"
        "v_max3_u32 v74, v74, v81, v94\n"
        "v_max3_u32 v74, v74, v95, v96\n"
        "s_waitcnt lgkmcnt(1)\n"
        "v_max3_u32 v74, v74, v97, v98\n"
        "v_max3_u32 v74, v74, v99, v100\n"
        "s_waitcnt lgkmcnt(0)\n"
        "v_max3_u32 v74, v74, v101, v102\n"
        "v_max3_u32 v74, v74, v103, v104\n"
        "v_max_u32 v74, v74, v105\n"
        "v_cmp_eq_u32 vcc, -1, v74\n"
        "s_waitcnt vmcnt(0) lgkmcnt(0)\n"
        "s_cbranch_vccnz L_loop_o00_%=\n"
        "L_out_o00_%=:\n"
        "s_mov_b32 s46, s36\n"
        "s_mov_b32 m0, s37\n"
        "s_nop 7\n"
        "s_branch L_ret_o00_%=\n"
        "L_slow_flag0_%=:\n"
        "s_nop 7\n"
        "s_mov_b32 s36, s46\n"
        "s_mov_b32 s37, m0\n"
        "L_loop_flag0_%=:\n"
        "s_cmp_eq_u32 s59, 0\n"
        "s_cbranch_scc1 L_out_flag0_%=\n"
        "s_sub_u32 s59, s59, 1\n"
        "v_lshlrev_b32_e64 v45, 6, v0\n"
        "s_mov_b32 s91, 0x1000\n"
        "buffer_load_dwordx2 v[46:47], v45, s[20:23], s91 offen sc1\n"
        "s_waitcnt vmcnt(0) lgkmcnt(0)\n"
        "L_chk_flag0_%=:\n"
        "v_cmp_ne_u64 vcc, v[46:47], s[62:63]\n"
        "s_waitcnt vmcnt(0) lgkmcnt(0)\n"
        "s_cbranch_vccnz L_loop_flag0_%=\n"
        "L_out_flag0_%=:\n"
        "s_mov_b32 s46, s36\n"
        "s_mov_b32 m0, s37\n"
        "s_nop 7\n"
        "s_branch L_ret_flag0_%=\n"
        "L_slow_o01_%=:\n"
        "s_nop 7\n"
        "s_mov_b32 s36, s46\n"
        "s_mov_b32 s37, m0\n"
        "L_loop_o01_%=:\n"
        "s_cmp_eq_u32 s59, 0\n"
        "s_cbranch_scc1 L_out_o01_%=\n"
        "s_sub_u32 s59, s59, 1\n"
        "s_add_u32 m0, s81, 0\n"
        "s_add_u32 s46, s83, 0x140000\n"
        "buffer_load_dwordx4 v4, s[20:23], s46 offen sc1 lds\n"
        "s_add_u32 m0, s81, 0x400\n"
        "s_add_u32 s46, s84, 0x140000\n"
        "buffer_load_dwordx4 v4, s[20:23], s46 offen sc1 lds\n"
        "s_add_u32 m0, s81, 0x800\n"
        "s_add_u32 s46, s85, 0x140000\n"
        "buffer_load_dwordx4 v4, s[20:23], s46 offen sc1 lds\n"
        "s_add_u32 m0, s81, 0xc00\n"
        "s_add_u32 s46, s86, 0x140000\n"
        "buffer_load_dwordx4 v4, s[20:23], s46 offen sc1 lds\n"
        "s_waitcnt vmcnt(0) lgkmcnt(0)\n"
        "L_chk_o01_%=:\n"
        "s_lshl_b32 s43, s51, 12\n"
        "s_add_u32 s43, s43, 0\n"
        "v_add_u32 v139, s43, v4\n"
        "ds_read_b128 v[142:145], v139\n"
        "ds_read_b128 v[146:149], v139 offset:1024\n"
        "ds_read_b128 v[150:153], v139 offset:2048\n"
        "ds_read_b128 v[154:157], v139 offset:3072\n"
        "s_waitcnt lgkmcnt(3)\n"
        "v_max3_u32 v138, v142, v143, v144\n"
        "s_waitcnt lgkmcnt(2)\n"
        "v_max3_u32 v138, v138, v145, v146\n"
        "v_max3_u32 v138, v138, v147, v148\n"
        "s_waitcnt lgkmcnt(1)\n"
        "v_max3_u32 v138, v138, v149, v150\n"
        "v_max3_u32 v138, v138, v151, v152\n"
        "s_waitcnt lgkmcnt(0)\n"
        "v_max3_u32 v138, v138, v153, v154\n"
        "v_max3_u32 v138, v138, v155, v156\n"
        "v_max_u32 v138, v138, v157\n"
        "v_cmp_eq_u32_e64 vcc, -1, v138\n"
        "s_waitcnt vmcnt(0) lgkmcnt(0)\n"
        "s_cbranch_vccnz L_loop_o01_%=\n"
        "L_out_o01_%=:\n"
        "s_mov_b32 s46, s36\n"
        "s_mov_b32 m0, s37\n"
        "s_nop 7\n"
        "s_branch L_ret_o01_%=\n"
        "L_slow_p300_%=:\n"
        "L_pg9_%=:\n"
        "s_cbranch_execz L_pgret_%=\n"
        "s_bitcmp1_b32 s76, 0\n"
        "s_cbranch_scc1 L_ret_p300_%=\n"
        "s_bitset1_b32 s76, 0\n"
        "s_add_u32 s59, s59, 1\n"
        "s_mov_b32 s36, s46\n"
        "L_loop_p300_%=:\n"
        "s_cmp_eq_u32 s59, 0\n"
        "s_cbranch_scc1 L_out_p300_%=\n"
        "s_sub_u32 s59, s59, 1\n"
        "s_mov_b32 s46, 0x10000\n"
        "buffer_load_dwordx4 v[62:65], v19, s[20:23], s46 offen sc1\n"
        "s_mov_b32 s46, 0x11000\n"
        "buffer_load_dwordx4 v[66:69], v19, s[20:23], s46 offen sc1\n"
        "s_mov_b32 s46, 0x12000\n"
        "buffer_load_dwordx4 v[70:73], v19, s[20:23], s46 offen sc1\n"
        "s_mov_b32 s46, 0x13000\n"
        "buffer_load_dwordx4 v[74:77], v19, s[20:23], s46 offen sc1\n"
        "s_mov_b32 s46, 0x14000\n"
        "buffer_load_dwordx4 v[78:81], v19, s[20:23], s46 offen sc1\n"
        "s_mov_b32 s46, 0x15000\n"
        "buffer_load_dwordx4 v[82:85], v19, s[20:23], s46 offen sc1\n"
        "s_mov_b32 s46, 0x16000\n"
        "buffer_load_dwordx4 v[86:89], v19, s[20:23], s46 offen sc1\n"
        "s_mov_b32 s46, 0x17000\n"
        "buffer_load_dwordx4 v[90:93], v19, s[20:23], s46 offen sc1\n"
        "s_lshl_b32 s46, s50, 12\n"
        "s_add_u32 s46, s46, 0x240000\n"
        "buffer_load_dwordx4 v[58:61], v19, s[20:23], s46 offen sc1\n"
        "s_waitcnt vmcnt(0) lgkmcnt(0)\n"
        "L_chk_p300_%=:\n"
        "v_max3_u32 v57, v62, v63, v64\n"
        "v_max3_u32 v57, v57, v65, v66\n"
        "v_max3_u32 v57, v57, v67, v68\n"
        "v_max3_u32 v57, v57, v69, v70\n"
        "v_max3_u32 v57, v57, v71, v72\n"
        "v_max3_u32 v57, v57, v73, v74\n"
        "v_max3_u32 v57, v57, v75, v76\n"
        "v_max3_u32 v57, v57, v77, v78\n"
        "v_max3_u32 v57, v57, v79, v80\n"
        "v_max3_u32 v57, v57, v81, v82\n"
        "v_max3_u32 v57, v57, v83, v84\n"
        "v_max3_u32 v57, v57, v85, v86\n"
        "v_max3_u32 v57, v57, v87, v88\n"
        "v_max3_u32 v57, v57, v89, v90\n"
        "v_max3_u32 v57, v57, v91, v92\n"
        "v_max3_u32 v57, v57, v93, v58\n"
        "v_max3_u32 v57, v57, v59, v60\n"
        "v_max_u32 v57, v57, v61\n"
        "v_cmp_eq_u32_e64 vcc, -1, v57\n"
        "s_waitcnt vmcnt(0) lgkmcnt(0)\n"
        "s_cbranch_vccnz L_loop_p300_%=\n"
        "L_out_p300_%=:\n"
        "s_mov_b32 s46, s36\n"
        "s_branch L_re_p300_%=\n"
        "L_slow_p301_%=:\n"
        "L_pg10_%=:\n"
        "s_cbranch_execz L_pgret_%=\n"
        "s_bitcmp1_b32 s76, 1\n"
        "s_cbranch_scc1 L_ret_p301_%=\n"
        "s_bitset1_b32 s76, 1\n"
        "s_add_u32 s59, s59, 1\n"
        "s_mov_b32 s36, s46\n"
        "L_loop_p301_%=:\n"
        "s_cmp_eq_u32 s59, 0\n"
        "s_cbranch_scc1 L_out_p301_%=\n"
        "s_sub_u32 s59, s59, 1\n"
        "s_mov_b32 s46, 0x18000\n"
        "buffer_load_dwordx4 v[146:149], v19, s[20:23], s46 offen sc1\n"
        "s_mov_b32 s46, 0x19000\n"
        "buffer_load_dwordx4 v[150:153], v19, s[20:23], s46 offen sc1\n"
        "s_mov_b32 s46, 0x1a000\n"
        "buffer_load_dwordx4 v[154:157], v19, s[20:23], s46 offen sc1\n"
        "s_mov_b32 s46, 0x1b000\n"
        "buffer_load_dwordx4 v[158:161], v19, s[20:23], s46 offen sc1\n"
        "s_mov_b32 s46, 0x1c000\n"
        "buffer_load_dwordx4 v[162:165], v19, s[20:23], s46 offen sc1\n"
        "s_mov_b32 s46, 0x1d000\n"
        "buffer_load_dwordx4 v[166:169], v19, s[20:23], s46 offen sc1\n"
        "s_mov_b32 s46, 0x1e000\n"
        "buffer_load_dwordx4 v[170:173], v19, s[20:23], s46 offen sc1\n"
        "s_mov_b32 s46, 0x1f000\n"
        "buffer_load_dwordx4 v[174:177], v19, s[20:23], s46 offen sc1\n"
        "s_lshl_b32 s46, s50, 12\n"
        "s_add_u32 s46, s46, 0x340000\n"
        "buffer_load_dwordx4 v[142:145], v19, s[20:23], s46 offen sc1\n"
        "s_waitcnt vmcnt(0) lgkmcnt(0)\n"
        "L_chk_p301_%=:\n"
        "v_max3_u32 v45, v146, v147, v148\n"
        "v_max3_u32 v45, v45, v149, v150\n"
        "v_max3_u32 v45, v45, v151, v152\n"
        "v_max3_u32 v45, v45, v153, v154\n"
        "v_max3_u32 v45, v45, v155, v156\n"
        "v_max3_u32 v45, v45, v157, v158\n"
        "v_max3_u32 v45, v45, v159, v160\n"
        "v_max3_u32 v45, v45, v161, v162\n"
        "v_max3_u32 v45, v45, v163, v164\n"
        "v_max3_u32 v45, v45, v165, v166\n"
        "v_max3_u32 v45, v45, v167, v168\n"
        "v_max3_u32 v45, v45, v169, v170\n"
        "v_max3_u32 v45, v45, v171, v172\n"
        "v_max3_u32 v45, v45, v173, v174\n"
        "v_max3_u32 v45, v45, v175, v176\n"
        "v_max3_u32 v45, v45, v177, v142\n"
        "v_max3_u32 v45, v45, v143, v144\n"
        "v_max_u32 v45, v45, v145\n"
        "v_cmp_eq_u32_e64 vcc, -1, v45\n"
        "s_waitcnt vmcnt(0) lgkmcnt(0)\n"
        "s_cbranch_vccnz L_loop_p301_%=\n"
        "L_out_p301_%=:\n"
        "s_mov_b32 s46, s36\n"
        "s_branch L_re_p301_%=\n"
        "L_slow_x20_%=:\n"
        "s_nop 7\n"
        "s_mov_b32 s36, s46\n"
        "s_mov_b32 s37, m0\n"
        "L_loop_x20_%=:\n"
        "s_cmp_eq_u32 s59, 0\n"
        "s_cbranch_scc1 L_out_x20_%=\n"
        "s_sub_u32 s59, s59, 1\n"
        "s_lshl1_add_u32 m0, s81, 0\n"
        "s_add_u32 s46, s28, 0x840000\n"
        "buffer_load_dwordx4 v4, s[20:23], s46 offen sc1 lds\n"
        "buffer_load_dwordx4 v4, s[20:23], s46 offen offset:1024 sc1 lds\n"
        "s_lshl1_add_u32 m0, s81, 0x800\n"
        "s_add_u32 s46, s29, 0x840000\n"
        "buffer_load_dwordx4 v4, s[20:23], s46 offen sc1 lds\n"
        "buffer_load_dwordx4 v4, s[20:23], s46 offen offset:1024 sc1 lds\n"
        "s_lshl1_add_u32 m0, s81, 0x1000\n"
        "s_add_u32 s46, s30, 0x840000\n"
        "buffer_load_dwordx4 v4, s[20:23], s46 offen sc1 lds\n"
        "buffer_load_dwordx4 v4, s[20:23], s46 offen offset:1024 sc1 lds\n"
        "s_lshl1_add_u32 m0, s81, 0x1800\n"
        "s_add_u32 s46, s31, 0x840000\n"
        "buffer_load_dwordx4 v4, s[20:23], s46 offen sc1 lds\n"
        "buffer_load_dwordx4 v4, s[20:23], s46 offen offset:1024 sc1 lds\n"
        "s_add_u32 m0, s79, 0x8000\n"
        "s_add_u32 s46, s33, 0x82c000\n"
        "buffer_load_dwordx4 v43, s[20:23], s46 offen sc1 lds\n"
        "s_add_u32 m0, s79, 0x8400\n"
        "s_add_u32 s46, s33, 0x82c000\n"
        "buffer_load_dwordx4 v44, s[20:23], s46 offen sc1 lds\n"
        "s_waitcnt vmcnt(0) lgkmcnt(0)\n"
        "L_chk_x20_%=:\n"
        "s_lshl1_add_u32 s43, s81, 0\n"
        "v_add_u32 v87, s43, v4\n"
        "s_add_u32 s43, s79, 0x8000\n"
        "v_add_u32 v88, s43, v4\n"
        "ds_read_b128 v[46:49], v87\n"
        "ds_read_b128 v[50:53], v87 offset:1024\n"
        "ds_read_b128 v[54:57], v87 offset:2048\n"
        "ds_read_b128 v[58:61], v87 offset:3072\n"
        "ds_read_b128 v[62:65], v87 offset:4096\n"
        "ds_read_b128 v[66:69], v87 offset:5120\n"
        "ds_read_b128 v[70:73], v87 offset:6144\n"
        "ds_read_b128 v[74:77], v87 offset:7168\n"
        "ds_read_b128 v[78:81], v88\n"
        "ds_read_b128 v[82:85], v88 offset:1024\n"
        "s_waitcnt lgkmcnt(9)\n"
        "v_max3_u32 v86, v46, v46, v47\n"
        "v_max3_u32 v86, v86, v48, v49\n"
        "s_waitcnt lgkmcnt(8)\n"
        "v_max3_u32 v86, v86, v50, v51\n"
        "v_max3_u32 v86, v86, v52, v53\n"
        "s_waitcnt lgkmcnt(7)\n"
        "v_max3_u32 v86, v86, v54, v55\n"
        "v_max3_u32 v86, v86, v56, v57\n"
        "s_waitcnt lgkmcnt(5)\n"
        "v_max3_u32 v86, v86, v58, v59\n"
        "v_max3_u32 v86, v86, v60, v61\n"
        "v_max3_u32 v86, v86, v62, v63\n"
        "v_max3_u32 v86, v86, v64, v65\n"
        "s_waitcnt lgkmcnt(0)\n"
        "v_max3_u32 v86, v86, v66, v67\n"
        "v_max3_u32 v86, v86, v68, v69\n"
        "v_max3_u32 v86, v86, v70, v71\n"
        "v_max3_u32 v86, v86, v72, v73\n"
        "v_max3_u32 v86, v86, v74, v75\n"
        "v_max3_u32 v86, v86, v76, v77\n"
        "v_max3_u32 v86, v86, v78, v79\n"
        "v_max3_u32 v86, v86, v80, v81\n"
        "v_max3_u32 v86, v86, v82, v83\n"
        "v_max3_u32 v86, v86, v84, v85\n"
        "v_cmp_eq_u32 vcc, -1, v86\n"
        "s_waitcnt vmcnt(0) lgkmcnt(0)\n"
        "s_cbranch_vccnz L_loop_x20_%=\n"
        "L_out_x20_%=:\n"
        "s_mov_b32 s46, s36\n"
        "s_mov_b32 m0, s37\n"
        "s_nop 7\n"
        "s_branch L_ret_x20_%=\n"
        "L_slow_x21_%=:\n"
        "s_nop 7\n"
        "s_mov_b32 s36, s46\n"
        "s_mov_b32 s37, m0\n"
        "L_loop_x21_%=:\n"
        "s_cmp_eq_u32 s59, 0\n"
        "s_cbranch_scc1 L_out_x21_%=\n"
        "s_sub_u32 s59, s59, 1\n"
        "s_lshl1_add_u32 m0, s81, 0xa500\n"
        "s_add_u32 s46, s28, 0x980000\n"
        "buffer_load_dwordx4 v4, s[20:23], s46 offen sc1 lds\n"
        "buffer_load_dwordx4 v4, s[20:23], s46 offen offset:1024 sc1 lds\n"
        "s_lshl1_add_u32 m0, s81, 0xad00\n"
        "s_add_u32 s46, s29, 0x980000\n"
        "buffer_load_dwordx4 v4, s[20:23], s46 offen sc1 lds\n"
        "buffer_load_dwordx4 v4, s[20:23], s46 offen offset:1024 sc1 lds\n"
        "s_lshl1_add_u32 m0, s81, 0xb500\n"
        "s_add_u32 s46, s30, 0x980000\n"
        "buffer_load_dwordx4 v4, s[20:23], s46 offen sc1 lds\n"
        "buffer_load_dwordx4 v4, s[20:23], s46 offen offset:1024 sc1 lds\n"
        "s_lshl1_add_u32 m0, s81, 0xbd00\n"
        "s_add_u32 s46, s31, 0x980000\n"
        "buffer_load_dwordx4 v4, s[20:23], s46 offen sc1 lds\n"
        "buffer_load_dwordx4 v4, s[20:23], s46 offen offset:1024 sc1 lds\n"
        "s_add_u32 m0, s79, 0x12500\n"
        "s_add_u32 s46, s32, 0x96c000\n"
        "buffer_load_dwordx4 v43, s[20:23], s46 offen sc1 lds\n"
        "s_add_u32 m0, s79, 0x12900\n"
        "s_add_u32 s46, s32, 0x96c000\n"
        "buffer_load_dwordx4 v44, s[20:23], s46 offen sc1 lds\n"
        "s_waitcnt vmcnt(0) lgkmcnt(0)\n"
        "L_chk_x21_%=:\n"
        "s_lshl1_add_u32 s44, s81, 0xa500\n"
        "v_add_u32 v188, s44, v4\n"
        "s_add_u32 s44, s79, 0x12500\n"
        "v_add_u32 v189, s44, v4\n"
        "ds_read_b128 v[184:187], v188\n"
        "ds_read_b128 v[180:183], v188 offset:1024\n"
        "s_waitcnt lgkmcnt(1)\n"
        "v_max3_u32 v179, v184, v184, v185\n"
        "v_max3_u32 v179, v179, v186, v187\n"
        "ds_read_b128 v[184:187], v188 offset:2048\n"
        "s_waitcnt lgkmcnt(1)\n"
        "v_max3_u32 v179, v179, v180, v181\n"
        "v_max3_u32 v179, v179, v182, v183\n"
        "ds_read_b128 v[180:183], v188 offset:3072\n"
        "s_waitcnt lgkmcnt(1)\n"
        "v_max3_u32 v179, v179, v184, v185\n"
        "v_max3_u32 v179, v179, v186, v187\n"
        "ds_read_b128 v[184:187], v188 offset:4096\n"
        "s_waitcnt lgkmcnt(1)\n"
        "v_max3_u32 v179, v179, v180, v181\n"
        "v_max3_u32 v179, v179, v182, v183\n"
        "ds_read_b128 v[180:183], v188 offset:5120\n"
        "s_waitcnt lgkmcnt(1)\n"
        "v_max3_u32 v179, v179, v184, v185\n"
        "v_max3_u32 v179, v179, v186, v187\n"
        "ds_read_b128 v[184:187], v188 offset:6144\n"
        "s_waitcnt lgkmcnt(1)\n"
        "v_max3_u32 v179, v179, v180, v181\n"
        "v_max3_u32 v179, v179, v182, v183\n"
        "ds_read_b128 v[180:183], v188 offset:7168\n"
        "s_waitcnt lgkmcnt(1)\n"
        "v_max3_u32 v179, v179, v184, v185\n"
        "v_max3_u32 v179, v179, v186, v187\n"
        "ds_read_b128 v[184:187], v189\n"
        "s_waitcnt lgkmcnt(1)\n"
        "v_max3_u32 v179, v179, v180, v181\n"
        "v_max3_u32 v179, v179, v182, v183\n"
        "ds_read_b128 v[180:183], v189 offset:1024\n"
        "s_waitcnt lgkmcnt(1)\n"
        "v_max3_u32 v179, v179, v184, v185\n"
        "v_max3_u32 v179, v179, v186, v187\n"
        "s_waitcnt lgkmcnt(0)\n"
        "v_max3_u32 v179, v179, v180, v181\n"
        "v_max3_u32 v179, v179, v182, v183\n"
        "v_cmp_eq_u32_e64 vcc, -1, v179\n"
        "s_waitcnt vmcnt(0) lgkmcnt(0)\n"
        "s_cbranch_vccnz L_loop_x21_%=\n"
        "L_out_x21_%=:\n"
        "s_mov_b32 s46, s36\n"
        "s_mov_b32 m0, s37\n"
        "s_nop 7\n"
        "s_branch L_ret_x21_%=\n"
        "L_slow_o10_%=:\n"
        "s_bitcmp1_b32 s76, 2\n"
        "s_cbranch_scc1 L_ret_o10_%=\n"
        "s_nop 7\n"
        "ds_read_b128 v[88:91], v4 offset:24576\n"
        "ds_read_b128 v[92:95], v4 offset:25600\n"
        "ds_read_b128 v[96:99], v4 offset:26624\n"
        "ds_read_b128 v[100:103], v4 offset:27648\n"
        "s_waitcnt lgkmcnt(3)\n"
        "v_max3_u32 v72, v88, v89, v90\n"
        "s_waitcnt lgkmcnt(2)\n"
        "v_max3_u32 v72, v72, v91, v92\n"
        "v_max3_u32 v72, v72, v93, v94\n"
        "s_waitcnt lgkmcnt(1)\n"
        "v_max3_u32 v72, v72, v95, v96\n"
        "v_max3_u32 v72, v72, v97, v98\n"
        "s_waitcnt lgkmcnt(0)\n"
        "v_max3_u32 v72, v72, v99, v100\n"
        "v_max3_u32 v72, v72, v101, v102\n"
        "v_max_u32 v72, v72, v103\n"
        "v_cmp_eq_u32 vcc, -1, v72\n"
        "ds_read_b128 v[88:91], v4 offset:28672\n"
        "ds_read_b128 v[92:95], v4 offset:29696\n"
        "ds_read_b128 v[96:99], v4 offset:30720\n"
        "ds_read_b128 v[100:103], v4 offset:31744\n"
        "s_waitcnt lgkmcnt(3)\n"
        "v_max3_u32 v72, v72, v88, v89\n"
        "v_max3_u32 v72, v72, v90, v91\n"
        "s_waitcnt lgkmcnt(2)\n"
        "v_max3_u32 v72, v72, v92, v93\n"
        "v_max3_u32 v72, v72, v94, v95\n"
        "s_waitcnt lgkmcnt(1)\n"
        "v_max3_u32 v72, v72, v96, v97\n"
        "v_max3_u32 v72, v72, v98, v99\n"
        "s_waitcnt lgkmcnt(0)\n"
        "v_max3_u32 v72, v72, v100, v101\n"
        "v_max3_u32 v72, v72, v102, v103\n"
        "v_cmp_eq_u32_e64 vcc, -1, v72\n"
        "ds_read_b128 v[88:91], v4 offset:32768\n"
        "ds_read_b128 v[92:95], v4 offset:33792\n"
        "ds_read_b128 v[96:99], v4 offset:34816\n"
        "ds_read_b128 v[100:103], v4 offset:35840\n"
        "s_waitcnt lgkmcnt(3)\n"
        "v_max3_u32 v72, v72, v88, v89\n"
        "v_max3_u32 v72, v72, v90, v91\n"
        "s_waitcnt lgkmcnt(2)\n"
        "v_max3_u32 v72, v72, v92, v93\n"
        "v_max3_u32 v72, v72, v94, v95\n"
        "s_waitcnt lgkmcnt(1)\n"
        "v_max3_u32 v72, v72, v96, v97\n"
        "v_max3_u32 v72, v72, v98, v99\n"
        "s_waitcnt lgkmcnt(0)\n"
        "v_max3_u32 v72, v72, v100, v101\n"
        "v_max3_u32 v72, v72, v102, v103\n"
        "v_cmp_eq_u32_e64 vcc, -1, v72\n"
        "ds_read_b128 v[88:91], v4 offset:36864\n"
        "ds_read_b128 v[92:95], v4 offset:37888\n"
        "ds_read_b128 v[96:99], v4 offset:38912\n"
        "ds_read_b128 v[100:103], v4 offset:39936\n"
        "s_waitcnt lgkmcnt(3)\n"
        "v_max3_u32 v72, v72, v88, v89\n"
        "v_max3_u32 v72, v72, v90, v91\n"
        "s_waitcnt lgkmcnt(2)\n"
        "v_max3_u32 v72, v72, v92, v93\n"
        "v_max3_u32 v72, v72, v94, v95\n"
        "s_waitcnt lgkmcnt(1)\n"
        "v_max3_u32 v72, v72, v96, v97\n"
        "v_max3_u32 v72, v72, v98, v99\n"
        "s_waitcnt lgkmcnt(0)\n"
        "v_max3_u32 v72, v72, v100, v101\n"
        "v_max3_u32 v72, v72, v102, v103\n"
        "v_cmp_eq_u32_e64 vcc, -1, v72\n"
        "s_waitcnt vmcnt(0) lgkmcnt(0)\n"
        "s_cbranch_vccz L_ret_o10_%=\n"
        "s_bitset1_b32 s76, 2\n"
        "s_barrier\n"
        "s_branch L_chk_o10_%=\n"
        "L_loop_o10_%=:\n"
        "s_cmp_eq_u32 s59, 0\n"
        "s_cbranch_scc1 L_out_o10_%=\n"
        "s_sub_u32 s59, s59, 1\n"
        "s_add_u32 m0, s81, 0x6000\n"
        "s_add_u32 s46, s83, 0x440000\n"
        "buffer_load_dwordx4 v4, s[20:23], s46 offen sc1 lds\n"
        "s_add_u32 m0, s81, 0x6400\n"
        "s_add_u32 s46, s84, 0x440000\n"
        "buffer_load_dwordx4 v4, s[20:23], s46 offen sc1 lds\n"
        "s_add_u32 m0, s81, 0x6800\n"
        "s_add_u32 s46, s85, 0x440000\n"
        "buffer_load_dwordx4 v4, s[20:23], s46 offen sc1 lds\n"
        "s_add_u32 m0, s81, 0x6c00\n"
        "s_add_u32 s46, s86, 0x440000\n"
        "buffer_load_dwordx4 v4, s[20:23], s46 offen sc1 lds\n"
        "s_waitcnt vmcnt(0) lgkmcnt(0)\n"
        "L_chk_o10_%=:\n"
        "s_lshl_b32 s43, s51, 12\n"
        "s_add_u32 s43, s43, 0x6000\n"
        "v_add_u32_e64 v73, s43, v4\n"
        "ds_read_b128 v[88:91], v73\n"
        "ds_read_b128 v[92:95], v73 offset:1024\n"
        "ds_read_b128 v[96:99], v73 offset:2048\n"
        "ds_read_b128 v[100:103], v73 offset:3072\n"
        "s_waitcnt lgkmcnt(3)\n"
        "v_max3_u32 v72, v88, v89, v90\n"
        "s_waitcnt lgkmcnt(2)\n"
        "v_max3_u32 v72, v72, v91, v92\n"
        "v_max3_u32 v72, v72, v93, v94\n"
        "s_waitcnt lgkmcnt(1)\n"
        "v_max3_u32 v72, v72, v95, v96\n"
        "v_max3_u32 v72, v72, v97, v98\n"
        "s_waitcnt lgkmcnt(0)\n"
        "v_max3_u32 v72, v72, v99, v100\n"
        "v_max3_u32 v72, v72, v101, v102\n"
        "v_max_u32 v72, v72, v103\n"
        "v_cmp_eq_u32 vcc, -1, v72\n"
        "s_waitcnt vmcnt(0) lgkmcnt(0)\n"
        "s_cbranch_vccnz L_loop_o10_%=\n"
        "L_out_o10_%=:\n"
        "s_barrier\n"
        "s_mov_b32 s43, 0xffff0000\n"
        "v_lshlrev_b32 v104, 16, v52\n"
        "v_and_b32 v105, s43, v52\n"
        "v_lshlrev_b32 v106, 16, v53\n"
        "v_and_b32 v107, s43, v53\n"
        "v_lshlrev_b32 v108, 16, v54\n"
        "v_and_b32 v109, s43, v54\n"
        "v_lshlrev_b32 v110, 16, v55\n"
        "v_and_b32_e64 v111, s43, v55\n"
        "ds_read_b128 v[88:91], v4 offset:24576\n"
        "ds_read_b128 v[92:95], v4 offset:25600\n"
        "ds_read_b128 v[96:99], v4 offset:26624\n"
        "ds_read_b128 v[100:103], v4 offset:27648\n"
        "s_waitcnt lgkmcnt(3)\n"
        "v_mfma_f32_16x16x32_bf16 v[104:107], a[192:195], v[88:91], v[104:107]\n"
        "v_mfma_f32_16x16x32_bf16 v[108:111], a[196:199], v[88:91], v[108:111]\n"
        "s_waitcnt lgkmcnt(2)\n"
        "v_mfma_f32_16x16x32_bf16 v[104:107], a[200:203], v[92:95], v[104:107]\n"
        "v_mfma_f32_16x16x32_bf16 v[108:111], a[204:207], v[92:95], v[108:111]\n"
        "s_waitcnt lgkmcnt(1)\n"
        "v_mfma_f32_16x16x32_bf16 v[104:107], a[208:211], v[96:99], v[104:107]\n"
        "v_mfma_f32_16x16x32_bf16 v[108:111], a[212:215], v[96:99], v[108:111]\n"
        "s_waitcnt lgkmcnt(0)\n"
        "v_mfma_f32_16x16x32_bf16 v[104:107], a[216:219], v[100:103], v[104:107]\n"
        "v_mfma_f32_16x16x32_bf16 v[108:111], a[220:223], v[100:103], v[108:111]\n"
        "ds_read_b128 v[88:91], v4 offset:28672\n"
        "ds_read_b128 v[92:95], v4 offset:29696\n"
        "ds_read_b128 v[96:99], v4 offset:30720\n"
        "ds_read_b128 v[100:103], v4 offset:31744\n"
        "s_waitcnt lgkmcnt(3)\n"
        "v_mfma_f32_16x16x32_bf16 v[104:107], a[224:227], v[88:91], v[104:107]\n"
        "v_mfma_f32_16x16x32_bf16 v[108:111], a[228:231], v[88:91], v[108:111]\n"
        "s_waitcnt lgkmcnt(2)\n"
        "v_mfma_f32_16x16x32_bf16 v[104:107], a[232:235], v[92:95], v[104:107]\n"
        "v_mfma_f32_16x16x32_bf16 v[108:111], a[236:239], v[92:95], v[108:111]\n"
        "s_waitcnt lgkmcnt(1)\n"
        "v_mfma_f32_16x16x32_bf16 v[104:107], a[240:243], v[96:99], v[104:107]\n"
        "v_mfma_f32_16x16x32_bf16 v[108:111], a[244:247], v[96:99], v[108:111]\n"
        "s_waitcnt lgkmcnt(0)\n"
        "v_mfma_f32_16x16x32_bf16 v[104:107], a[248:251], v[100:103], v[104:107]\n"
        "v_mfma_f32_16x16x32_bf16 v[108:111], a[252:255], v[100:103], v[108:111]\n"
        "ds_read_b128 v[88:91], v4 offset:32768\n"
        "ds_read_b128 v[92:95], v4 offset:33792\n"
        "ds_read_b128 v[96:99], v4 offset:34816\n"
        "ds_read_b128 v[100:103], v4 offset:35840\n"
        "s_waitcnt lgkmcnt(3)\n"
        "v_mfma_f32_16x16x32_bf16 v[104:107], v[192:195], v[88:91], v[104:107]\n"
        "v_mfma_f32_16x16x32_bf16 v[108:111], v[196:199], v[88:91], v[108:111]\n"
        "s_waitcnt lgkmcnt(2)\n"
        "v_mfma_f32_16x16x32_bf16 v[104:107], v[200:203], v[92:95], v[104:107]\n"
        "v_mfma_f32_16x16x32_bf16 v[108:111], v[204:207], v[92:95], v[108:111]\n"
        "s_waitcnt lgkmcnt(1)\n"
        "v_mfma_f32_16x16x32_bf16 v[104:107], v[208:211], v[96:99], v[104:107]\n"
        "v_mfma_f32_16x16x32_bf16 v[108:111], v[212:215], v[96:99], v[108:111]\n"
        "s_waitcnt lgkmcnt(0)\n"
        "v_mfma_f32_16x16x32_bf16 v[104:107], v[216:219], v[100:103], v[104:107]\n"
        "v_mfma_f32_16x16x32_bf16 v[108:111], v[220:223], v[100:103], v[108:111]\n"
        "ds_read_b128 v[88:91], v4 offset:36864\n"
        "ds_read_b128 v[92:95], v4 offset:37888\n"
        "ds_read_b128 v[96:99], v4 offset:38912\n"
        "ds_read_b128 v[100:103], v4 offset:39936\n"
        "s_waitcnt lgkmcnt(3)\n"
        "v_mfma_f32_16x16x32_bf16 v[104:107], v[224:227], v[88:91], v[104:107]\n"
        "v_mfma_f32_16x16x32_bf16 v[108:111], v[228:231], v[88:91], v[108:111]\n"
        "s_waitcnt lgkmcnt(2)\n"
        "v_mfma_f32_16x16x32_bf16 v[104:107], v[232:235], v[92:95], v[104:107]\n"
        "v_mfma_f32_16x16x32_bf16 v[108:111], v[236:239], v[92:95], v[108:111]\n"
        "s_waitcnt lgkmcnt(1)\n"
        "v_mfma_f32_16x16x32_bf16 v[104:107], v[240:243], v[96:99], v[104:107]\n"
        "v_mfma_f32_16x16x32_bf16 v[108:111], v[244:247], v[96:99], v[108:111]\n"
        "s_waitcnt lgkmcnt(0)\n"
        "v_mfma_f32_16x16x32_bf16 v[104:107], v[248:251], v[100:103], v[104:107]\n"
        "v_mfma_f32_16x16x32_bf16 v[108:111], v[252:255], v[100:103], v[108:111]\n"
        "s_nop 7\n"
        "s_branch L_re_o10_%=\n"
        "L_slow_flag1_%=:\n"
        "s_nop 7\n"
        "s_mov_b32 s36, s46\n"
        "s_mov_b32 s37, m0\n"
        "L_loop_flag1_%=:\n"
        "s_cmp_eq_u32 s59, 0\n"
        "s_cbranch_scc1 L_out_flag1_%=\n"
        "s_sub_u32 s59, s59, 1\n"
        "v_lshlrev_b32_e64 v186, 6, v0\n"
        "s_mov_b32 s91, 0x5000\n"
        "buffer_load_dwordx2 v[184:185], v186, s[20:23], s91 offen sc1\n"
        "s_waitcnt vmcnt(0) lgkmcnt(0)\n"
        "L_chk_flag1_%=:\n"
        "v_cmp_ne_u64 vcc, v[184:185], s[62:63]\n"
        "s_waitcnt vmcnt(0) lgkmcnt(0)\n"
        "s_cbranch_vccnz L_loop_flag1_%=\n"
        "L_out_flag1_%=:\n"
        "s_mov_b32 s46, s36\n"
        "s_mov_b32 m0, s37\n"
        "s_nop 7\n"
        "s_branch L_ret_flag1_%=\n"
        "L_slow_o11_%=:\n"
        "s_nop 7\n"
        "s_mov_b32 s36, s46\n"
        "s_mov_b32 s37, m0\n"
        "L_loop_o11_%=:\n"
        "s_cmp_eq_u32 s59, 0\n"
        "s_cbranch_scc1 L_out_o11_%=\n"
        "s_sub_u32 s59, s59, 1\n"
        "s_add_u32 m0, s81, 0\n"
        "s_add_u32 s46, s83, 0x540000\n"
        "buffer_load_dwordx4 v4, s[20:23], s46 offen sc1 lds\n"
        "s_add_u32 m0, s81, 0x400\n"
        "s_add_u32 s46, s84, 0x540000\n"
        "buffer_load_dwordx4 v4, s[20:23], s46 offen sc1 lds\n"
        "s_add_u32 m0, s81, 0x800\n"
        "s_add_u32 s46, s85, 0x540000\n"
        "buffer_load_dwordx4 v4, s[20:23], s46 offen sc1 lds\n"
        "s_add_u32 m0, s81, 0xc00\n"
        "s_add_u32 s46, s86, 0x540000\n"
        "buffer_load_dwordx4 v4, s[20:23], s46 offen sc1 lds\n"
        "s_waitcnt vmcnt(0) lgkmcnt(0)\n"
        "L_chk_o11_%=:\n"
        "s_lshl_b32 s43, s51, 12\n"
        "s_add_u32 s43, s43, 0\n"
        "v_add_u32 v101, s43, v4\n"
        "ds_read_b128 v[104:107], v101\n"
        "ds_read_b128 v[108:111], v101 offset:1024\n"
        "ds_read_b128 v[112:115], v101 offset:2048\n"
        "ds_read_b128 v[116:119], v101 offset:3072\n"
        "s_waitcnt lgkmcnt(3)\n"
        "v_max3_u32 v100, v104, v105, v106\n"
        "s_waitcnt lgkmcnt(2)\n"
        "v_max3_u32 v100, v100, v107, v108\n"
        "v_max3_u32 v100, v100, v109, v110\n"
        "s_waitcnt lgkmcnt(1)\n"
        "v_max3_u32 v100, v100, v111, v112\n"
        "v_max3_u32 v100, v100, v113, v114\n"
        "s_waitcnt lgkmcnt(0)\n"
        "v_max3_u32 v100, v100, v115, v116\n"
        "v_max3_u32 v100, v100, v117, v118\n"
        "v_max_u32 v100, v100, v119\n"
        "v_cmp_eq_u32_e64 vcc, -1, v100\n"
        "s_waitcnt vmcnt(0) lgkmcnt(0)\n"
        "s_cbranch_vccnz L_loop_o11_%=\n"
        "L_out_o11_%=:\n"
        "s_mov_b32 s46, s36\n"
        "s_mov_b32 m0, s37\n"
        "s_nop 7\n"
        "s_branch L_ret_o11_%=\n"
        "L_slow_p310_%=:\n"
        "L_pg11_%=:\n"
        "s_cbranch_execz L_pgret_%=\n"
        "s_bitcmp1_b32 s76, 3\n"
        "s_cbranch_scc1 L_ret_p310_%=\n"
        "s_bitset1_b32 s76, 3\n"
        "s_add_u32 s59, s59, 1\n"
        "s_mov_b32 s36, s46\n"
        "L_loop_p310_%=:\n"
        "s_cmp_eq_u32 s59, 0\n"
        "s_cbranch_scc1 L_out_p310_%=\n"
        "s_sub_u32 s59, s59, 1\n"
        "s_mov_b32 s46, 0x20000\n"
        "buffer_load_dwordx4 v[60:63], v19, s[20:23], s46 offen sc1\n"
        "s_mov_b32 s46, 0x21000\n"
        "buffer_load_dwordx4 v[64:67], v19, s[20:23], s46 offen sc1\n"
        "s_mov_b32 s46, 0x22000\n"
        "buffer_load_dwordx4 v[68:71], v19, s[20:23], s46 offen sc1\n"
        "s_mov_b32 s46, 0x23000\n"
        "buffer_load_dwordx4 v[72:75], v19, s[20:23], s46 offen sc1\n"
        "s_mov_b32 s46, 0x24000\n"
        "buffer_load_dwordx4 v[76:79], v19, s[20:23], s46 offen sc1\n"
        "s_mov_b32 s46, 0x25000\n"
        "buffer_load_dwordx4 v[80:83], v19, s[20:23], s46 offen sc1\n"
        "s_mov_b32 s46, 0x26000\n"
        "buffer_load_dwordx4 v[84:87], v19, s[20:23], s46 offen sc1\n"
        "s_mov_b32 s46, 0x27000\n"
        "buffer_load_dwordx4 v[88:91], v19, s[20:23], s46 offen sc1\n"
        "s_lshl_b32 s46, s50, 12\n"
        "s_add_u32 s46, s46, 0x640000\n"
        "buffer_load_dwordx4 v[46:49], v19, s[20:23], s46 offen sc1\n"
        "s_waitcnt vmcnt(0) lgkmcnt(0)\n"
        "L_chk_p310_%=:\n"
        "v_max3_u32 v50, v60, v61, v62\n"
        "v_max3_u32 v50, v50, v63, v64\n"
        "v_max3_u32 v50, v50, v65, v66\n"
        "v_max3_u32 v50, v50, v67, v68\n"
        "v_max3_u32 v50, v50, v69, v70\n"
        "v_max3_u32 v50, v50, v71, v72\n"
        "v_max3_u32 v50, v50, v73, v74\n"
        "v_max3_u32 v50, v50, v75, v76\n"
        "v_max3_u32 v50, v50, v77, v78\n"
        "v_max3_u32 v50, v50, v79, v80\n"
        "v_max3_u32 v50, v50, v81, v82\n"
        "v_max3_u32 v50, v50, v83, v84\n"
        "v_max3_u32 v50, v50, v85, v86\n"
        "v_max3_u32 v50, v50, v87, v88\n"
        "v_max3_u32 v50, v50, v89, v90\n"
        "v_max3_u32 v50, v50, v91, v46\n"
        "v_max3_u32 v50, v50, v47, v48\n"
        "v_max_u32 v50, v50, v49\n"
        "v_cmp_eq_u32_e64 vcc, -1, v50\n"
        "s_waitcnt vmcnt(0) lgkmcnt(0)\n"
        "s_cbranch_vccnz L_loop_p310_%=\n"
        "L_out_p310_%=:\n"
        "s_mov_b32 s46, s36\n"
        "s_branch L_re_p310_%=\n"
        "L_slow_p311_%=:\n"
        "L_pg12_%=:\n"
        "s_cbranch_execz L_pgret_%=\n"
        "s_bitcmp1_b32 s76, 4\n"
        "s_cbranch_scc1 L_ret_p311_%=\n"
        "s_bitset1_b32 s76, 4\n"
        "s_add_u32 s59, s59, 1\n"
        "s_mov_b32 s36, s46\n"
        "L_loop_p311_%=:\n"
        "s_cmp_eq_u32 s59, 0\n"
        "s_cbranch_scc1 L_out_p311_%=\n"
        "s_sub_u32 s59, s59, 1\n"
        "s_mov_b32 s46, 0x28000\n"
        "buffer_load_dwordx4 v[116:119], v19, s[20:23], s46 offen sc1\n"
        "s_mov_b32 s46, 0x29000\n"
        "buffer_load_dwordx4 v[120:123], v19, s[20:23], s46 offen sc1\n"
        "s_mov_b32 s46, 0x2a000\n"
        "buffer_load_dwordx4 v[124:127], v19, s[20:23], s46 offen sc1\n"
        "s_mov_b32 s46, 0x2b000\n"
        "buffer_load_dwordx4 v[128:131], v19, s[20:23], s46 offen sc1\n"
        "s_mov_b32 s46, 0x2c000\n"
        "buffer_load_dwordx4 v[132:135], v19, s[20:23], s46 offen sc1\n"
        "s_mov_b32 s46, 0x2d000\n"
        "buffer_load_dwordx4 v[136:139], v19, s[20:23], s46 offen sc1\n"
        "s_mov_b32 s46, 0x2e000\n"
        "buffer_load_dwordx4 v[140:143], v19, s[20:23], s46 offen sc1\n"
        "s_mov_b32 s46, 0x2f000\n"
        "buffer_load_dwordx4 v[144:147], v19, s[20:23], s46 offen sc1\n"
        "s_lshl_b32 s46, s50, 12\n"
        "s_add_u32 s46, s46, 0x740000\n"
        "buffer_load_dwordx4 v[112:115], v19, s[20:23], s46 offen sc1\n"
        "s_waitcnt vmcnt(0) lgkmcnt(0)\n"
        "L_chk_p311_%=:\n"
        "v_max3_u32 v45, v116, v117, v118\n"
        "v_max3_u32 v45, v45, v119, v120\n"
        "v_max3_u32 v45, v45, v121, v122\n"
        "v_max3_u32 v45, v45, v123, v124\n"
        "v_max3_u32 v45, v45, v125, v126\n"
        "v_max3_u32 v45, v45, v127, v128\n"
        "v_max3_u32 v45, v45, v129, v130\n"
        "v_max3_u32 v45, v45, v131, v132\n"
        "v_max3_u32 v45, v45, v133, v134\n"
        "v_max3_u32 v45, v45, v135, v136\n"
        "v_max3_u32 v45, v45, v137, v138\n"
        "v_max3_u32 v45, v45, v139, v140\n"
        "v_max3_u32 v45, v45, v141, v142\n"
        "v_max3_u32 v45, v45, v143, v144\n"
        "v_max3_u32 v45, v45, v145, v146\n"
        "v_max3_u32 v45, v45, v147, v112\n"
        "v_max3_u32 v45, v45, v113, v114\n"
        "v_max_u32 v45, v45, v115\n"
        "v_cmp_eq_u32_e64 vcc, -1, v45\n"
        "s_waitcnt vmcnt(0) lgkmcnt(0)\n"
        "s_cbranch_vccnz L_loop_p311_%=\n"
        "L_out_p311_%=:\n"
        "s_mov_b32 s46, s36\n"
        "s_branch L_re_p311_%=\n"
        "L_slow_y0_%=:\n"
        "s_nop 7\n"
        "s_mov_b32 s36, s46\n"
        "s_mov_b32 s37, m0\n"
        "L_loop_y0_%=:\n"
        "s_cmp_eq_u32 s59, 0\n"
        "s_cbranch_scc1 L_out_y0_%=\n"
        "s_sub_u32 s59, s59, 1\n"
        "s_lshr_b32 s46, s79, 1\n"
        "s_add_u32 m0, s46, 0xa500\n"
        "s_add_u32 s46, s9, 0xad4000\n"
        "buffer_load_dwordx4 v4, s[20:23], s46 offen sc1 lds\n"
        "s_waitcnt vmcnt(0) lgkmcnt(0)\n"
        "L_chk_y0_%=:\n"
        "ds_read_b128 v[116:119], v32\n"
        "ds_read_b64_tr_b16 v[112:113], v33\n"
        "ds_read_b64_tr_b16 v[114:115], v33 offset:256\n"
        "s_waitcnt lgkmcnt(2)\n"
        "v_max3_u32 v116, v116, v117, v118\n"
        "v_max_u32 v116, v116, v119\n"
        "v_cmp_eq_u32 vcc, -1, v116\n"
        "s_waitcnt vmcnt(0) lgkmcnt(0)\n"
        "s_cbranch_vccnz L_loop_y0_%=\n"
        "L_out_y0_%=:\n"
        "s_mov_b32 s46, s36\n"
        "s_mov_b32 m0, s37\n"
        "s_nop 7\n"
        "s_branch L_ret_y0_%=\n"
        "L_end_%=:\n"
        "s_endpgm\n"
        :
        : "s"(ka), "s"(__builtin_amdgcn_workgroup_id_x()), "v"(__builtin_amdgcn_workitem_id_x()),
          "s"((unsigned)(unsigned long long)(__attribute__((address_space(3))) unsigned char*)lds)
        : "v1", "v2", "v3", "v4", "v5", "v6", "v7", "v8", "v9", "v10", "v11", "v12", "v13", "v14", "v15", "v16", "v17", "v18", "v19", "v20", "v21", "v22", "v23", "v24", "v25", "v26", "v27", "v28", "v29", "v30", "v31", "v32", "v33", "v34", "v35", "v36", "v37", "v38", "v39", "v40", "v41", "v42", "v43", "v44", "v45", "v46", "v47", "v48", "v49", "v50", "v51", "v52", "v53", "v54", "v55", "v56", "v57", "v58", "v59", "v60", "v61", "v62", "v63", "v64", "v65", "v66", "v67", "v68", "v69", "v70", "v71", "v72", "v73", "v74", "v75", "v76", "v77", "v78", "v79", "v80", "v81", "v82", "v83", "v84", "v85", "v86", "v87", "v88", "v89", "v90", "v91", "v92", "v93", "v94", "v95", "v96", "v97", "v98", "v99", "v100", "v101", "v102", "v103", "v104", "v105", "v106", "v107", "v108", "v109", "v110", "v111", "v112", "v113", "v114", "v115", "v116", "v117", "v118", "v119", "v120", "v121", "v122", "v123", "v124", "v125", "v126", "v127", "v128", "v129", "v130", "v131", "v132", "v133", "v134", "v135", "v136", "v137", "v138", "v139", "v140", "v141", "v142", "v143", "v144", "v145", "v146", "v147", "v148", "v149", "v150", "v151", "v152", "v153", "v154", "v155", "v156", "v157", "v158", "v159", "v160", "v161", "v162", "v163", "v164", "v165", "v166", "v167", "v168", "v169", "v170", "v171", "v172", "v173", "v174", "v175", "v176", "v177", "v178", "v179", "v180", "v181", "v182", "v183", "v184", "v185", "v186", "v187", "v188", "v189", "v190", "v191", "v192", "v193", "v194", "v195", "v196", "v197", "v198", "v199", "v200", "v201", "v202", "v203", "v204", "v205", "v206", "v207", "v208", "v209", "v210", "v211", "v212", "v213", "v214", "v215", "v216", "v217", "v218", "v219", "v220", "v221", "v222", "v223", "v224", "v225", "v226", "v227", "v228", "v229", "v230", "v231", "v232", "v233", "v234", "v235", "v236", "v237", "v238", "v239", "v240", "v241", "v242", "v243", "v244", "v245", "v246", "v247", "v248", "v249", "v250", "v251", "v252", "v253", "v254", "v255", "a0", "a1", "a2", "a3", "a4", "a5", "a6", "a7", "a8", "a9", "a10", "a11", "a12", "a13", "a14", "a15", "a16", "a17", "a18", "a19", "a20", "a21", "a22", "a23", "a24", "a25", "a26", "a27", "a28", "a29", "a30", "a31", "a32", "a33", "a34", "a35", "a36", "a37", "a38", "a39", "a40", "a41", "a42", "a43", "a44", "a45", "a46", "a47", "a48", "a49", "a50", "a51", "a52", "a53", "a54", "a55", "a56", "a57", "a58", "a59", "a60", "a61", "a62", "a63", "a64", "a65", "a66", "a67", "a68", "a69", "a70", "a71", "a72", "a73", "a74", "a75", "a76", "a77", "a78", "a79", "a80", "a81", "a82", "a83", "a84", "a85", "a86", "a87", "a88", "a89", "a90", "a91", "a92", "a93", "a94", "a95", "a96", "a97", "a98", "a99", "a100", "a101", "a102", "a103", "a104", "a105", "a106", "a107", "a108", "a109", "a110", "a111", "a112", "a113", "a114", "a115", "a116", "a117", "a118", "a119", "a120", "a121", "a122", "a123", "a124", "a125", "a126", "a127", "a128", "a129", "a130", "a131", "a132", "a133", "a134", "a135", "a136", "a137", "a138", "a139", "a140", "a141", "a142", "a143", "a144", "a145", "a146", "a147", "a148", "a149", "a150", "a151", "a152", "a153", "a154", "a155", "a156", "a157", "a158", "a159", "a160", "a161", "a162", "a163", "a164", "a165", "a166", "a167", "a168", "a169", "a170", "a171", "a172", "a173", "a174", "a175", "a176", "a177", "a178", "a179", "a180", "a181", "a182", "a183", "a184", "a185", "a186", "a187", "a188", "a189", "a190", "a191", "a192", "a193", "a194", "a195", "a196", "a197", "a198", "a199", "a200", "a201", "a202", "a203", "a204", "a205", "a206", "a207", "a208", "a209", "a210", "a211", "a212", "a213", "a214", "a215", "a216", "a217", "a218", "a219", "a220", "a221", "a222", "a223", "a224", "a225", "a226", "a227", "a228", "a229", "a230", "a231", "a232", "a233", "a234", "a235", "a236", "a237", "a238", "a239", "a240", "a241", "a242", "a243", "a244", "a245", "a246", "a247", "a248", "a249", "a250", "a251", "a252", "a253", "a254", "a255", "s4", "s5", "s6", "s7", "s8", "s9", "s10", "s11", "s12", "s13", "s14", "s15", "s16", "s17", "s18", "s19", "s20", "s21", "s22", "s23", "s24", "s25", "s26", "s27", "s28", "s29", "s30", "s31", "s33", "s34", "s35", "s36", "s37", "s38", "s39", "s40", "s41", "s42", "s43", "s44", "s45", "s46", "s47", "s48", "s49", "s50", "s51", "s52", "s53", "s54", "s55", "s56", "s57", "s58", "s59", "s60", "s61", "s62", "s63", "s64", "s65", "s66", "s67", "s68", "s69", "s70", "s71", "s72", "s73", "s74", "s75", "s76", "s77", "s78", "s79", "s80", "s81", "s82", "s83", "s84", "s85", "s86", "s87", "s88", "s89", "s90", "s91", "s92", "s93", "s94", "s95", "s96", "s97", "s98", "s99", "vcc", "scc", "memory");
#endif
}

extern "C" void kernel_launch(void* const* d_in, const int* in_sizes, int n_in, void* d_out, int out_size,
                              void* d_ws, size_t ws_size, hipStream_t stream) {
    const unsigned F = 0x20000u;
    fused_layers_kernel<<<256, 256, 0, stream>>>(d_in[0], 2048u * 512u * 2u, F, d_in[1], 512u * 1536u * 2u, F,
                                                 d_in[2], 512u * 512u * 2u, F, d_ws, 13975552u, F, d_out, 2048u * 512u * 2u, F,
                                                 d_in[4], d_in[5], d_in[6], d_in[7], d_in[8], d_in[9]);
}
```
